```python
import torch

def _softmax(x, axis=-1):
    return torch.softmax(x, dim=axis)
def _gelu(x):
    return torch.nn.functional.gelu(x, approximate="tanh")
def _silu(x):
    return torch.nn.functional.silu(x)
def _sigmoid(x):
    return torch.sigmoid(x)
def _layernorm(x, axis=-1, eps=1e-5):
    m = x.mean(dim=axis, keepdim=True)
    v = x.var(dim=axis, keepdim=True, unbiased=False)
    return (x - m) / torch.sqrt(v + eps)
def _scatter_add(tgt, idx, src, axis=0):
    return tgt.clone().index_add_(axis, idx, src)
def _segment_sum(src, seg, n):
    out = torch.zeros((n,) + src.shape[1:], dtype=src.dtype)
    return out.index_add_(0, seg, src)
def _pad(x, pw):
    flat = tuple(v for pair in reversed(pw) for v in pair)
    return torch.nn.functional.pad(x, flat)

kernel_name = 'sequential_preLN__ssm_conv_s4d__glu_gated_reglu__prompt'

def setup_inputs():

    _w = (lambda a: (lambda t: t.to(torch.bfloat16) if t.is_floating_point() else t)(torch.as_tensor(a)))
    def _A_setup():
        Bsz, L, D, N = 1, 2048, 576, 128
        return {
            "u": _w((lambda *s: torch.randn(*s, dtype=torch.bfloat16) * 0.05)(Bsz, L, D)),
            "log_A_real": _w((lambda *s: torch.randn(*s, dtype=torch.bfloat16) * 6.4)(D, N) * 0.5),
            "A_imag": _w((lambda *s: torch.randn(*s, dtype=torch.bfloat16) * 0.05)(D, N) * 3.14),
            "C_real": _w((lambda *s: torch.randn(*s, dtype=torch.bfloat16) * 0.05)(D, N) / N ** 0.5),
            "C_imag": _w((lambda *s: torch.randn(*s, dtype=torch.bfloat16) * 0.05)(D, N) / N ** 0.5),
            "log_dt": _w((lambda *s: torch.randn(*s, dtype=torch.bfloat16) * 0.05)(D) * 0.5 - 4.0),
            "D_skip": _w((lambda *s: torch.randn(*s, dtype=torch.bfloat16) * 0.05)(D)),
        }
    _A_in = _A_setup()

    def _B_setup():
        B, S, D, F = 1, 2048, 576, 4608
        return {
            "x": _w((lambda *s: torch.randn(*s, dtype=torch.bfloat16) * 0.05)(B, S, D)),
            "W_gate": _w((lambda *s: torch.randn(*s, dtype=torch.bfloat16) * 0.4)(D, F) * (2.0 / D) ** 0.5),
            "W_up": _w((lambda *s: torch.randn(*s, dtype=torch.bfloat16) * 0.4)(D, F) * (2.0 / D) ** 0.5),
            "W_down": _w((lambda *s: torch.randn(*s, dtype=torch.bfloat16) * 0.4)(F, D) * (1.0 / F) ** 0.5),
        }
    _B_in = _B_setup()

    return {
        "A_u": _A_in["u"],
        "A_log_A_real": _A_in["log_A_real"],
        "A_A_imag": _A_in["A_imag"],
        "A_C_real": _A_in["C_real"],
        "A_C_imag": _A_in["C_imag"],
        "A_log_dt": _A_in["log_dt"],
        "A_D_skip": _A_in["D_skip"],
        "B_x": _B_in["x"],
        "B_W_gate": _B_in["W_gate"],
        "B_W_up": _B_in["W_up"],
        "B_W_down": _B_in["W_down"],
    }

def reference(A_u, A_log_A_real, A_A_imag, A_C_real, A_C_imag, A_log_dt, A_D_skip, B_x, B_W_gate, B_W_up, B_W_down):
    def _A_ref(u, log_A_real, A_imag, C_real, C_imag, log_dt, D_skip):
        L = u.shape[1]
        dt = torch.exp(log_dt)
        A = -torch.exp(log_A_real) + 1j * A_imag
        C = (C_real + 1j * C_imag) * dt[:, None]
        pos = (lambda x, dt: x.to(dt))(torch.arange(L), log_dt.dtype)
        P = torch.exp((A * dt[:, None])[:, :, None] * pos[None, None, :])
        K = 2.0 * torch.einsum("dn,dnl->dl", C, P).real
        L2 = 2 * L
        u_t = (lambda x, *d: x.permute(*d))(u, 0, 2, 1)
        Kf = (lambda x, *a, **k: torch.fft.rfft(x.float(), *a, **k))(K, L2)
        Uf = (lambda x, *a, **k: torch.fft.rfft(x.float(), *a, **k))(u_t, L2)
        y = (lambda x, *a, **k: torch.fft.irfft(x, *a, **k).to(torch.bfloat16))(Uf * Kf[None, :, :], L2)[:, :, :L]
        y = (lambda x, *d: x.permute(*d))(y, 0, 2, 1)
        return y + u * D_skip[None, None, :]

    def _B_ref(x, W_gate, W_up, W_down):
        gate = torch.einsum("bsd,df->bsf", x, W_gate)
        up = torch.einsum("bsd,df->bsf", x, W_up)
        gate = gate * (lambda x, dt: x.to(dt))(gate > 0, gate.dtype)
        h = gate * up
        return torch.einsum("bsf,fd->bsd", h, W_down)

    x = A_u
    h = x + _A_ref(_layernorm(x, axis=-1), A_log_A_real, A_A_imag, A_C_real, A_C_imag, A_log_dt, A_D_skip)
    h2 = h + _B_ref(_layernorm(h, axis=-1), B_W_gate, B_W_up, B_W_down)
    return h2
```

```cpp
#include <hip/hip_runtime.h>
#include <hip/hip_bf16.h>
#include <stdint.h>

typedef unsigned short u16;
constexpr int L = 2048, D = 576, NS = 128, F = 4608;

__device__ __forceinline__ float bf2f(u16 v) { return __uint_as_float(((unsigned)v) << 16); }
__device__ __forceinline__ u16 f2bf(float f) { __hip_bfloat16 b = __float2bfloat16(f); return *reinterpret_cast<u16*>(&b); }
__device__ __forceinline__ float rbf(float f) { return bf2f(f2bf(f)); }

__global__ void ln_kernel(const u16* x, u16* out) {
  int row = blockIdx.x, lane = threadIdx.x;
  float v[9]; float s = 0.f;
  for (int i = 0; i < 9; ++i) { v[i] = bf2f(x[row * D + lane + 64 * i]); s += v[i]; }
  for (int o = 32; o; o >>= 1) s += __shfl_xor(s, o);
  float m = s / D; float q = 0.f;
  for (int i = 0; i < 9; ++i) { float d = v[i] - m; q += d * d; }
  for (int o = 32; o; o >>= 1) q += __shfl_xor(q, o);
  float mb = rbf(m), vb = rbf(q / D);
  float sd = rbf(sqrtf(rbf(vb + 1e-5f)));
  for (int i = 0; i < 9; ++i) out[row * D + lane + 64 * i] = f2bf(rbf(v[i] - mb) / sd);
}

__global__ void kgen_kernel(const u16* lar, const u16* aim, const u16* cr, const u16* ci, const u16* ldt, float* K) {
  int d = blockIdx.x;
  __shared__ float zr[NS], zi[NS], c_r[NS], c_i[NS];
  float dt = rbf(expf(bf2f(ldt[d])));
  if (threadIdx.x < NS) {
    int n = threadIdx.x;
    zr[n] = -rbf(expf(bf2f(lar[d * NS + n]))) * dt;
    zi[n] = bf2f(aim[d * NS + n]) * dt;
    c_r[n] = bf2f(cr[d * NS + n]) * dt;
    c_i[n] = bf2f(ci[d * NS + n]) * dt;
  }
  __syncthreads();
  for (int l = threadIdx.x; l < L; l += blockDim.x) {
    float p = rbf((float)l); float s = 0.f;
    for (int n = 0; n < NS; ++n) {
      float e = expf(zr[n] * p); float sn, cs; sincosf(zi[n] * p, &sn, &cs);
      s += c_r[n] * (e * cs) - c_i[n] * (e * sn);
    }
    K[d * L + l] = 2.f * s;
  }
}

__global__ void conv_kernel(const u16* x, const u16* u, const float* K, const u16* dskip, u16* h) {
  int d = blockIdx.x;
  __shared__ float ks[L], us[L];
  for (int l = threadIdx.x; l < L; l += blockDim.x) { ks[l] = K[d * L + l]; us[l] = bf2f(u[l * D + d]); }
  __syncthreads();
  float ds = bf2f(dskip[d]);
  for (int l = threadIdx.x; l < L; l += blockDim.x) {
    float s = 0.f;
    for (int j = 0; j <= l; ++j) s += ks[j] * us[l - j];
    float y = rbf(rbf(s) + rbf(us[l] * ds));
    h[l * D + d] = f2bf(bf2f(x[l * D + d]) + y);
  }
}

__global__ void gemm1_kernel(const u16* xn, const u16* wg, const u16* wu, u16* H) {
  int f = blockIdx.x * 64 + threadIdx.x; int m0 = blockIdx.y * 16;
  __shared__ float xs[16][D];
  for (int i = threadIdx.x; i < 16 * D; i += 64) xs[i / D][i % D] = bf2f(xn[m0 * D + i]);
  __syncthreads();
  float g[16] = {}, up[16] = {};
  for (int k = 0; k < D; ++k) {
    float a = bf2f(wg[k * F + f]), b = bf2f(wu[k * F + f]);
#pragma unroll
    for (int i = 0; i < 16; ++i) { g[i] += xs[i][k] * a; up[i] += xs[i][k] * b; }
  }
#pragma unroll
  for (int i = 0; i < 16; ++i) { float gg = rbf(g[i]); gg = gg > 0.f ? gg : 0.f; H[(m0 + i) * F + f] = f2bf(gg * rbf(up[i])); }
}

__global__ void gemm2_kernel(const u16* H, const u16* wd, const u16* h, u16* out) {
  int n = blockIdx.x * 64 + threadIdx.x; int m0 = blockIdx.y * 8;
  __shared__ float hs[8][512];
  float acc[8] = {};
  for (int k0 = 0; k0 < F; k0 += 512) {
    __syncthreads();
    for (int i = threadIdx.x; i < 8 * 512; i += 64) hs[i / 512][i % 512] = bf2f(H[(m0 + i / 512) * F + k0 + i % 512]);
    __syncthreads();
    for (int k = 0; k < 512; ++k) {
      float w = bf2f(wd[(k0 + k) * D + n]);
#pragma unroll
      for (int i = 0; i < 8; ++i) acc[i] += hs[i][k] * w;
    }
  }
#pragma unroll
  for (int i = 0; i < 8; ++i) out[(m0 + i) * D + n] = f2bf(bf2f(h[(m0 + i) * D + n]) + rbf(acc[i]));
}

extern "C" void kernel_launch(void* const* d_in, const int* in_sizes, int n_in,
                              void* d_out, int out_size, void* d_ws, size_t ws_size,
                              hipStream_t stream) {
  const u16* x = (const u16*)d_in[0];
  char* ws = (char*)d_ws;
  u16* u = (u16*)ws; u16* h = (u16*)(ws + (4 << 20)); u16* xn = (u16*)(ws + (8 << 20));
  float* K = (float*)(ws + (12 << 20)); u16* H = (u16*)(ws + (20 << 20));
  ln_kernel<<<L, 64, 0, stream>>>(x, u);
  kgen_kernel<<<D, 256, 0, stream>>>((const u16*)d_in[1], (const u16*)d_in[2], (const u16*)d_in[3], (const u16*)d_in[4], (const u16*)d_in[5], K);
  conv_kernel<<<D, 256, 0, stream>>>(x, u, K, (const u16*)d_in[6], h);
  ln_kernel<<<L, 64, 0, stream>>>(h, xn);
  gemm1_kernel<<<dim3(F / 64, L / 16), 64, 0, stream>>>(xn, (const u16*)d_in[8], (const u16*)d_in[9], H);
  gemm2_kernel<<<dim3(D / 64, L / 8), 64, 0, stream>>>(H, (const u16*)d_in[10], h, (u16*)d_out);
}
```

```cpp
#include <hip/hip_runtime.h>
#include <stdint.h>
#define SSM_ASM \
  "ssm_code_begin:\n\t" \
  "s_load_dwordx16 s[16:31], %0, 0x0\n\t" \
  "s_load_dwordx4 s[36:39], %0, 0x40\n\t" \
  "s_load_dwordx2 s[40:41], %0, 0x50\n\t" \
  "v_mbcnt_lo_u32_b32 v0, -1, 0\n\t" \
  "v_mbcnt_hi_u32_b32 v0, -1, v0\n\t" \
  "s_mov_b32 s42, %1\n\t" \
  "s_mov_b32 s43, %2\n\t" \
  "s_and_b32 s44, s42, 7\n\t" \
  "s_lshr_b32 s45, s42, 3\n\t" \
  "s_cmp_lt_u32 s45, 18\n\t" \
  "s_cbranch_scc0 IDLE_%=\n\t" \
  "s_mul_i32 s46, s44, 18\n\t" \
  "s_add_u32 s46, s46, s45\n\t" \
  "s_and_b32 s47, s43, 3\n\t" \
  "s_lshr_b32 s48, s43, 2\n\t" \
  "v_lshlrev_b32 v1, 4, v0\n\t" \
  "v_lshlrev_b32 v2, 1, v0\n\t" \
  "s_lshl_b32 s56, s42, 3\n\t" \
  "s_add_u32 s56, s56, s43\n\t" \
  "s_mul_i32 s57, s56, 2304\n\t" \
  "s_lshl_b32 s58, s46, 2\n\t" \
  "s_add_u32 s58, s58, s47\n\t" \
  "s_lshl_b32 s59, s58, 1\n\t" \
  "s_add_u32 s59, s59, s48\n\t" \
  "s_lshl_b32 s59, s59, 7\n\t" \
  "v_add_u32 v13, s59, v2\n\t" \
  "s_cmp_lt_u32 s45, 16\n\t" \
  "s_waitcnt lgkmcnt(0)\n\t" \
  "s_cbranch_scc0 NOROW_%=\n\t" \
  "s_add_u32 s60, s16, s57\n\t" \
  "s_addc_u32 s61, s17, 0\n\t" \
  "global_load_dwordx4 v[4:7], v1, s[60:61]\n\t" \
  "global_load_dwordx4 v[8:11], v1, s[60:61] offset:1152\n\t" \
  "global_load_ushort v56, v2, s[60:61] offset:1024\n\t" \
  "global_load_ushort v57, v2, s[60:61] offset:2176\n\t" \
  "NOROW_%=:\n\t" \
  "s_cmp_lg_u32 s48, 0\n\t" \
  "s_cbranch_scc0 NOPRIO_%=\n\t" \
  "s_setprio 1\n\t" \
  "NOPRIO_%=:\n\t" \
  "s_load_dword s49, s[40:41], 0x0\n\t" \
  "global_load_ushort v40, v13, s[18:19]\n\t" \
  "global_load_ushort v41, v13, s[20:21]\n\t" \
  "global_load_ushort v42, v13, s[22:23]\n\t" \
  "global_load_ushort v43, v13, s[24:25]\n\t" \
  "v_xor_b32 v13, 128, v13\n\t" \
  "global_load_ushort v44, v13, s[22:23]\n\t" \
  "global_load_ushort v45, v13, s[24:25]\n\t" \
  "s_lshl_b32 s59, s58, 1\n\t" \
  "s_and_b32 s59, s59, -4\n\t" \
  "s_load_dword s50, s[26:27], s59\n\t" \
  "s_lshl_b32 s59, s46, 3\n\t" \
  "s_load_dwordx2 s[52:53], s[28:29], s59\n\t" \
  "s_mul_i32 s57, s47, 6656\n\t" \
  "s_add_u32 s57, s57, 109392\n\t" \
  "s_mov_b64 s[64:65], 1\n\t" \
  "s_cmp_lg_u32 s48, 0\n\t" \
  "s_cbranch_scc1 FARSLOT_%=\n\t" \
  "s_movk_i32 s59, 128\n\t" \
  "v_mov_b32 v20, 0x4280\n\t" \
  "v_add_u32 v15, -3, v1\n\t" \
  "v_max_i32 v15, 0, v15\n\t" \
  "v_cvt_f32_u32 v17, v15\n\t" \
  "v_add_u32 v16, -2, v1\n\t" \
  "v_max_i32 v16, 0, v16\n\t" \
  "v_cvt_f32_u32 v18, v16\n\t" \
  "v_cvt_pk_bf16_f32 v19, v17, v18\n\t" \
  "v_sub_u32_sdwa v17, v19, v20 dst_sel:DWORD dst_unused:UNUSED_PAD src0_sel:WORD_0 src1_sel:DWORD\n\t" \
  "v_sub_u32_sdwa v18, v19, v20 dst_sel:DWORD dst_unused:UNUSED_PAD src0_sel:WORD_1 src1_sel:DWORD\n\t" \
  "v_med3_i32 v15, v17, v15, s59\n\t" \
  "v_lshrrev_b32 v17, 5, v15\n\t" \
  "v_add_u32 v15, v15, v17\n\t" \
  "v_lshl_add_u32 v64, v15, 2, s57\n\t" \
  "v_med3_i32 v16, v18, v16, s59\n\t" \
  "v_lshrrev_b32 v18, 5, v16\n\t" \
  "v_add_u32 v16, v16, v18\n\t" \
  "v_lshl_add_u32 v65, v16, 2, s57\n\t" \
  "v_add_u32 v15, -1, v1\n\t" \
  "v_max_i32 v15, 0, v15\n\t" \
  "v_cvt_f32_u32 v17, v15\n\t" \
  "v_add_u32 v16, 0, v1\n\t" \
  "v_cvt_f32_u32 v18, v16\n\t" \
  "v_cvt_pk_bf16_f32 v19, v17, v18\n\t" \
  "v_sub_u32_sdwa v17, v19, v20 dst_sel:DWORD dst_unused:UNUSED_PAD src0_sel:WORD_0 src1_sel:DWORD\n\t" \
  "v_sub_u32_sdwa v18, v19, v20 dst_sel:DWORD dst_unused:UNUSED_PAD src0_sel:WORD_1 src1_sel:DWORD\n\t" \
  "v_med3_i32 v15, v17, v15, s59\n\t" \
  "v_lshrrev_b32 v17, 5, v15\n\t" \
  "v_add_u32 v15, v15, v17\n\t" \
  "v_lshl_add_u32 v66, v15, 2, s57\n\t" \
  "v_med3_i32 v16, v18, v16, s59\n\t" \
  "v_lshrrev_b32 v18, 5, v16\n\t" \
  "v_add_u32 v16, v16, v18\n\t" \
  "v_lshl_add_u32 v67, v16, 2, s57\n\t" \
  "v_add_u32 v15, 1, v1\n\t" \
  "v_cvt_f32_u32 v17, v15\n\t" \
  "v_add_u32 v16, 2, v1\n\t" \
  "v_cvt_f32_u32 v18, v16\n\t" \
  "v_cvt_pk_bf16_f32 v19, v17, v18\n\t" \
  "v_sub_u32_sdwa v17, v19, v20 dst_sel:DWORD dst_unused:UNUSED_PAD src0_sel:WORD_0 src1_sel:DWORD\n\t" \
  "v_sub_u32_sdwa v18, v19, v20 dst_sel:DWORD dst_unused:UNUSED_PAD src0_sel:WORD_1 src1_sel:DWORD\n\t" \
  "v_med3_i32 v15, v17, v15, s59\n\t" \
  "v_lshrrev_b32 v17, 5, v15\n\t" \
  "v_add_u32 v15, v15, v17\n\t" \
  "v_lshl_add_u32 v68, v15, 2, s57\n\t" \
  "v_med3_i32 v16, v18, v16, s59\n\t" \
  "v_lshrrev_b32 v18, 5, v16\n\t" \
  "v_add_u32 v16, v16, v18\n\t" \
  "v_lshl_add_u32 v69, v16, 2, s57\n\t" \
  "v_add_u32 v15, 3, v1\n\t" \
  "v_cvt_f32_u32 v17, v15\n\t" \
  "v_add_u32 v16, 4, v1\n\t" \
  "v_cvt_f32_u32 v18, v16\n\t" \
  "v_cvt_pk_bf16_f32 v19, v17, v18\n\t" \
  "v_sub_u32_sdwa v17, v19, v20 dst_sel:DWORD dst_unused:UNUSED_PAD src0_sel:WORD_0 src1_sel:DWORD\n\t" \
  "v_sub_u32_sdwa v18, v19, v20 dst_sel:DWORD dst_unused:UNUSED_PAD src0_sel:WORD_1 src1_sel:DWORD\n\t" \
  "v_med3_i32 v15, v17, v15, s59\n\t" \
  "v_lshrrev_b32 v17, 5, v15\n\t" \
  "v_add_u32 v15, v15, v17\n\t" \
  "v_lshl_add_u32 v70, v15, 2, s57\n\t" \
  "v_med3_i32 v16, v18, v16, s59\n\t" \
  "v_lshrrev_b32 v18, 5, v16\n\t" \
  "v_add_u32 v16, v16, v18\n\t" \
  "v_lshl_add_u32 v71, v16, 2, s57\n\t" \
  "v_add_u32 v15, 5, v1\n\t" \
  "v_cvt_f32_u32 v17, v15\n\t" \
  "v_add_u32 v16, 6, v1\n\t" \
  "v_cvt_f32_u32 v18, v16\n\t" \
  "v_cvt_pk_bf16_f32 v19, v17, v18\n\t" \
  "v_sub_u32_sdwa v17, v19, v20 dst_sel:DWORD dst_unused:UNUSED_PAD src0_sel:WORD_0 src1_sel:DWORD\n\t" \
  "v_sub_u32_sdwa v18, v19, v20 dst_sel:DWORD dst_unused:UNUSED_PAD src0_sel:WORD_1 src1_sel:DWORD\n\t" \
  "v_med3_i32 v15, v17, v15, s59\n\t" \
  "v_lshrrev_b32 v17, 5, v15\n\t" \
  "v_add_u32 v15, v15, v17\n\t" \
  "v_lshl_add_u32 v72, v15, 2, s57\n\t" \
  "v_med3_i32 v16, v18, v16, s59\n\t" \
  "v_lshrrev_b32 v18, 5, v16\n\t" \
  "v_add_u32 v16, v16, v18\n\t" \
  "v_lshl_add_u32 v73, v16, 2, s57\n\t" \
  "v_add_u32 v15, 7, v1\n\t" \
  "v_cvt_f32_u32 v17, v15\n\t" \
  "v_add_u32 v16, 8, v1\n\t" \
  "v_cvt_f32_u32 v18, v16\n\t" \
  "v_cvt_pk_bf16_f32 v19, v17, v18\n\t" \
  "v_sub_u32_sdwa v17, v19, v20 dst_sel:DWORD dst_unused:UNUSED_PAD src0_sel:WORD_0 src1_sel:DWORD\n\t" \
  "v_sub_u32_sdwa v18, v19, v20 dst_sel:DWORD dst_unused:UNUSED_PAD src0_sel:WORD_1 src1_sel:DWORD\n\t" \
  "v_med3_i32 v15, v17, v15, s59\n\t" \
  "v_lshrrev_b32 v17, 5, v15\n\t" \
  "v_add_u32 v15, v15, v17\n\t" \
  "v_lshl_add_u32 v74, v15, 2, s57\n\t" \
  "v_med3_i32 v16, v18, v16, s59\n\t" \
  "v_lshrrev_b32 v18, 5, v16\n\t" \
  "v_add_u32 v16, v16, v18\n\t" \
  "v_lshl_add_u32 v75, v16, 2, s57\n\t" \
  "v_add_u32 v15, 9, v1\n\t" \
  "v_cvt_f32_u32 v17, v15\n\t" \
  "v_add_u32 v16, 10, v1\n\t" \
  "v_cvt_f32_u32 v18, v16\n\t" \
  "v_cvt_pk_bf16_f32 v19, v17, v18\n\t" \
  "v_sub_u32_sdwa v17, v19, v20 dst_sel:DWORD dst_unused:UNUSED_PAD src0_sel:WORD_0 src1_sel:DWORD\n\t" \
  "v_sub_u32_sdwa v18, v19, v20 dst_sel:DWORD dst_unused:UNUSED_PAD src0_sel:WORD_1 src1_sel:DWORD\n\t" \
  "v_med3_i32 v15, v17, v15, s59\n\t" \
  "v_lshrrev_b32 v17, 5, v15\n\t" \
  "v_add_u32 v15, v15, v17\n\t" \
  "v_lshl_add_u32 v76, v15, 2, s57\n\t" \
  "v_med3_i32 v16, v18, v16, s59\n\t" \
  "v_lshrrev_b32 v18, 5, v16\n\t" \
  "v_add_u32 v16, v16, v18\n\t" \
  "v_lshl_add_u32 v77, v16, 2, s57\n\t" \
  "v_add_u32 v15, 11, v1\n\t" \
  "v_cvt_f32_u32 v17, v15\n\t" \
  "v_add_u32 v16, 12, v1\n\t" \
  "v_cvt_f32_u32 v18, v16\n\t" \
  "v_cvt_pk_bf16_f32 v19, v17, v18\n\t" \
  "v_sub_u32_sdwa v17, v19, v20 dst_sel:DWORD dst_unused:UNUSED_PAD src0_sel:WORD_0 src1_sel:DWORD\n\t" \
  "v_sub_u32_sdwa v18, v19, v20 dst_sel:DWORD dst_unused:UNUSED_PAD src0_sel:WORD_1 src1_sel:DWORD\n\t" \
  "v_med3_i32 v15, v17, v15, s59\n\t" \
  "v_lshrrev_b32 v17, 5, v15\n\t" \
  "v_add_u32 v15, v15, v17\n\t" \
  "v_lshl_add_u32 v78, v15, 2, s57\n\t" \
  "v_med3_i32 v16, v18, v16, s59\n\t" \
  "v_lshrrev_b32 v18, 5, v16\n\t" \
  "v_add_u32 v16, v16, v18\n\t" \
  "v_lshl_add_u32 v79, v16, 2, s57\n\t" \
  "v_add_u32 v15, 13, v1\n\t" \
  "v_cvt_f32_u32 v17, v15\n\t" \
  "v_add_u32 v16, 14, v1\n\t" \
  "v_cvt_f32_u32 v18, v16\n\t" \
  "v_cvt_pk_bf16_f32 v19, v17, v18\n\t" \
  "v_sub_u32_sdwa v17, v19, v20 dst_sel:DWORD dst_unused:UNUSED_PAD src0_sel:WORD_0 src1_sel:DWORD\n\t" \
  "v_sub_u32_sdwa v18, v19, v20 dst_sel:DWORD dst_unused:UNUSED_PAD src0_sel:WORD_1 src1_sel:DWORD\n\t" \
  "v_med3_i32 v15, v17, v15, s59\n\t" \
  "v_lshrrev_b32 v17, 5, v15\n\t" \
  "v_add_u32 v15, v15, v17\n\t" \
  "v_lshl_add_u32 v80, v15, 2, s57\n\t" \
  "v_med3_i32 v16, v18, v16, s59\n\t" \
  "v_lshrrev_b32 v18, 5, v16\n\t" \
  "v_add_u32 v16, v16, v18\n\t" \
  "v_lshl_add_u32 v81, v16, 2, s57\n\t" \
  "v_add_u32 v15, 15, v1\n\t" \
  "v_cvt_f32_u32 v17, v15\n\t" \
  "v_cvt_pk_bf16_f32 v19, v17, v17\n\t" \
  "v_sub_u32_sdwa v17, v19, v20 dst_sel:DWORD dst_unused:UNUSED_PAD src0_sel:WORD_0 src1_sel:DWORD\n\t" \
  "v_med3_i32 v15, v17, v15, s59\n\t" \
  "v_lshrrev_b32 v17, 5, v15\n\t" \
  "v_add_u32 v15, v15, v17\n\t" \
  "v_lshl_add_u32 v82, v15, 2, s57\n\t" \
  "s_branch SLOTS_%=\n\t" \
  "FARSLOT_%=:\n\t" \
  "v_add_u32 v15, 512, v2\n\t" \
  "v_add_u32 v16, 2, v15\n\t" \
  "v_lshrrev_b32 v23, 5, v15\n\t" \
  "v_add_u32 v15, v15, v23\n\t" \
  "v_lshl_add_u32 v65, v15, 2, s57\n\t" \
  "v_lshrrev_b32 v23, 5, v16\n\t" \
  "v_add_u32 v16, v16, v23\n\t" \
  "v_lshl_add_u32 v67, v16, 2, s57\n\t" \
  "v_add_u32 v66, 4, v65\n\t" \
  "s_add_u32 s59, s57, 2104\n\t" \
  "v_mov_b32 v15, s59\n\t" \
  "v_cndmask_b32 v64, v65, v15, s[64:65]\n\t" \
  "SLOTS_%=:\n\t" \
  "s_mul_i32 s59, s48, 3328\n\t" \
  "s_add_u32 s57, s57, s59\n\t" \
  "v_and_b32 v17, 31, v0\n\t" \
  "v_lshrrev_b32 v18, 5, v0\n\t" \
  "v_lshlrev_b32 v19, 3, v17\n\t" \
  "v_lshl_add_u32 v19, v18, 2, v19\n\t" \
  "v_sub_u32 v20, 2, v18\n\t" \
  "v_lshlrev_b32 v20, v20, v17\n\t" \
  "v_lshl_add_u32 v20, v18, 7, v20\n\t" \
  "v_add_u32 v20, 256, v20\n\t" \
  "v_mul_u32_u24 v21, 62, v18\n\t" \
  "v_add3_u32 v21, v20, v21, 2\n\t" \
  "v_lshl_add_u32 v22, v18, 7, v17\n\t" \
  "v_add_u32 v22, 512, v22\n\t" \
  "v_lshrrev_b32 v23, 5, v19\n\t" \
  "v_add_u32 v19, v19, v23\n\t" \
  "v_lshl_add_u32 v85, v19, 2, s57\n\t" \
  "v_lshrrev_b32 v23, 5, v20\n\t" \
  "v_add_u32 v20, v20, v23\n\t" \
  "v_lshl_add_u32 v86, v20, 2, s57\n\t" \
  "v_lshrrev_b32 v23, 5, v21\n\t" \
  "v_add_u32 v21, v21, v23\n\t" \
  "v_lshl_add_u32 v87, v21, 2, s57\n\t" \
  "v_lshrrev_b32 v23, 5, v22\n\t" \
  "v_add_u32 v22, v22, v23\n\t" \
  "v_lshl_add_u32 v88, v22, 2, s57\n\t" \
  "s_lshl_b32 s57, s43, 8\n\t" \
  "v_mul_u32_u24 v17, 2064, v17\n\t" \
  "v_lshl_add_u32 v17, v18, 4, v17\n\t" \
  "v_add_u32 v92, s57, v17\n\t" \
  "s_cmp_lt_u32 s45, 16\n\t" \
  "s_cbranch_scc1 TRANS_%=\n\t" \
  "s_waitcnt vmcnt(0) lgkmcnt(0)\n\t" \
  "s_add_u32 s49, s49, 1\n\t" \
  "s_lshl_b32 s57, s42, 2\n\t" \
  "s_add_u32 s57, s57, 512\n\t" \
  "v_mov_b32 v14, s57\n\t" \
  "v_mov_b32 v15, s49\n\t" \
  "s_cmp_lg_u32 s43, 0\n\t" \
  "s_cbranch_scc1 NOACK_%=\n\t" \
  "s_mov_b64 exec, 1\n\t" \
  "global_store_dword v14, v15, s[40:41] sc1\n\t" \
  "s_mov_b64 exec, -1\n\t" \
  "NOACK_%=:\n\t" \
  "s_sleep 8\n\t" \
  "s_branch KGEN_%=\n\t" \
  "TRANS_%=:\n\t" \
  "s_lshl_b32 s57, s43, 4\n\t" \
  "s_add_u32 s57, s57, 109392\n\t" \
  "v_mul_u32_u24 v14, 288, v0\n\t" \
  "v_add_u32 v14, s57, v14\n\t" \
  "v_lshrrev_b32 v15, 2, v0\n\t" \
  "v_and_b32 v16, 3, v0\n\t" \
  "v_mul_u32_u24 v18, 144, v15\n\t" \
  "v_lshl_add_u32 v18, v16, 1, v18\n\t" \
  "s_add_u32 s57, s57, 18432\n\t" \
  "v_add_u32 v18, s57, v18\n\t" \
  "v_lshrrev_b32 v15, 3, v0\n\t" \
  "v_and_b32 v16, 7, v0\n\t" \
  "v_mul_u32_u24 v17, 144, v15\n\t" \
  "v_lshl_add_u32 v17, v16, 4, v17\n\t" \
  "s_mul_i32 s57, s47, 1152\n\t" \
  "s_add_u32 s57, s57, 109392\n\t" \
  "v_add_u32 v17, s57, v17\n\t" \
  "v_lshlrev_b32 v54, 4, v16\n\t" \
  "v_lshlrev_b32 v15, 14, v15\n\t" \
  "v_add_u32 v15, v54, v15\n\t" \
  "s_lshl_b32 s57, s47, 17\n\t" \
  "s_lshl_b32 s59, s42, 7\n\t" \
  "s_add_u32 s57, s57, s59\n\t" \
  "s_add_u32 s57, s57, 41943040\n\t" \
  "v_add_u32 v15, s57, v15\n\t" \
  "s_lshl_b32 s57, s43, 4\n\t" \
  "s_add_u32 s57, s57, 136016\n\t" \
  "v_mov_b32 v55, s57\n\t" \
  "v_mov_b32 v52, 136144\n\t" \
  "v_mov_b32 v53, 0\n\t" \
  "ds_write_b32 v52, v53\n\t" \
  "v_mov_b32 v53, 1\n\t" \
  "v_mov_b32 v20, 0x3f803f80\n\t" \
  "s_waitcnt vmcnt(0)\n\t" \
  "ds_write2_b64 v14, v[4:5], v[8:9] offset0:0 offset1:1\n\t" \
  "ds_write2_b64 v14, v[6:7], v[10:11] offset0:18 offset1:19\n\t" \
  "ds_write_b16 v18, v56\n\t" \
  "ds_write_b16 v18, v57 offset:8\n\t" \
  "v_lshlrev_b32 v60, 16, v56\n\t" \
  "v_lshlrev_b32 v62, 16, v57\n\t" \
  "v_mul_f32 v61, v60, v60\n\t" \
  "v_mul_f32 v63, v62, v62\n\t" \
  "v_dot2_f32_bf16 v60, v4, v20, v60\n\t" \
  "v_dot2_f32_bf16 v61, v4, v4, v61\n\t" \
  "v_dot2_f32_bf16 v62, v8, v20, v62\n\t" \
  "v_dot2_f32_bf16 v63, v8, v8, v63\n\t" \
  "v_dot2_f32_bf16 v60, v5, v20, v60\n\t" \
  "v_dot2_f32_bf16 v61, v5, v5, v61\n\t" \
  "v_dot2_f32_bf16 v62, v9, v20, v62\n\t" \
  "v_dot2_f32_bf16 v63, v9, v9, v63\n\t" \
  "v_dot2_f32_bf16 v60, v6, v20, v60\n\t" \
  "v_dot2_f32_bf16 v61, v6, v6, v61\n\t" \
  "v_dot2_f32_bf16 v62, v10, v20, v62\n\t" \
  "v_dot2_f32_bf16 v63, v10, v10, v63\n\t" \
  "v_dot2_f32_bf16 v60, v7, v20, v60\n\t" \
  "v_dot2_f32_bf16 v61, v7, v7, v61\n\t" \
  "v_dot2_f32_bf16 v62, v11, v20, v62\n\t" \
  "v_dot2_f32_bf16 v63, v11, v11, v63\n\t" \
  "s_cmp_lg_u32 s48, 0\n\t" \
  "s_waitcnt lgkmcnt(0)\n\t" \
  "s_barrier\n\t" \
  "s_cbranch_scc0 NOST_%=\n\t" \
  "ds_read_b128 v[96:99], v17 offset:0\n\t" \
  "ds_read_b128 v[100:103], v17 offset:4608\n\t" \
  "ds_read_b128 v[104:107], v17 offset:9216\n\t" \
  "ds_read_b128 v[108:111], v17 offset:13824\n\t" \
  "ds_read_b128 v[112:115], v17 offset:18432\n\t" \
  "s_waitcnt lgkmcnt(4)\n\t" \
  "global_store_dwordx4 v15, v[96:99], s[40:41] sc1\n\t" \
  "v_add_u32 v15, 524288, v15\n\t" \
  "s_waitcnt lgkmcnt(3)\n\t" \
  "global_store_dwordx4 v15, v[100:103], s[40:41] sc1\n\t" \
  "v_add_u32 v15, 524288, v15\n\t" \
  "s_waitcnt lgkmcnt(2)\n\t" \
  "global_store_dwordx4 v15, v[104:107], s[40:41] sc1\n\t" \
  "v_add_u32 v15, 524288, v15\n\t" \
  "s_waitcnt lgkmcnt(1)\n\t" \
  "global_store_dwordx4 v15, v[108:111], s[40:41] sc1\n\t" \
  "v_add_u32 v15, 524288, v15\n\t" \
  "s_cmp_lt_u32 s47, 2\n\t" \
  "s_cbranch_scc0 NOST_%=\n\t" \
  "s_waitcnt lgkmcnt(0)\n\t" \
  "global_store_dwordx4 v15, v[112:115], s[40:41] sc1\n\t" \
  "NOST_%=:\n\t" \
  "v_add_f32_dpp v60, v60, v60 row_shr:1 row_mask:0xf bank_mask:0xf bound_ctrl:1\n\t" \
  "v_add_f32_dpp v61, v61, v61 row_shr:1 row_mask:0xf bank_mask:0xf bound_ctrl:1\n\t" \
  "v_add_f32_dpp v62, v62, v62 row_shr:1 row_mask:0xf bank_mask:0xf bound_ctrl:1\n\t" \
  "v_add_f32_dpp v63, v63, v63 row_shr:1 row_mask:0xf bank_mask:0xf bound_ctrl:1\n\t" \
  "v_add_f32_dpp v60, v60, v60 row_shr:2 row_mask:0xf bank_mask:0xf bound_ctrl:1\n\t" \
  "v_add_f32_dpp v61, v61, v61 row_shr:2 row_mask:0xf bank_mask:0xf bound_ctrl:1\n\t" \
  "v_add_f32_dpp v62, v62, v62 row_shr:2 row_mask:0xf bank_mask:0xf bound_ctrl:1\n\t" \
  "v_add_f32_dpp v63, v63, v63 row_shr:2 row_mask:0xf bank_mask:0xf bound_ctrl:1\n\t" \
  "v_add_f32_dpp v60, v60, v60 row_shr:4 row_mask:0xf bank_mask:0xf bound_ctrl:1\n\t" \
  "v_add_f32_dpp v61, v61, v61 row_shr:4 row_mask:0xf bank_mask:0xf bound_ctrl:1\n\t" \
  "v_add_f32_dpp v62, v62, v62 row_shr:4 row_mask:0xf bank_mask:0xf bound_ctrl:1\n\t" \
  "v_add_f32_dpp v63, v63, v63 row_shr:4 row_mask:0xf bank_mask:0xf bound_ctrl:1\n\t" \
  "v_add_f32_dpp v60, v60, v60 row_shr:8 row_mask:0xf bank_mask:0xf bound_ctrl:1\n\t" \
  "v_add_f32_dpp v61, v61, v61 row_shr:8 row_mask:0xf bank_mask:0xf bound_ctrl:1\n\t" \
  "v_add_f32_dpp v62, v62, v62 row_shr:8 row_mask:0xf bank_mask:0xf bound_ctrl:1\n\t" \
  "v_add_f32_dpp v63, v63, v63 row_shr:8 row_mask:0xf bank_mask:0xf bound_ctrl:1\n\t" \
  "v_add_f32_dpp v60, v60, v60 row_bcast:15 row_mask:0xa bank_mask:0xf\n\t" \
  "v_add_f32_dpp v61, v61, v61 row_bcast:15 row_mask:0xa bank_mask:0xf\n\t" \
  "v_add_f32_dpp v62, v62, v62 row_bcast:15 row_mask:0xa bank_mask:0xf\n\t" \
  "v_add_f32_dpp v63, v63, v63 row_bcast:15 row_mask:0xa bank_mask:0xf\n\t" \
  "v_add_f32_dpp v60, v60, v60 row_bcast:31 row_mask:0xc bank_mask:0xf\n\t" \
  "v_add_f32_dpp v61, v61, v61 row_bcast:31 row_mask:0xc bank_mask:0xf\n\t" \
  "v_add_f32_dpp v62, v62, v62 row_bcast:31 row_mask:0xc bank_mask:0xf\n\t" \
  "v_add_f32_dpp v63, v63, v63 row_bcast:31 row_mask:0xc bank_mask:0xf\n\t" \
  "v_mul_f32 v60, 0x3ae38e39, v60\n\t" \
  "v_mul_f32 v62, 0x3ae38e39, v62\n\t" \
  "v_mul_f32 v61, 0x3ae38e39, v61\n\t" \
  "v_mul_f32 v63, 0x3ae38e39, v63\n\t" \
  "v_fma_f32 v61, -v60, v60, v61\n\t" \
  "v_fma_f32 v63, -v62, v62, v63\n\t" \
  "v_max_f32 v61, 0, v61\n\t" \
  "v_max_f32 v63, 0, v63\n\t" \
  "v_cvt_pk_bf16_f32 v61, v61, v61\n\t" \
  "v_cvt_pk_bf16_f32 v63, v63, v63\n\t" \
  "v_lshlrev_b32 v61, 16, v61\n\t" \
  "v_lshlrev_b32 v63, 16, v63\n\t" \
  "v_add_f32 v61, 0x3727c5ac, v61\n\t" \
  "v_add_f32 v63, 0x3727c5ac, v63\n\t" \
  "v_cvt_pk_bf16_f32 v61, v61, v61\n\t" \
  "v_cvt_pk_bf16_f32 v63, v63, v63\n\t" \
  "v_lshlrev_b32 v61, 16, v61\n\t" \
  "v_lshlrev_b32 v63, 16, v63\n\t" \
  "v_sqrt_f32 v61, v61\n\t" \
  "v_sqrt_f32 v63, v63\n\t" \
  "s_add_u32 s49, s49, 1\n\t" \
  "v_cvt_pk_bf16_f32 v60, v60, v61\n\t" \
  "v_cvt_pk_bf16_f32 v62, v62, v63\n\t" \
  "v_mov_b32 v61, s49\n\t" \
  "v_mov_b32 v63, s49\n\t" \
  "s_mov_b32 exec_lo, 0\n\t" \
  "s_brev_b32 exec_hi, 1\n\t" \
  "ds_write_b128 v55, v[60:63]\n\t" \
  "s_waitcnt vmcnt(0) lgkmcnt(0)\n\t" \
  "ds_add_rtn_u32 v53, v52, v53\n\t" \
  "s_waitcnt lgkmcnt(0)\n\t" \
  "v_readfirstlane_b32 s57, v53\n\t" \
  "s_mov_b64 exec, -1\n\t" \
  "s_cmp_lg_u32 s57, 7\n\t" \
  "s_cbranch_scc1 KGEN_%=\n\t" \
  "v_add_u32 v55, 136016, v54\n\t" \
  "ds_read_b128 v[60:63], v55\n\t" \
  "s_lshl_b32 s57, s42, 7\n\t" \
  "s_add_u32 s57, s57, 4096\n\t" \
  "v_add_u32 v54, s57, v54\n\t" \
  "s_mov_b64 exec, 0xff\n\t" \
  "s_waitcnt lgkmcnt(0)\n\t" \
  "global_store_dwordx4 v54, v[60:63], s[40:41] sc1\n\t" \
  "s_mov_b64 exec, -1\n\t" \
  "KGEN_%=:\n\t" \
  "s_mov_b32 s57, 0x3fb8aa3b\n\t" \
  "s_lshl_b32 s59, s58, 4\n\t" \
  "s_lshr_b32 s50, s50, s59\n\t" \
  "s_lshl_b32 s50, s50, 16\n\t" \
  "v_mov_b32 v8, s50\n\t" \
  "v_mul_f32 v14, 0x3fb8aa3b, v8\n\t" \
  "v_fma_f32 v15, v8, s57, -v14\n\t" \
  "v_exp_f32 v14, v14\n\t" \
  "v_fmac_f32 v15, 0x32a57060, v8\n\t" \
  "v_mul_f32 v15, 0x3f317218, v15\n\t" \
  "v_fmac_f32 v14, v14, v15\n\t" \
  "v_cvt_pk_bf16_f32 v8, v14, v14\n\t" \
  "v_lshlrev_b32 v8, 16, v8\n\t" \
  "v_lshlrev_b32 v12, 16, v43\n\t" \
  "v_lshlrev_b32 v26, 16, v44\n\t" \
  "v_lshlrev_b32 v27, 16, v45\n\t" \
  "v_lshlrev_b32 v11, 16, v42\n\t" \
  "v_add_f32 v26, |v26|, |v27|\n\t" \
  "v_add_f32 v13, |v11|, |v12|\n\t" \
  "v_lshlrev_b32 v9, 16, v40\n\t" \
  "v_add_f32 v13, v13, v26\n\t" \
  "v_lshlrev_b32 v10, 16, v41\n\t" \
  "v_mul_f32 v14, 0x3fb8aa3b, v9\n\t" \
  "v_fma_f32 v15, v9, s57, -v14\n\t" \
  "v_exp_f32 v14, v14\n\t" \
  "v_fmac_f32 v15, 0x32a57060, v9\n\t" \
  "v_mul_f32 v15, 0x3f317218, v15\n\t" \
  "v_fmac_f32 v14, v14, v15\n\t" \
  "v_cvt_pk_bf16_f32 v9, v14, v14\n\t" \
  "v_lshlrev_b32 v9, 16, v9\n\t" \
  "v_add_f32_dpp v13, v13, v13 row_shr:1 row_mask:0xf bank_mask:0xf bound_ctrl:1\n\t" \
  "s_nop 1\n\t" \
  "v_add_f32_dpp v13, v13, v13 row_shr:2 row_mask:0xf bank_mask:0xf bound_ctrl:1\n\t" \
  "s_nop 1\n\t" \
  "v_add_f32_dpp v13, v13, v13 row_shr:4 row_mask:0xf bank_mask:0xf bound_ctrl:1\n\t" \
  "s_nop 1\n\t" \
  "v_add_f32_dpp v13, v13, v13 row_shr:8 row_mask:0xf bank_mask:0xf bound_ctrl:1\n\t" \
  "s_nop 1\n\t" \
  "v_add_f32_dpp v13, v13, v13 row_bcast:15 row_mask:0xa bank_mask:0xf\n\t" \
  "s_nop 1\n\t" \
  "v_add_f32_dpp v13, v13, v13 row_bcast:31 row_mask:0xc bank_mask:0xf\n\t" \
  "s_nop 1\n\t" \
  "v_readlane_b32 s51, v13, 63\n\t" \
  "v_mul_f32 v9, v9, v8\n\t" \
  "v_mul_f32 v10, v10, v8\n\t" \
  "v_add_f32 v8, v8, v8\n\t" \
  "v_mul_f32 v9, 0xbfb8aa3b, v9\n\t" \
  "v_mul_f32 v32, 0.15915494, v10\n\t" \
  "s_mov_b32 s57, 0x3e22f983\n\t" \
  "v_fma_f32 v33, v10, s57, -v32\n\t" \
  "v_fmac_f32 v33, 0x31dc9c88, v10\n\t" \
  "v_mul_f32 v13, s51, v8\n\t" \
  "v_mul_f32 v11, v11, v8\n\t" \
  "v_mul_f32 v12, v12, v8\n\t" \
  "v_frexp_exp_i32_f32 v13, v13\n\t" \
  "s_movk_i32 s57, 100\n\t" \
  "s_movk_i32 s59, -100\n\t" \
  "v_mov_b32 v15, s59\n\t" \
  "v_med3_i32 v13, v13, v15, s57\n\t" \
  "v_sub_u32 v14, 15, v13\n\t" \
  "v_add_u32 v13, -15, v13\n\t" \
  "v_ldexp_f32 v24, v11, v14\n\t" \
  "v_ldexp_f32 v25, v12, v14\n\t" \
  "v_ldexp_f32 v13, 1.0, v13\n\t" \
  "s_lshl_b32 s59, s47, 2\n\t" \
  "s_add_u32 s59, s59, 154448\n\t" \
  "v_mov_b32 v14, s59\n\t" \
  "ds_write_b32 v14, v13\n\t" \
  "v_exp_f32 v34, v9\n\t" \
  "v_fract_f32 v35, v32\n\t" \
  "v_add_f32 v35, v35, v33\n\t" \
  "v_cos_f32 v14, v35\n\t" \
  "v_sin_f32 v15, v35\n\t" \
  "s_nop 0\n\t" \
  "v_mul_f32 v14, v34, v14\n\t" \
  "v_mul_f32_e64 v15, v34, v15\n\t" \
  "v_mul_f32 v34, 0x43800000, v9\n\t" \
  "v_mul_f32 v35, 0x43800000, v32\n\t" \
  "v_exp_f32 v34, v34\n\t" \
  "v_fract_f32 v35, v35\n\t" \
  "v_fmac_f32 v35, 0x43800000, v33\n\t" \
  "v_cos_f32 v18, v35\n\t" \
  "v_sin_f32 v19, v35\n\t" \
  "s_nop 0\n\t" \
  "v_mul_f32 v18, v34, v18\n\t" \
  "v_mul_f32_e64 v19, v34, v19\n\t" \
  "v_mul_f32 v34, 0x41000000, v9\n\t" \
  "v_mul_f32 v35, 0x41000000, v32\n\t" \
  "v_exp_f32 v34, v34\n\t" \
  "v_fract_f32 v35, v35\n\t" \
  "v_fmac_f32 v35, 0x41000000, v33\n\t" \
  "v_cos_f32 v16, v35\n\t" \
  "v_sin_f32 v17, v35\n\t" \
  "s_nop 0\n\t" \
  "v_mul_f32 v16, v34, v16\n\t" \
  "v_mul_f32_e64 v17, -v34, v17\n\t" \
  "s_lshl_b32 s57, s43, 8\n\t" \
  "s_mov_b32 m0, s57\n\t" \
  "v_cvt_pk_f16_f32 v34, v24, v25\n\t" \
  "ds_write_addtid_b32 v34 offset:0\n\t" \
  "v_pk_mul_f32 v[32:33], v[14:15], v[14:15] op_sel:[1,1] op_sel_hi:[1,0] neg_lo:[1,0]\n\t" \
  "v_pk_fma_f32 v[20:21], v[14:15], v[14:15], v[32:33] op_sel:[0,0,0] op_sel_hi:[0,1,1]\n\t" \
  "v_pk_mul_f32 v[32:33], v[24:25], v[18:19] op_sel:[1,1] op_sel_hi:[1,0] neg_lo:[1,0]\n\t" \
  "v_pk_fma_f32 v[28:29], v[24:25], v[18:19], v[32:33] op_sel:[0,0,0] op_sel_hi:[0,1,1]\n\t" \
  "v_pk_mul_f32 v[32:33], v[20:21], v[20:21] op_sel:[1,1] op_sel_hi:[1,0] neg_lo:[1,0]\n\t" \
  "v_pk_fma_f32 v[22:23], v[20:21], v[20:21], v[32:33] op_sel:[0,0,0] op_sel_hi:[0,1,1]\n\t" \
  "v_pk_mul_f32 v[32:33], v[24:25], v[14:15] op_sel:[1,1] op_sel_hi:[1,0] neg_lo:[1,0]\n\t" \
  "v_pk_fma_f32 v[26:27], v[24:25], v[14:15], v[32:33] op_sel:[0,0,0] op_sel_hi:[0,1,1]\n\t" \
  "v_cvt_pk_f16_f32 v35, v26, v27\n\t" \
  "ds_write_addtid_b32 v35 offset:2064\n\t" \
  "v_pk_mul_f32 v[32:33], v[26:27], v[14:15] op_sel:[1,1] op_sel_hi:[1,0] neg_lo:[1,0]\n\t" \
  "v_pk_fma_f32 v[24:25], v[26:27], v[14:15], v[32:33] op_sel:[0,0,0] op_sel_hi:[0,1,1]\n\t" \
  "v_cvt_pk_f16_f32 v34, v24, v25\n\t" \
  "ds_write_addtid_b32 v34 offset:4128\n\t" \
  "v_pk_mul_f32 v[32:33], v[24:25], v[14:15] op_sel:[1,1] op_sel_hi:[1,0] neg_lo:[1,0]\n\t" \
  "v_pk_fma_f32 v[26:27], v[24:25], v[14:15], v[32:33] op_sel:[0,0,0] op_sel_hi:[0,1,1]\n\t" \
  "v_cvt_pk_f16_f32 v35, v26, v27\n\t" \
  "ds_write_addtid_b32 v35 offset:6192\n\t" \
  "v_pk_mul_f32 v[32:33], v[26:27], v[14:15] op_sel:[1,1] op_sel_hi:[1,0] neg_lo:[1,0]\n\t" \
  "v_pk_fma_f32 v[24:25], v[26:27], v[14:15], v[32:33] op_sel:[0,0,0] op_sel_hi:[0,1,1]\n\t" \
  "v_cvt_pk_f16_f32 v34, v24, v25\n\t" \
  "ds_write_addtid_b32 v34 offset:8256\n\t" \
  "v_pk_mul_f32 v[32:33], v[24:25], v[14:15] op_sel:[1,1] op_sel_hi:[1,0] neg_lo:[1,0]\n\t" \
  "v_pk_fma_f32 v[26:27], v[24:25], v[14:15], v[32:33] op_sel:[0,0,0] op_sel_hi:[0,1,1]\n\t" \
  "v_cvt_pk_f16_f32 v35, v26, v27\n\t" \
  "ds_write_addtid_b32 v35 offset:10320\n\t" \
  "v_pk_mul_f32 v[32:33], v[26:27], v[14:15] op_sel:[1,1] op_sel_hi:[1,0] neg_lo:[1,0]\n\t" \
  "v_pk_fma_f32 v[24:25], v[26:27], v[14:15], v[32:33] op_sel:[0,0,0] op_sel_hi:[0,1,1]\n\t" \
  "v_cvt_pk_f16_f32 v34, v24, v25\n\t" \
  "ds_write_addtid_b32 v34 offset:12384\n\t" \
  "v_pk_mul_f32 v[32:33], v[24:25], v[14:15] op_sel:[1,1] op_sel_hi:[1,0] neg_lo:[1,0]\n\t" \
  "v_pk_fma_f32 v[26:27], v[24:25], v[14:15], v[32:33] op_sel:[0,0,0] op_sel_hi:[0,1,1]\n\t" \
  "v_cvt_pk_f16_f32 v35, v26, v27\n\t" \
  "ds_write_addtid_b32 v35 offset:14448\n\t" \
  "v_cvt_pk_f16_f32 v34, v28, v29\n\t" \
  "ds_write_addtid_b32 v34 offset:16512\n\t" \
  "v_pk_mul_f32 v[32:33], v[28:29], v[20:21] op_sel:[1,1] op_sel_hi:[1,0] neg_lo:[1,0]\n\t" \
  "v_pk_fma_f32 v[24:25], v[28:29], v[20:21], v[32:33] op_sel:[0,0,0] op_sel_hi:[0,1,1]\n\t" \
  "v_cvt_pk_f16_f32 v35, v24, v25\n\t" \
  "ds_write_addtid_b32 v35 offset:18576\n\t" \
  "v_pk_mul_f32 v[32:33], v[28:29], v[22:23] op_sel:[1,1] op_sel_hi:[1,0] neg_lo:[1,0]\n\t" \
  "v_pk_fma_f32 v[24:25], v[28:29], v[22:23], v[32:33] op_sel:[0,0,0] op_sel_hi:[0,1,1]\n\t" \
  "v_cvt_pk_f16_f32 v34, v24, v25\n\t" \
  "ds_write_addtid_b32 v34 offset:20640\n\t" \
  "v_pk_mul_f32 v[32:33], v[24:25], v[20:21] op_sel:[1,1] op_sel_hi:[1,0] neg_lo:[1,0]\n\t" \
  "v_pk_fma_f32 v[26:27], v[24:25], v[20:21], v[32:33] op_sel:[0,0,0] op_sel_hi:[0,1,1]\n\t" \
  "v_cvt_pk_f16_f32 v35, v26, v27\n\t" \
  "ds_write_addtid_b32 v35 offset:22704\n\t" \
  "v_pk_mul_f32 v[32:33], v[28:29], v[18:19] op_sel:[1,1] op_sel_hi:[1,0] neg_lo:[1,0]\n\t" \
  "v_pk_fma_f32 v[30:31], v[28:29], v[18:19], v[32:33] op_sel:[0,0,0] op_sel_hi:[0,1,1]\n\t" \
  "v_cvt_pk_f16_f32 v34, v30, v31\n\t" \
  "ds_write_addtid_b32 v34 offset:24768\n\t" \
  "v_pk_mul_f32 v[32:33], v[30:31], v[22:23] op_sel:[1,1] op_sel_hi:[1,0] neg_lo:[1,0]\n\t" \
  "v_pk_fma_f32 v[24:25], v[30:31], v[22:23], v[32:33] op_sel:[0,0,0] op_sel_hi:[0,1,1]\n\t" \
  "v_cvt_pk_f16_f32 v35, v24, v25\n\t" \
  "ds_write_addtid_b32 v35 offset:26832\n\t" \
  "v_pk_mul_f32 v[32:33], v[30:31], v[18:19] op_sel:[1,1] op_sel_hi:[1,0] neg_lo:[1,0]\n\t" \
  "v_pk_fma_f32 v[28:29], v[30:31], v[18:19], v[32:33] op_sel:[0,0,0] op_sel_hi:[0,1,1]\n\t" \
  "v_cvt_pk_f16_f32 v34, v28, v29\n\t" \
  "ds_write_addtid_b32 v34 offset:28896\n\t" \
  "v_pk_mul_f32 v[32:33], v[28:29], v[22:23] op_sel:[1,1] op_sel_hi:[1,0] neg_lo:[1,0]\n\t" \
  "v_pk_fma_f32 v[24:25], v[28:29], v[22:23], v[32:33] op_sel:[0,0,0] op_sel_hi:[0,1,1]\n\t" \
  "v_cvt_pk_f16_f32 v35, v24, v25\n\t" \
  "ds_write_addtid_b32 v35 offset:30960\n\t" \
  "v_pk_mul_f32 v[32:33], v[28:29], v[18:19] op_sel:[1,1] op_sel_hi:[1,0] neg_lo:[1,0]\n\t" \
  "v_pk_fma_f32 v[30:31], v[28:29], v[18:19], v[32:33] op_sel:[0,0,0] op_sel_hi:[0,1,1]\n\t" \
  "v_cvt_pk_f16_f32 v34, v30, v31\n\t" \
  "ds_write_addtid_b32 v34 offset:33024\n\t" \
  "v_pk_mul_f32 v[32:33], v[30:31], v[18:19] op_sel:[1,1] op_sel_hi:[1,0] neg_lo:[1,0]\n\t" \
  "v_pk_fma_f32 v[28:29], v[30:31], v[18:19], v[32:33] op_sel:[0,0,0] op_sel_hi:[0,1,1]\n\t" \
  "v_cvt_pk_f16_f32 v35, v28, v29\n\t" \
  "ds_write_addtid_b32 v35 offset:35088\n\t" \
  "v_pk_mul_f32 v[32:33], v[28:29], v[18:19] op_sel:[1,1] op_sel_hi:[1,0] neg_lo:[1,0]\n\t" \
  "v_pk_fma_f32 v[30:31], v[28:29], v[18:19], v[32:33] op_sel:[0,0,0] op_sel_hi:[0,1,1]\n\t" \
  "v_cvt_pk_f16_f32 v34, v30, v31\n\t" \
  "ds_write_addtid_b32 v34 offset:37152\n\t" \
  "v_pk_mul_f32 v[32:33], v[30:31], v[18:19] op_sel:[1,1] op_sel_hi:[1,0] neg_lo:[1,0]\n\t" \
  "v_pk_fma_f32 v[28:29], v[30:31], v[18:19], v[32:33] op_sel:[0,0,0] op_sel_hi:[0,1,1]\n\t" \
  "v_cvt_pk_f16_f32 v35, v28, v29\n\t" \
  "ds_write_addtid_b32 v35 offset:39216\n\t" \
  "v_pk_mul_f32 v[32:33], v[28:29], v[18:19] op_sel:[1,1] op_sel_hi:[1,0] neg_lo:[1,0]\n\t" \
  "v_pk_fma_f32 v[30:31], v[28:29], v[18:19], v[32:33] op_sel:[0,0,0] op_sel_hi:[0,1,1]\n\t" \
  "v_cvt_pk_f16_f32 v34, v30, v31\n\t" \
  "ds_write_addtid_b32 v34 offset:41280\n\t" \
  "v_pk_mul_f32 v[32:33], v[16:17], v[16:17] op_sel:[1,1] op_sel_hi:[1,0] neg_lo:[1,0]\n\t" \
  "v_pk_fma_f32 v[20:21], v[16:17], v[16:17], v[32:33] op_sel:[0,0,0] op_sel_hi:[0,1,1]\n\t" \
  "v_mov_b32 v35, 0x3c00\n\t" \
  "ds_write_addtid_b32 v35 offset:43344\n\t" \
  "v_cvt_pk_f16_f32 v34, v16, v17\n\t" \
  "ds_write_addtid_b32 v34 offset:45408\n\t" \
  "v_cvt_pk_f16_f32 v35, v20, v21\n\t" \
  "ds_write_addtid_b32 v35 offset:47472\n\t" \
  "v_pk_mul_f32 v[32:33], v[16:17], v[20:21] op_sel:[1,1] op_sel_hi:[1,0] neg_lo:[1,0]\n\t" \
  "v_pk_fma_f32 v[30:31], v[16:17], v[20:21], v[32:33] op_sel:[0,0,0] op_sel_hi:[0,1,1]\n\t" \
  "v_cvt_pk_f16_f32 v34, v30, v31\n\t" \
  "ds_write_addtid_b32 v34 offset:49536\n\t" \
  "v_pk_mul_f32 v[32:33], v[30:31], v[20:21] op_sel:[1,1] op_sel_hi:[1,0] neg_lo:[1,0]\n\t" \
  "v_pk_fma_f32 v[28:29], v[30:31], v[20:21], v[32:33] op_sel:[0,0,0] op_sel_hi:[0,1,1]\n\t" \
  "v_pk_mul_f32 v[32:33], v[20:21], v[20:21] op_sel:[1,1] op_sel_hi:[1,0] neg_lo:[1,0]\n\t" \
  "v_pk_fma_f32 v[24:25], v[20:21], v[20:21], v[32:33] op_sel:[0,0,0] op_sel_hi:[0,1,1]\n\t" \
  "v_cvt_pk_f16_f32 v35, v24, v25\n\t" \
  "ds_write_addtid_b32 v35 offset:51600\n\t" \
  "v_cvt_pk_f16_f32 v34, v28, v29\n\t" \
  "ds_write_addtid_b32 v34 offset:53664\n\t" \
  "v_pk_mul_f32 v[32:33], v[28:29], v[20:21] op_sel:[1,1] op_sel_hi:[1,0] neg_lo:[1,0]\n\t" \
  "v_pk_fma_f32 v[30:31], v[28:29], v[20:21], v[32:33] op_sel:[0,0,0] op_sel_hi:[0,1,1]\n\t" \
  "s_add_u32 m0, m0, 55728\n\t" \
  "v_pk_mul_f32 v[32:33], v[24:25], v[20:21] op_sel:[1,1] op_sel_hi:[1,0] neg_lo:[1,0]\n\t" \
  "v_pk_fma_f32 v[26:27], v[24:25], v[20:21], v[32:33] op_sel:[0,0,0] op_sel_hi:[0,1,1]\n\t" \
  "v_cvt_pk_f16_f32 v35, v26, v27\n\t" \
  "ds_write_addtid_b32 v35 offset:0\n\t" \
  "v_cvt_pk_f16_f32 v34, v30, v31\n\t" \
  "ds_write_addtid_b32 v34 offset:2064\n\t" \
  "v_pk_mul_f32 v[32:33], v[30:31], v[20:21] op_sel:[1,1] op_sel_hi:[1,0] neg_lo:[1,0]\n\t" \
  "v_pk_fma_f32 v[28:29], v[30:31], v[20:21], v[32:33] op_sel:[0,0,0] op_sel_hi:[0,1,1]\n\t" \
  "v_pk_mul_f32 v[32:33], v[26:27], v[20:21] op_sel:[1,1] op_sel_hi:[1,0] neg_lo:[1,0]\n\t" \
  "v_pk_fma_f32 v[24:25], v[26:27], v[20:21], v[32:33] op_sel:[0,0,0] op_sel_hi:[0,1,1]\n\t" \
  "v_cvt_pk_f16_f32 v35, v24, v25\n\t" \
  "ds_write_addtid_b32 v35 offset:4128\n\t" \
  "v_cvt_pk_f16_f32 v34, v28, v29\n\t" \
  "ds_write_addtid_b32 v34 offset:6192\n\t" \
  "v_pk_mul_f32 v[32:33], v[28:29], v[20:21] op_sel:[1,1] op_sel_hi:[1,0] neg_lo:[1,0]\n\t" \
  "v_pk_fma_f32 v[30:31], v[28:29], v[20:21], v[32:33] op_sel:[0,0,0] op_sel_hi:[0,1,1]\n\t" \
  "v_pk_mul_f32 v[32:33], v[24:25], v[20:21] op_sel:[1,1] op_sel_hi:[1,0] neg_lo:[1,0]\n\t" \
  "v_pk_fma_f32 v[26:27], v[24:25], v[20:21], v[32:33] op_sel:[0,0,0] op_sel_hi:[0,1,1]\n\t" \
  "v_cvt_pk_f16_f32 v35, v26, v27\n\t" \
  "ds_write_addtid_b32 v35 offset:8256\n\t" \
  "v_cvt_pk_f16_f32 v34, v30, v31\n\t" \
  "ds_write_addtid_b32 v34 offset:10320\n\t" \
  "v_pk_mul_f32 v[32:33], v[30:31], v[20:21] op_sel:[1,1] op_sel_hi:[1,0] neg_lo:[1,0]\n\t" \
  "v_pk_fma_f32 v[28:29], v[30:31], v[20:21], v[32:33] op_sel:[0,0,0] op_sel_hi:[0,1,1]\n\t" \
  "v_pk_mul_f32 v[32:33], v[26:27], v[20:21] op_sel:[1,1] op_sel_hi:[1,0] neg_lo:[1,0]\n\t" \
  "v_pk_fma_f32 v[24:25], v[26:27], v[20:21], v[32:33] op_sel:[0,0,0] op_sel_hi:[0,1,1]\n\t" \
  "v_cvt_pk_f16_f32 v35, v24, v25\n\t" \
  "ds_write_addtid_b32 v35 offset:12384\n\t" \
  "v_cvt_pk_f16_f32 v34, v28, v29\n\t" \
  "ds_write_addtid_b32 v34 offset:14448\n\t" \
  "v_pk_mul_f32 v[32:33], v[28:29], v[20:21] op_sel:[1,1] op_sel_hi:[1,0] neg_lo:[1,0]\n\t" \
  "v_pk_fma_f32 v[30:31], v[28:29], v[20:21], v[32:33] op_sel:[0,0,0] op_sel_hi:[0,1,1]\n\t" \
  "v_pk_mul_f32 v[32:33], v[24:25], v[20:21] op_sel:[1,1] op_sel_hi:[1,0] neg_lo:[1,0]\n\t" \
  "v_pk_fma_f32 v[26:27], v[24:25], v[20:21], v[32:33] op_sel:[0,0,0] op_sel_hi:[0,1,1]\n\t" \
  "v_cvt_pk_f16_f32 v35, v26, v27\n\t" \
  "ds_write_addtid_b32 v35 offset:16512\n\t" \
  "v_cvt_pk_f16_f32 v34, v30, v31\n\t" \
  "ds_write_addtid_b32 v34 offset:18576\n\t" \
  "v_pk_mul_f32 v[32:33], v[30:31], v[20:21] op_sel:[1,1] op_sel_hi:[1,0] neg_lo:[1,0]\n\t" \
  "v_pk_fma_f32 v[28:29], v[30:31], v[20:21], v[32:33] op_sel:[0,0,0] op_sel_hi:[0,1,1]\n\t" \
  "v_pk_mul_f32 v[32:33], v[26:27], v[20:21] op_sel:[1,1] op_sel_hi:[1,0] neg_lo:[1,0]\n\t" \
  "v_pk_fma_f32 v[24:25], v[26:27], v[20:21], v[32:33] op_sel:[0,0,0] op_sel_hi:[0,1,1]\n\t" \
  "v_cvt_pk_f16_f32 v35, v24, v25\n\t" \
  "ds_write_addtid_b32 v35 offset:20640\n\t" \
  "v_cvt_pk_f16_f32 v34, v28, v29\n\t" \
  "ds_write_addtid_b32 v34 offset:22704\n\t" \
  "ds_read_b128 v[104:107], v92 offset:0\n\t" \
  "v_pk_mul_f32 v[32:33], v[28:29], v[20:21] op_sel:[1,1] op_sel_hi:[1,0] neg_lo:[1,0]\n\t" \
  "v_pk_fma_f32 v[30:31], v[28:29], v[20:21], v[32:33] op_sel:[0,0,0] op_sel_hi:[0,1,1]\n\t" \
  "v_pk_mul_f32 v[32:33], v[24:25], v[20:21] op_sel:[1,1] op_sel_hi:[1,0] neg_lo:[1,0]\n\t" \
  "v_pk_fma_f32 v[26:27], v[24:25], v[20:21], v[32:33] op_sel:[0,0,0] op_sel_hi:[0,1,1]\n\t" \
  "v_cvt_pk_f16_f32 v35, v26, v27\n\t" \
  "ds_write_addtid_b32 v35 offset:24768\n\t" \
  "v_cvt_pk_f16_f32 v34, v30, v31\n\t" \
  "ds_write_addtid_b32 v34 offset:26832\n\t" \
  "ds_read_b128 v[108:111], v92 offset:32\n\t" \
  "v_pk_mul_f32 v[32:33], v[30:31], v[20:21] op_sel:[1,1] op_sel_hi:[1,0] neg_lo:[1,0]\n\t" \
  "v_pk_fma_f32 v[28:29], v[30:31], v[20:21], v[32:33] op_sel:[0,0,0] op_sel_hi:[0,1,1]\n\t" \
  "v_pk_mul_f32 v[32:33], v[26:27], v[20:21] op_sel:[1,1] op_sel_hi:[1,0] neg_lo:[1,0]\n\t" \
  "v_pk_fma_f32 v[24:25], v[26:27], v[20:21], v[32:33] op_sel:[0,0,0] op_sel_hi:[0,1,1]\n\t" \
  "v_cvt_pk_f16_f32 v35, v24, v25\n\t" \
  "ds_write_addtid_b32 v35 offset:28896\n\t" \
  "v_cvt_pk_f16_f32 v34, v28, v29\n\t" \
  "ds_write_addtid_b32 v34 offset:30960\n\t" \
  "ds_read_b128 v[112:115], v92 offset:64\n\t" \
  "v_pk_mul_f32 v[32:33], v[28:29], v[20:21] op_sel:[1,1] op_sel_hi:[1,0] neg_lo:[1,0]\n\t" \
  "v_pk_fma_f32 v[30:31], v[28:29], v[20:21], v[32:33] op_sel:[0,0,0] op_sel_hi:[0,1,1]\n\t" \
  "v_pk_mul_f32 v[32:33], v[24:25], v[20:21] op_sel:[1,1] op_sel_hi:[1,0] neg_lo:[1,0]\n\t" \
  "v_pk_fma_f32 v[26:27], v[24:25], v[20:21], v[32:33] op_sel:[0,0,0] op_sel_hi:[0,1,1]\n\t" \
  "v_cvt_pk_f16_f32 v35, v26, v27\n\t" \
  "ds_write_addtid_b32 v35 offset:33024\n\t" \
  "v_cvt_pk_f16_f32 v34, v30, v31\n\t" \
  "ds_write_addtid_b32 v34 offset:35088\n\t" \
  "ds_read_b128 v[116:119], v92 offset:96\n\t" \
  "v_pk_mul_f32 v[32:33], v[30:31], v[20:21] op_sel:[1,1] op_sel_hi:[1,0] neg_lo:[1,0]\n\t" \
  "v_pk_fma_f32 v[28:29], v[30:31], v[20:21], v[32:33] op_sel:[0,0,0] op_sel_hi:[0,1,1]\n\t" \
  "v_pk_mul_f32 v[32:33], v[26:27], v[20:21] op_sel:[1,1] op_sel_hi:[1,0] neg_lo:[1,0]\n\t" \
  "v_pk_fma_f32 v[24:25], v[26:27], v[20:21], v[32:33] op_sel:[0,0,0] op_sel_hi:[0,1,1]\n\t" \
  "v_cvt_pk_f16_f32 v35, v24, v25\n\t" \
  "ds_write_addtid_b32 v35 offset:37152\n\t" \
  "v_cvt_pk_f16_f32 v34, v28, v29\n\t" \
  "ds_write_addtid_b32 v34 offset:39216\n\t" \
  "ds_read_b128 v[120:123], v92 offset:128\n\t" \
  "s_cmp_lg_u32 s48, 0\n\t" \
  "s_cbranch_scc0 NOGR_%=\n\t" \
  "s_lshl_b32 s57, s47, 6\n\t" \
  "v_add_u32 v3, s57, v0\n\t" \
  "v_lshlrev_b32 v6, 4, v3\n\t" \
  "v_add_u32 v7, 8192, v6\n\t" \
  "s_add_u32 s60, s40, 8192\n\t" \
  "s_addc_u32 s61, s41, 0\n\t" \
  "global_load_dwordx4 v[200:203], v6, s[60:61] offset:-4096 sc1\n\t" \
  "global_load_dwordx4 v[204:207], v6, s[60:61] offset:0 sc1\n\t" \
  "global_load_dwordx4 v[208:211], v7, s[60:61] offset:-4096 sc1\n\t" \
  "global_load_dwordx4 v[212:215], v7, s[60:61] offset:0 sc1\n\t" \
  "NOGR_%=:\n\t" \
  "v_pk_mul_f32 v[32:33], v[28:29], v[20:21] op_sel:[1,1] op_sel_hi:[1,0] neg_lo:[1,0]\n\t" \
  "v_pk_fma_f32 v[30:31], v[28:29], v[20:21], v[32:33] op_sel:[0,0,0] op_sel_hi:[0,1,1]\n\t" \
  "v_pk_mul_f32 v[32:33], v[24:25], v[20:21] op_sel:[1,1] op_sel_hi:[1,0] neg_lo:[1,0]\n\t" \
  "v_pk_fma_f32 v[26:27], v[24:25], v[20:21], v[32:33] op_sel:[0,0,0] op_sel_hi:[0,1,1]\n\t" \
  "v_cvt_pk_f16_f32 v35, v26, v27\n\t" \
  "ds_write_addtid_b32 v35 offset:41280\n\t" \
  "v_cvt_pk_f16_f32 v34, v30, v31\n\t" \
  "ds_write_addtid_b32 v34 offset:43344\n\t" \
  "ds_read_b128 v[124:127], v92 offset:160\n\t" \
  "v_pk_mul_f32 v[32:33], v[30:31], v[20:21] op_sel:[1,1] op_sel_hi:[1,0] neg_lo:[1,0]\n\t" \
  "v_pk_fma_f32 v[28:29], v[30:31], v[20:21], v[32:33] op_sel:[0,0,0] op_sel_hi:[0,1,1]\n\t" \
  "v_pk_mul_f32 v[32:33], v[26:27], v[20:21] op_sel:[1,1] op_sel_hi:[1,0] neg_lo:[1,0]\n\t" \
  "v_pk_fma_f32 v[24:25], v[26:27], v[20:21], v[32:33] op_sel:[0,0,0] op_sel_hi:[0,1,1]\n\t" \
  "v_cvt_pk_f16_f32 v35, v24, v25\n\t" \
  "ds_write_addtid_b32 v35 offset:45408\n\t" \
  "v_cvt_pk_f16_f32 v34, v28, v29\n\t" \
  "ds_write_addtid_b32 v34 offset:47472\n\t" \
  "ds_read_b128 v[128:131], v92 offset:192\n\t" \
  "v_pk_mul_f32 v[32:33], v[28:29], v[20:21] op_sel:[1,1] op_sel_hi:[1,0] neg_lo:[1,0]\n\t" \
  "v_pk_fma_f32 v[30:31], v[28:29], v[20:21], v[32:33] op_sel:[0,0,0] op_sel_hi:[0,1,1]\n\t" \
  "v_pk_mul_f32 v[32:33], v[24:25], v[20:21] op_sel:[1,1] op_sel_hi:[1,0] neg_lo:[1,0]\n\t" \
  "v_pk_fma_f32 v[26:27], v[24:25], v[20:21], v[32:33] op_sel:[0,0,0] op_sel_hi:[0,1,1]\n\t" \
  "v_cvt_pk_f16_f32 v35, v26, v27\n\t" \
  "ds_write_addtid_b32 v35 offset:49536\n\t" \
  "v_cvt_pk_f16_f32 v34, v30, v31\n\t" \
  "ds_write_addtid_b32 v34 offset:51600\n\t" \
  "ds_read_b128 v[132:135], v92 offset:224\n\t" \
  "ds_read_b128 v[136:139], v92 offset:43344\n\t" \
  "ds_read_b128 v[140:143], v92 offset:43376\n\t" \
  "ds_read_b128 v[144:147], v92 offset:43408\n\t" \
  "ds_read_b128 v[148:151], v92 offset:43440\n\t" \
  "ds_read_b128 v[152:155], v92 offset:43472\n\t" \
  "ds_read_b128 v[156:159], v92 offset:43504\n\t" \
  "ds_read_b128 v[160:163], v92 offset:43536\n\t" \
  "ds_read_b128 v[164:167], v92 offset:43568\n\t" \
  "s_waitcnt lgkmcnt(7)\n\t" \
  "v_mfma_f32_32x32x16_f16 v[168:183], v[104:107], v[136:139], 0\n\t" \
  "v_mov_b32 v24, 0\n\t" \
  "v_mov_b32 v25, 0\n\t" \
  "s_waitcnt lgkmcnt(6)\n\t" \
  "v_mfma_f32_32x32x16_f16 v[168:183], v[108:111], v[140:143], v[168:183]\n\t" \
  "v_mov_b32 v26, 0\n\t" \
  "v_mov_b32 v27, 0\n\t" \
  "s_waitcnt lgkmcnt(5)\n\t" \
  "v_mfma_f32_32x32x16_f16 v[168:183], v[112:115], v[144:147], v[168:183]\n\t" \
  "s_mul_i32 s59, s47, 16640\n\t" \
  "v_lshl_add_u32 v5, v0, 5, s59\n\t" \
  "s_waitcnt lgkmcnt(4)\n\t" \
  "v_mfma_f32_32x32x16_f16 v[168:183], v[116:119], v[148:151], v[168:183]\n\t" \
  "s_waitcnt lgkmcnt(3)\n\t" \
  "v_mfma_f32_32x32x16_f16 v[168:183], v[120:123], v[152:155], v[168:183]\n\t" \
  "s_waitcnt lgkmcnt(2)\n\t" \
  "v_mfma_f32_32x32x16_f16 v[168:183], v[124:127], v[156:159], v[168:183]\n\t" \
  "s_waitcnt lgkmcnt(1)\n\t" \
  "v_mfma_f32_32x32x16_f16 v[168:183], v[128:131], v[160:163], v[168:183]\n\t" \
  "s_waitcnt lgkmcnt(0)\n\t" \
  "v_mfma_f32_32x32x16_f16 v[168:183], v[132:135], v[164:167], v[168:183]\n\t" \
  "s_cmp_lg_u32 s48, 0\n\t" \
  "s_nop 10\n\t" \
  "ds_write2_b32 v85, v168, v169 offset0:0 offset1:1\n\t" \
  "ds_write2_b32 v85, v170, v171 offset0:2 offset1:3\n\t" \
  "ds_write2_b32 v86, v172, v173 offset0:0 offset1:1\n\t" \
  "ds_write2_b32 v87, v174, v175 offset0:0 offset1:1\n\t" \
  "ds_write2_b32 v88, v176, v177 offset0:0 offset1:33\n\t" \
  "ds_write2_b32 v88, v178, v179 offset0:66 offset1:99\n\t" \
  "s_waitcnt lgkmcnt(0)\n\t" \
  "s_barrier\n\t" \
  "s_cbranch_scc1 ROWS_%=\n\t" \
  "ds_read2st64_b32 v[104:105], v64 offset0:0 offset1:13\n\t" \
  "ds_read2st64_b32 v[106:107], v65 offset0:0 offset1:13\n\t" \
  "ds_read2st64_b32 v[108:109], v66 offset0:0 offset1:13\n\t" \
  "ds_read2st64_b32 v[110:111], v67 offset0:0 offset1:13\n\t" \
  "ds_read2st64_b32 v[112:113], v68 offset0:0 offset1:13\n\t" \
  "ds_read2st64_b32 v[114:115], v69 offset0:0 offset1:13\n\t" \
  "ds_read2st64_b32 v[116:117], v70 offset0:0 offset1:13\n\t" \
  "ds_read2st64_b32 v[118:119], v71 offset0:0 offset1:13\n\t" \
  "ds_read2st64_b32 v[120:121], v72 offset0:0 offset1:13\n\t" \
  "ds_read2st64_b32 v[122:123], v73 offset0:0 offset1:13\n\t" \
  "ds_read2st64_b32 v[124:125], v74 offset0:0 offset1:13\n\t" \
  "ds_read2st64_b32 v[126:127], v75 offset0:0 offset1:13\n\t" \
  "ds_read2st64_b32 v[128:129], v76 offset0:0 offset1:13\n\t" \
  "ds_read2st64_b32 v[130:131], v77 offset0:0 offset1:13\n\t" \
  "ds_read2st64_b32 v[132:133], v78 offset0:0 offset1:13\n\t" \
  "ds_read2st64_b32 v[134:135], v79 offset0:0 offset1:13\n\t" \
  "ds_read2st64_b32 v[136:137], v80 offset0:0 offset1:13\n\t" \
  "ds_read2st64_b32 v[138:139], v81 offset0:0 offset1:13\n\t" \
  "ds_read2st64_b32 v[140:141], v82 offset0:0 offset1:13\n\t" \
  "v_lshrrev_b32 v4, 1, v0\n\t" \
  "v_and_b32 v7, 1, v0\n\t" \
  "v_mul_u32_u24 v4, 4160, v4\n\t" \
  "v_lshl_add_u32 v4, v7, 4, v4\n\t" \
  "v_add_u32 v4, s59, v4\n\t" \
  "s_waitcnt lgkmcnt(15)\n\t" \
  "v_add_f32 v104, v104, v105\n\t" \
  "v_cndmask_b32 v104, v104, 0, s[64:65]\n\t" \
  "s_waitcnt lgkmcnt(15)\n\t" \
  "v_add_f32 v106, v106, v107\n\t" \
  "v_cndmask_b32 v106, v106, 0, s[64:65]\n\t" \
  "s_waitcnt lgkmcnt(15)\n\t" \
  "v_add_f32 v108, v108, v109\n\t" \
  "v_cndmask_b32 v108, v108, 0, s[64:65]\n\t" \
  "s_waitcnt lgkmcnt(15)\n\t" \
  "v_add_f32 v110, v110, v111\n\t" \
  "s_waitcnt lgkmcnt(14)\n\t" \
  "v_add_f32 v112, v112, v113\n\t" \
  "s_waitcnt lgkmcnt(13)\n\t" \
  "v_add_f32 v114, v114, v115\n\t" \
  "s_waitcnt lgkmcnt(12)\n\t" \
  "v_add_f32 v116, v116, v117\n\t" \
  "s_waitcnt lgkmcnt(11)\n\t" \
  "v_add_f32 v118, v118, v119\n\t" \
  "s_waitcnt lgkmcnt(10)\n\t" \
  "v_add_f32 v120, v120, v121\n\t" \
  "s_waitcnt lgkmcnt(9)\n\t" \
  "v_add_f32 v122, v122, v123\n\t" \
  "s_waitcnt lgkmcnt(8)\n\t" \
  "v_add_f32 v124, v124, v125\n\t" \
  "s_waitcnt lgkmcnt(7)\n\t" \
  "v_add_f32 v126, v126, v127\n\t" \
  "s_waitcnt lgkmcnt(6)\n\t" \
  "v_add_f32 v128, v128, v129\n\t" \
  "s_waitcnt lgkmcnt(5)\n\t" \
  "v_add_f32 v130, v130, v131\n\t" \
  "s_waitcnt lgkmcnt(4)\n\t" \
  "v_add_f32 v132, v132, v133\n\t" \
  "s_waitcnt lgkmcnt(3)\n\t" \
  "v_add_f32 v134, v134, v135\n\t" \
  "s_waitcnt lgkmcnt(2)\n\t" \
  "v_add_f32 v136, v136, v137\n\t" \
  "s_waitcnt lgkmcnt(1)\n\t" \
  "v_add_f32 v138, v138, v139\n\t" \
  "s_waitcnt lgkmcnt(0)\n\t" \
  "v_add_f32 v140, v140, v141\n\t" \
  "s_mov_b64 exec, 0xff\n\t" \
  "ds_write_b128 v4, v[24:27]\n\t" \
  "s_mov_b64 exec, -1\n\t" \
  "v_cvt_pk_f16_f32 v144, v110, v112\n\t" \
  "v_cvt_pk_f16_f32 v145, v114, v116\n\t" \
  "v_cvt_pk_f16_f32 v146, v118, v120\n\t" \
  "v_cvt_pk_f16_f32 v147, v122, v124\n\t" \
  "v_cvt_pk_f16_f32 v148, v126, v128\n\t" \
  "v_cvt_pk_f16_f32 v149, v130, v132\n\t" \
  "v_cvt_pk_f16_f32 v150, v134, v136\n\t" \
  "v_cvt_pk_f16_f32 v151, v138, v140\n\t" \
  "ds_write_b128 v5, v[144:147] offset:32\n\t" \
  "ds_write_b128 v5, v[148:151] offset:48\n\t" \
  "v_cvt_pk_f16_f32 v152, v108, v110\n\t" \
  "v_cvt_pk_f16_f32 v153, v112, v114\n\t" \
  "v_cvt_pk_f16_f32 v154, v116, v118\n\t" \
  "v_cvt_pk_f16_f32 v155, v120, v122\n\t" \
  "v_cvt_pk_f16_f32 v156, v124, v126\n\t" \
  "v_cvt_pk_f16_f32 v157, v128, v130\n\t" \
  "v_cvt_pk_f16_f32 v158, v132, v134\n\t" \
  "v_cvt_pk_f16_f32 v159, v136, v138\n\t" \
  "ds_write_b128 v5, v[152:155] offset:4192\n\t" \
  "ds_write_b128 v5, v[156:159] offset:4208\n\t" \
  "v_cvt_pk_f16_f32 v144, v106, v108\n\t" \
  "v_cvt_pk_f16_f32 v145, v110, v112\n\t" \
  "v_cvt_pk_f16_f32 v146, v114, v116\n\t" \
  "v_cvt_pk_f16_f32 v147, v118, v120\n\t" \
  "v_cvt_pk_f16_f32 v148, v122, v124\n\t" \
  "v_cvt_pk_f16_f32 v149, v126, v128\n\t" \
  "v_cvt_pk_f16_f32 v150, v130, v132\n\t" \
  "v_cvt_pk_f16_f32 v151, v134, v136\n\t" \
  "ds_write_b128 v5, v[144:147] offset:8352\n\t" \
  "ds_write_b128 v5, v[148:151] offset:8368\n\t" \
  "v_cvt_pk_f16_f32 v152, v104, v106\n\t" \
  "v_cvt_pk_f16_f32 v153, v108, v110\n\t" \
  "v_cvt_pk_f16_f32 v154, v112, v114\n\t" \
  "v_cvt_pk_f16_f32 v155, v116, v118\n\t" \
  "v_cvt_pk_f16_f32 v156, v120, v122\n\t" \
  "v_cvt_pk_f16_f32 v157, v124, v126\n\t" \
  "v_cvt_pk_f16_f32 v158, v128, v130\n\t" \
  "v_cvt_pk_f16_f32 v159, v132, v134\n\t" \
  "ds_write_b128 v5, v[152:155] offset:12512\n\t" \
  "ds_write_b128 v5, v[156:159] offset:12528\n\t" \
  "v_and_b32 v10, 15, v0\n\t" \
  "v_lshrrev_b32 v11, 4, v0\n\t" \
  "v_lshrrev_b32 v6, 2, v10\n\t" \
  "v_and_b32 v7, 3, v0\n\t" \
  "s_mul_i32 s57, s43, 4608\n\t" \
  "s_add_u32 s57, s57, 136432\n\t" \
  "v_lshl_add_u32 v8, v10, 5, s57\n\t" \
  "v_lshl_add_u32 v8, v11, 4, v8\n\t" \
  "s_lshl_b32 s57, s43, 12\n\t" \
  "s_add_u32 s57, s57, 66560\n\t" \
  "v_lshl_add_u32 v10, v10, 5, s57\n\t" \
  "v_lshl_add_u32 v10, v11, 3, v10\n\t" \
  "s_add_u32 s57, s59, 56\n\t" \
  "v_mul_u32_u24 v9, 4160, v6\n\t" \
  "v_lshl_add_u32 v9, v7, 3, v9\n\t" \
  "v_lshlrev_b32 v11, 4, v11\n\t" \
  "v_sub_u32 v9, v9, v11\n\t" \
  "v_add_u32 v9, s57, v9\n\t" \
  "ds_read_b64_tr_b16 v[192:193], v9 offset:8\n\t" \
  "ds_read_b64_tr_b16 v[194:195], v9 offset:0\n\t" \
  "ds_read_b64_tr_b16 v[196:197], v9 offset:520\n\t" \
  "ds_read_b64_tr_b16 v[198:199], v9 offset:512\n\t" \
  "ds_read_b64_tr_b16 v[200:201], v9 offset:1032\n\t" \
  "ds_read_b64_tr_b16 v[202:203], v9 offset:1024\n\t" \
  "s_waitcnt lgkmcnt(0)\n\t" \
  "s_barrier\n\t" \
  "ds_read_b128 v[128:131], v8 offset:64\n\t" \
  "ds_read_b128 v[132:135], v8 offset:576\n\t" \
  "ds_read_b128 v[136:139], v8 offset:1088\n\t" \
  "ds_read_b128 v[140:143], v8 offset:1600\n\t" \
  "ds_read_b128 v[144:147], v8 offset:2112\n\t" \
  "ds_read_b128 v[148:151], v8 offset:2624\n\t" \
  "ds_read_b128 v[152:155], v8 offset:3136\n\t" \
  "ds_read_b128 v[156:159], v8 offset:3648\n\t" \
  "ds_read_b64_tr_b16 v[204:205], v9 offset:1544\n\t" \
  "ds_read_b64_tr_b16 v[206:207], v9 offset:1536\n\t" \
  "ds_read_b64_tr_b16 v[208:209], v9 offset:2056\n\t" \
  "ds_read_b64_tr_b16 v[210:211], v9 offset:2048\n\t" \
  "ds_read_b64_tr_b16 v[212:213], v9 offset:2568\n\t" \
  "ds_read_b64_tr_b16 v[214:215], v9 offset:2560\n\t" \
  "ds_read_b64_tr_b16 v[216:217], v9 offset:3080\n\t" \
  "ds_read_b64_tr_b16 v[218:219], v9 offset:3072\n\t" \
  "ds_read_b64_tr_b16 v[220:221], v9 offset:3592\n\t" \
  "ds_read_b64_tr_b16 v[222:223], v9 offset:3584\n\t" \
  "s_waitcnt lgkmcnt(15)\n\t" \
  "v_mfma_f32_16x16x32_f16 v[96:99], v[192:195], v[128:131], 0\n\t" \
  "ds_read_b64_tr_b16 v[224:225], v9 offset:72\n\t" \
  "s_waitcnt lgkmcnt(15)\n\t" \
  "v_mfma_f32_16x16x32_f16 v[100:103], v[192:195], v[132:135], 0\n\t" \
  "ds_read_b64_tr_b16 v[226:227], v9 offset:64\n\t" \
  "s_waitcnt lgkmcnt(15)\n\t" \
  "v_mfma_f32_16x16x32_f16 v[104:107], v[192:195], v[136:139], 0\n\t" \
  "ds_read_b128 v[160:163], v8 offset:0\n\t" \
  "s_waitcnt lgkmcnt(15)\n\t" \
  "v_mfma_f32_16x16x32_f16 v[108:111], v[192:195], v[140:143], 0\n\t" \
  "ds_read_b64_tr_b16 v[228:229], v9 offset:584\n\t" \
  "s_waitcnt lgkmcnt(15)\n\t" \
  "v_mfma_f32_16x16x32_f16 v[112:115], v[192:195], v[144:147], 0\n\t" \
  "ds_read_b64_tr_b16 v[230:231], v9 offset:576\n\t" \
  "s_waitcnt lgkmcnt(15)\n\t" \
  "v_mfma_f32_16x16x32_f16 v[116:119], v[192:195], v[148:151], 0\n\t" \
  "ds_read_b128 v[164:167], v8 offset:512\n\t" \
  "s_waitcnt lgkmcnt(15)\n\t" \
  "v_mfma_f32_16x16x32_f16 v[120:123], v[192:195], v[152:155], 0\n\t" \
  "ds_read_b64_tr_b16 v[232:233], v9 offset:1096\n\t" \
  "s_waitcnt lgkmcnt(15)\n\t" \
  "v_mfma_f32_16x16x32_f16 v[124:127], v[192:195], v[156:159], 0\n\t" \
  "ds_read_b64_tr_b16 v[234:235], v9 offset:1088\n\t" \
  "v_mfma_f32_16x16x32_f16 v[100:103], v[196:199], v[128:131], v[100:103]\n\t" \
  "ds_read_b128 v[168:171], v8 offset:1024\n\t" \
  "v_mfma_f32_16x16x32_f16 v[104:107], v[196:199], v[132:135], v[104:107]\n\t" \
  "ds_read_b64_tr_b16 v[236:237], v9 offset:1608\n\t" \
  "v_mfma_f32_16x16x32_f16 v[108:111], v[196:199], v[136:139], v[108:111]\n\t" \
  "ds_read_b64_tr_b16 v[238:239], v9 offset:1600\n\t" \
  "v_mfma_f32_16x16x32_f16 v[112:115], v[196:199], v[140:143], v[112:115]\n\t" \
  "ds_read_b128 v[172:175], v8 offset:1536\n\t" \
  "v_mfma_f32_16x16x32_f16 v[116:119], v[196:199], v[144:147], v[116:119]\n\t" \
  "ds_read_b64_tr_b16 v[240:241], v9 offset:2120\n\t" \
  "v_mfma_f32_16x16x32_f16 v[120:123], v[196:199], v[148:151], v[120:123]\n\t" \
  "ds_read_b64_tr_b16 v[242:243], v9 offset:2112\n\t" \
  "v_mfma_f32_16x16x32_f16 v[124:127], v[196:199], v[152:155], v[124:127]\n\t" \
  "ds_read_b128 v[176:179], v8 offset:2048\n\t" \
  "v_mfma_f32_16x16x32_f16 v[104:107], v[200:203], v[128:131], v[104:107]\n\t" \
  "ds_read_b64_tr_b16 v[244:245], v9 offset:2632\n\t" \
  "v_mfma_f32_16x16x32_f16 v[108:111], v[200:203], v[132:135], v[108:111]\n\t" \
  "ds_read_b64_tr_b16 v[246:247], v9 offset:2624\n\t" \
  "v_mfma_f32_16x16x32_f16 v[112:115], v[200:203], v[136:139], v[112:115]\n\t" \
  "ds_read_b128 v[180:183], v8 offset:2560\n\t" \
  "v_mfma_f32_16x16x32_f16 v[116:119], v[200:203], v[140:143], v[116:119]\n\t" \
  "ds_read_b64_tr_b16 v[248:249], v9 offset:3144\n\t" \
  "v_mfma_f32_16x16x32_f16 v[120:123], v[200:203], v[144:147], v[120:123]\n\t" \
  "ds_read_b64_tr_b16 v[250:251], v9 offset:3136\n\t" \
  "v_mfma_f32_16x16x32_f16 v[124:127], v[200:203], v[148:151], v[124:127]\n\t" \
  "ds_read_b128 v[184:187], v8 offset:3072\n\t" \
  "s_waitcnt lgkmcnt(15)\n\t" \
  "v_mfma_f32_16x16x32_f16 v[108:111], v[204:207], v[128:131], v[108:111]\n\t" \
  "ds_read_b64_tr_b16 v[252:253], v9 offset:3656\n\t" \
  "v_mfma_f32_16x16x32_f16 v[112:115], v[204:207], v[132:135], v[112:115]\n\t" \
  "ds_read_b64_tr_b16 v[254:255], v9 offset:3648\n\t" \
  "v_mfma_f32_16x16x32_f16 v[116:119], v[204:207], v[136:139], v[116:119]\n\t" \
  "ds_read_b128 v[188:191], v8 offset:3584\n\t" \
  "v_mfma_f32_16x16x32_f16 v[120:123], v[204:207], v[140:143], v[120:123]\n\t" \
  "v_subrev_u32 v8, 128, v8\n\t" \
  "v_mfma_f32_16x16x32_f16 v[124:127], v[204:207], v[144:147], v[124:127]\n\t" \
  "v_add_u32 v9, 128, v9\n\t" \
  "v_mfma_f32_16x16x32_f16 v[112:115], v[208:211], v[128:131], v[112:115]\n\t" \
  "v_mfma_f32_16x16x32_f16 v[116:119], v[208:211], v[132:135], v[116:119]\n\t" \
  "v_mfma_f32_16x16x32_f16 v[120:123], v[208:211], v[136:139], v[120:123]\n\t" \
  "v_mfma_f32_16x16x32_f16 v[124:127], v[208:211], v[140:143], v[124:127]\n\t" \
  "v_mfma_f32_16x16x32_f16 v[116:119], v[212:215], v[128:131], v[116:119]\n\t" \
  "v_mfma_f32_16x16x32_f16 v[120:123], v[212:215], v[132:135], v[120:123]\n\t" \
  "v_mfma_f32_16x16x32_f16 v[124:127], v[212:215], v[136:139], v[124:127]\n\t" \
  "v_mfma_f32_16x16x32_f16 v[120:123], v[216:219], v[128:131], v[120:123]\n\t" \
  "v_mfma_f32_16x16x32_f16 v[124:127], v[216:219], v[132:135], v[124:127]\n\t" \
  "v_mfma_f32_16x16x32_f16 v[124:127], v[220:223], v[128:131], v[124:127]\n\t" \
  "s_mov_b32 s58, 3\n\t" \
  "CLOOP_%=:\n\t" \
  "s_waitcnt lgkmcnt(0)\n\t" \
  "v_mfma_f32_16x16x32_f16 v[96:99], v[224:227], v[160:163], v[96:99]\n\t" \
  "ds_read_b64_tr_b16 v[192:193], v9 offset:8\n\t" \
  "v_mfma_f32_16x16x32_f16 v[100:103], v[224:227], v[164:167], v[100:103]\n\t" \
  "ds_read_b64_tr_b16 v[194:195], v9 offset:0\n\t" \
  "v_mfma_f32_16x16x32_f16 v[104:107], v[224:227], v[168:171], v[104:107]\n\t" \
  "ds_read_b128 v[128:131], v8 offset:64\n\t" \
  "v_mfma_f32_16x16x32_f16 v[108:111], v[224:227], v[172:175], v[108:111]\n\t" \
  "ds_read_b64_tr_b16 v[196:197], v9 offset:520\n\t" \
  "v_mfma_f32_16x16x32_f16 v[112:115], v[224:227], v[176:179], v[112:115]\n\t" \
  "ds_read_b64_tr_b16 v[198:199], v9 offset:512\n\t" \
  "v_mfma_f32_16x16x32_f16 v[116:119], v[224:227], v[180:183], v[116:119]\n\t" \
  "ds_read_b128 v[132:135], v8 offset:576\n\t" \
  "v_mfma_f32_16x16x32_f16 v[120:123], v[224:227], v[184:187], v[120:123]\n\t" \
  "ds_read_b64_tr_b16 v[200:201], v9 offset:1032\n\t" \
  "v_mfma_f32_16x16x32_f16 v[124:127], v[224:227], v[188:191], v[124:127]\n\t" \
  "ds_read_b64_tr_b16 v[202:203], v9 offset:1024\n\t" \
  "v_mfma_f32_16x16x32_f16 v[100:103], v[228:231], v[160:163], v[100:103]\n\t" \
  "ds_read_b128 v[136:139], v8 offset:1088\n\t" \
  "v_mfma_f32_16x16x32_f16 v[104:107], v[228:231], v[164:167], v[104:107]\n\t" \
  "ds_read_b64_tr_b16 v[204:205], v9 offset:1544\n\t" \
  "v_mfma_f32_16x16x32_f16 v[108:111], v[228:231], v[168:171], v[108:111]\n\t" \
  "ds_read_b64_tr_b16 v[206:207], v9 offset:1536\n\t" \
  "v_mfma_f32_16x16x32_f16 v[112:115], v[228:231], v[172:175], v[112:115]\n\t" \
  "ds_read_b128 v[140:143], v8 offset:1600\n\t" \
  "v_mfma_f32_16x16x32_f16 v[116:119], v[228:231], v[176:179], v[116:119]\n\t" \
  "ds_read_b64_tr_b16 v[208:209], v9 offset:2056\n\t" \
  "v_mfma_f32_16x16x32_f16 v[120:123], v[228:231], v[180:183], v[120:123]\n\t" \
  "ds_read_b64_tr_b16 v[210:211], v9 offset:2048\n\t" \
  "v_mfma_f32_16x16x32_f16 v[124:127], v[228:231], v[184:187], v[124:127]\n\t" \
  "ds_read_b128 v[144:147], v8 offset:2112\n\t" \
  "v_mfma_f32_16x16x32_f16 v[104:107], v[232:235], v[160:163], v[104:107]\n\t" \
  "ds_read_b64_tr_b16 v[212:213], v9 offset:2568\n\t" \
  "v_mfma_f32_16x16x32_f16 v[108:111], v[232:235], v[164:167], v[108:111]\n\t" \
  "ds_read_b64_tr_b16 v[214:215], v9 offset:2560\n\t" \
  "v_mfma_f32_16x16x32_f16 v[112:115], v[232:235], v[168:171], v[112:115]\n\t" \
  "ds_read_b128 v[148:151], v8 offset:2624\n\t" \
  "v_mfma_f32_16x16x32_f16 v[116:119], v[232:235], v[172:175], v[116:119]\n\t" \
  "ds_read_b64_tr_b16 v[216:217], v9 offset:3080\n\t" \
  "v_mfma_f32_16x16x32_f16 v[120:123], v[232:235], v[176:179], v[120:123]\n\t" \
  "ds_read_b64_tr_b16 v[218:219], v9 offset:3072\n\t" \
  "v_mfma_f32_16x16x32_f16 v[124:127], v[232:235], v[180:183], v[124:127]\n\t" \
  "ds_read_b128 v[152:155], v8 offset:3136\n\t" \
  "v_mfma_f32_16x16x32_f16 v[108:111], v[236:239], v[160:163], v[108:111]\n\t" \
  "ds_read_b64_tr_b16 v[220:221], v9 offset:3592\n\t" \
  "v_mfma_f32_16x16x32_f16 v[112:115], v[236:239], v[164:167], v[112:115]\n\t" \
  "ds_read_b64_tr_b16 v[222:223], v9 offset:3584\n\t" \
  "v_mfma_f32_16x16x32_f16 v[116:119], v[236:239], v[168:171], v[116:119]\n\t" \
  "ds_read_b128 v[156:159], v8 offset:3648\n\t" \
  "v_mfma_f32_16x16x32_f16 v[120:123], v[236:239], v[172:175], v[120:123]\n\t" \
  "v_mfma_f32_16x16x32_f16 v[124:127], v[236:239], v[176:179], v[124:127]\n\t" \
  "v_mfma_f32_16x16x32_f16 v[112:115], v[240:243], v[160:163], v[112:115]\n\t" \
  "v_mfma_f32_16x16x32_f16 v[116:119], v[240:243], v[164:167], v[116:119]\n\t" \
  "v_mfma_f32_16x16x32_f16 v[120:123], v[240:243], v[168:171], v[120:123]\n\t" \
  "v_mfma_f32_16x16x32_f16 v[124:127], v[240:243], v[172:175], v[124:127]\n\t" \
  "v_mfma_f32_16x16x32_f16 v[116:119], v[244:247], v[160:163], v[116:119]\n\t" \
  "v_mfma_f32_16x16x32_f16 v[120:123], v[244:247], v[164:167], v[120:123]\n\t" \
  "v_mfma_f32_16x16x32_f16 v[124:127], v[244:247], v[168:171], v[124:127]\n\t" \
  "v_mfma_f32_16x16x32_f16 v[120:123], v[248:251], v[160:163], v[120:123]\n\t" \
  "v_mfma_f32_16x16x32_f16 v[124:127], v[248:251], v[164:167], v[124:127]\n\t" \
  "v_mfma_f32_16x16x32_f16 v[124:127], v[252:255], v[160:163], v[124:127]\n\t" \
  "s_waitcnt lgkmcnt(0)\n\t" \
  "v_mfma_f32_16x16x32_f16 v[96:99], v[192:195], v[128:131], v[96:99]\n\t" \
  "ds_read_b64_tr_b16 v[224:225], v9 offset:72\n\t" \
  "v_mfma_f32_16x16x32_f16 v[100:103], v[192:195], v[132:135], v[100:103]\n\t" \
  "ds_read_b64_tr_b16 v[226:227], v9 offset:64\n\t" \
  "v_mfma_f32_16x16x32_f16 v[104:107], v[192:195], v[136:139], v[104:107]\n\t" \
  "ds_read_b128 v[160:163], v8 offset:0\n\t" \
  "v_mfma_f32_16x16x32_f16 v[108:111], v[192:195], v[140:143], v[108:111]\n\t" \
  "ds_read_b64_tr_b16 v[228:229], v9 offset:584\n\t" \
  "v_mfma_f32_16x16x32_f16 v[112:115], v[192:195], v[144:147], v[112:115]\n\t" \
  "ds_read_b64_tr_b16 v[230:231], v9 offset:576\n\t" \
  "v_mfma_f32_16x16x32_f16 v[116:119], v[192:195], v[148:151], v[116:119]\n\t" \
  "ds_read_b128 v[164:167], v8 offset:512\n\t" \
  "v_mfma_f32_16x16x32_f16 v[120:123], v[192:195], v[152:155], v[120:123]\n\t" \
  "ds_read_b64_tr_b16 v[232:233], v9 offset:1096\n\t" \
  "v_mfma_f32_16x16x32_f16 v[124:127], v[192:195], v[156:159], v[124:127]\n\t" \
  "ds_read_b64_tr_b16 v[234:235], v9 offset:1088\n\t" \
  "v_mfma_f32_16x16x32_f16 v[100:103], v[196:199], v[128:131], v[100:103]\n\t" \
  "ds_read_b128 v[168:171], v8 offset:1024\n\t" \
  "v_mfma_f32_16x16x32_f16 v[104:107], v[196:199], v[132:135], v[104:107]\n\t" \
  "ds_read_b64_tr_b16 v[236:237], v9 offset:1608\n\t" \
  "v_mfma_f32_16x16x32_f16 v[108:111], v[196:199], v[136:139], v[108:111]\n\t" \
  "ds_read_b64_tr_b16 v[238:239], v9 offset:1600\n\t" \
  "v_mfma_f32_16x16x32_f16 v[112:115], v[196:199], v[140:143], v[112:115]\n\t" \
  "ds_read_b128 v[172:175], v8 offset:1536\n\t" \
  "v_mfma_f32_16x16x32_f16 v[116:119], v[196:199], v[144:147], v[116:119]\n\t" \
  "ds_read_b64_tr_b16 v[240:241], v9 offset:2120\n\t" \
  "v_mfma_f32_16x16x32_f16 v[120:123], v[196:199], v[148:151], v[120:123]\n\t" \
  "ds_read_b64_tr_b16 v[242:243], v9 offset:2112\n\t" \
  "v_mfma_f32_16x16x32_f16 v[124:127], v[196:199], v[152:155], v[124:127]\n\t" \
  "ds_read_b128 v[176:179], v8 offset:2048\n\t" \
  "v_mfma_f32_16x16x32_f16 v[104:107], v[200:203], v[128:131], v[104:107]\n\t" \
  "ds_read_b64_tr_b16 v[244:245], v9 offset:2632\n\t" \
  "v_mfma_f32_16x16x32_f16 v[108:111], v[200:203], v[132:135], v[108:111]\n\t" \
  "ds_read_b64_tr_b16 v[246:247], v9 offset:2624\n\t" \
  "v_mfma_f32_16x16x32_f16 v[112:115], v[200:203], v[136:139], v[112:115]\n\t" \
  "ds_read_b128 v[180:183], v8 offset:2560\n\t" \
  "v_mfma_f32_16x16x32_f16 v[116:119], v[200:203], v[140:143], v[116:119]\n\t" \
  "ds_read_b64_tr_b16 v[248:249], v9 offset:3144\n\t" \
  "v_mfma_f32_16x16x32_f16 v[120:123], v[200:203], v[144:147], v[120:123]\n\t" \
  "ds_read_b64_tr_b16 v[250:251], v9 offset:3136\n\t" \
  "v_mfma_f32_16x16x32_f16 v[124:127], v[200:203], v[148:151], v[124:127]\n\t" \
  "ds_read_b128 v[184:187], v8 offset:3072\n\t" \
  "v_mfma_f32_16x16x32_f16 v[108:111], v[204:207], v[128:131], v[108:111]\n\t" \
  "ds_read_b64_tr_b16 v[252:253], v9 offset:3656\n\t" \
  "v_mfma_f32_16x16x32_f16 v[112:115], v[204:207], v[132:135], v[112:115]\n\t" \
  "ds_read_b64_tr_b16 v[254:255], v9 offset:3648\n\t" \
  "v_mfma_f32_16x16x32_f16 v[116:119], v[204:207], v[136:139], v[116:119]\n\t" \
  "ds_read_b128 v[188:191], v8 offset:3584\n\t" \
  "v_mfma_f32_16x16x32_f16 v[120:123], v[204:207], v[140:143], v[120:123]\n\t" \
  "v_subrev_u32 v8, 128, v8\n\t" \
  "v_mfma_f32_16x16x32_f16 v[124:127], v[204:207], v[144:147], v[124:127]\n\t" \
  "v_add_u32 v9, 128, v9\n\t" \
  "v_mfma_f32_16x16x32_f16 v[112:115], v[208:211], v[128:131], v[112:115]\n\t" \
  "s_sub_u32 s58, s58, 1\n\t" \
  "v_mfma_f32_16x16x32_f16 v[116:119], v[208:211], v[132:135], v[116:119]\n\t" \
  "s_cmp_lg_u32 s58, 0\n\t" \
  "v_mfma_f32_16x16x32_f16 v[120:123], v[208:211], v[136:139], v[120:123]\n\t" \
  "v_mfma_f32_16x16x32_f16 v[124:127], v[208:211], v[140:143], v[124:127]\n\t" \
  "v_mfma_f32_16x16x32_f16 v[116:119], v[212:215], v[128:131], v[116:119]\n\t" \
  "v_mfma_f32_16x16x32_f16 v[120:123], v[212:215], v[132:135], v[120:123]\n\t" \
  "v_mfma_f32_16x16x32_f16 v[124:127], v[212:215], v[136:139], v[124:127]\n\t" \
  "v_mfma_f32_16x16x32_f16 v[120:123], v[216:219], v[128:131], v[120:123]\n\t" \
  "v_mfma_f32_16x16x32_f16 v[124:127], v[216:219], v[132:135], v[124:127]\n\t" \
  "v_mfma_f32_16x16x32_f16 v[124:127], v[220:223], v[128:131], v[124:127]\n\t" \
  "s_cbranch_scc1 CLOOP_%=\n\t" \
  "s_waitcnt lgkmcnt(0)\n\t" \
  "v_mfma_f32_16x16x32_f16 v[96:99], v[224:227], v[160:163], v[96:99]\n\t" \
  "v_mfma_f32_16x16x32_f16 v[100:103], v[224:227], v[164:167], v[100:103]\n\t" \
  "v_mfma_f32_16x16x32_f16 v[104:107], v[224:227], v[168:171], v[104:107]\n\t" \
  "v_mfma_f32_16x16x32_f16 v[108:111], v[224:227], v[172:175], v[108:111]\n\t" \
  "v_mfma_f32_16x16x32_f16 v[112:115], v[224:227], v[176:179], v[112:115]\n\t" \
  "v_mfma_f32_16x16x32_f16 v[116:119], v[224:227], v[180:183], v[116:119]\n\t" \
  "v_mfma_f32_16x16x32_f16 v[120:123], v[224:227], v[184:187], v[120:123]\n\t" \
  "v_mfma_f32_16x16x32_f16 v[124:127], v[224:227], v[188:191], v[124:127]\n\t" \
  "v_mfma_f32_16x16x32_f16 v[100:103], v[228:231], v[160:163], v[100:103]\n\t" \
  "v_mfma_f32_16x16x32_f16 v[104:107], v[228:231], v[164:167], v[104:107]\n\t" \
  "v_mfma_f32_16x16x32_f16 v[108:111], v[228:231], v[168:171], v[108:111]\n\t" \
  "v_cvt_pk_bf16_f32 v96, v96, v97\n\t" \
  "v_mfma_f32_16x16x32_f16 v[112:115], v[228:231], v[172:175], v[112:115]\n\t" \
  "v_cvt_pk_bf16_f32 v97, v98, v99\n\t" \
  "v_mfma_f32_16x16x32_f16 v[116:119], v[228:231], v[176:179], v[116:119]\n\t" \
  "ds_write_b64 v10, v[96:97] offset:0\n\t" \
  "v_mfma_f32_16x16x32_f16 v[120:123], v[228:231], v[180:183], v[120:123]\n\t" \
  "v_mfma_f32_16x16x32_f16 v[124:127], v[228:231], v[184:187], v[124:127]\n\t" \
  "v_mfma_f32_16x16x32_f16 v[104:107], v[232:235], v[160:163], v[104:107]\n\t" \
  "v_mfma_f32_16x16x32_f16 v[108:111], v[232:235], v[164:167], v[108:111]\n\t" \
  "v_cvt_pk_bf16_f32 v100, v100, v101\n\t" \
  "v_mfma_f32_16x16x32_f16 v[112:115], v[232:235], v[168:171], v[112:115]\n\t" \
  "v_cvt_pk_bf16_f32 v101, v102, v103\n\t" \
  "v_mfma_f32_16x16x32_f16 v[116:119], v[232:235], v[172:175], v[116:119]\n\t" \
  "ds_write_b64 v10, v[100:101] offset:512\n\t" \
  "v_mfma_f32_16x16x32_f16 v[120:123], v[232:235], v[176:179], v[120:123]\n\t" \
  "v_mfma_f32_16x16x32_f16 v[124:127], v[232:235], v[180:183], v[124:127]\n\t" \
  "v_mfma_f32_16x16x32_f16 v[108:111], v[236:239], v[160:163], v[108:111]\n\t" \
  "v_mfma_f32_16x16x32_f16 v[112:115], v[236:239], v[164:167], v[112:115]\n\t" \
  "v_cvt_pk_bf16_f32 v104, v104, v105\n\t" \
  "v_mfma_f32_16x16x32_f16 v[116:119], v[236:239], v[168:171], v[116:119]\n\t" \
  "v_cvt_pk_bf16_f32 v105, v106, v107\n\t" \
  "v_mfma_f32_16x16x32_f16 v[120:123], v[236:239], v[172:175], v[120:123]\n\t" \
  "ds_write_b64 v10, v[104:105] offset:1024\n\t" \
  "v_mfma_f32_16x16x32_f16 v[124:127], v[236:239], v[176:179], v[124:127]\n\t" \
  "v_mfma_f32_16x16x32_f16 v[112:115], v[240:243], v[160:163], v[112:115]\n\t" \
  "v_mfma_f32_16x16x32_f16 v[116:119], v[240:243], v[164:167], v[116:119]\n\t" \
  "v_mfma_f32_16x16x32_f16 v[120:123], v[240:243], v[168:171], v[120:123]\n\t" \
  "v_cvt_pk_bf16_f32 v108, v108, v109\n\t" \
  "v_mfma_f32_16x16x32_f16 v[124:127], v[240:243], v[172:175], v[124:127]\n\t" \
  "v_cvt_pk_bf16_f32 v109, v110, v111\n\t" \
  "v_mfma_f32_16x16x32_f16 v[116:119], v[244:247], v[160:163], v[116:119]\n\t" \
  "ds_write_b64 v10, v[108:109] offset:1536\n\t" \
  "v_mfma_f32_16x16x32_f16 v[120:123], v[244:247], v[164:167], v[120:123]\n\t" \
  "v_mfma_f32_16x16x32_f16 v[124:127], v[244:247], v[168:171], v[124:127]\n\t" \
  "v_cvt_pk_bf16_f32 v112, v112, v113\n\t" \
  "v_mfma_f32_16x16x32_f16 v[120:123], v[248:251], v[160:163], v[120:123]\n\t" \
  "v_cvt_pk_bf16_f32 v113, v114, v115\n\t" \
  "v_mfma_f32_16x16x32_f16 v[124:127], v[248:251], v[164:167], v[124:127]\n\t" \
  "ds_write_b64 v10, v[112:113] offset:2048\n\t" \
  "v_mfma_f32_16x16x32_f16 v[124:127], v[252:255], v[160:163], v[124:127]\n\t" \
  "s_nop 3\n\t" \
  "v_cvt_pk_bf16_f32 v116, v116, v117\n\t" \
  "v_cvt_pk_bf16_f32 v117, v118, v119\n\t" \
  "ds_write_b64 v10, v[116:117] offset:2560\n\t" \
  "v_cvt_pk_bf16_f32 v120, v120, v121\n\t" \
  "v_cvt_pk_bf16_f32 v121, v122, v123\n\t" \
  "ds_write_b64 v10, v[120:121] offset:3072\n\t" \
  "v_cvt_pk_bf16_f32 v124, v124, v125\n\t" \
  "v_cvt_pk_bf16_f32 v125, v126, v127\n\t" \
  "ds_write_b64 v10, v[124:125] offset:3584\n\t" \
  "s_waitcnt lgkmcnt(0)\n\t" \
  "s_barrier\n\t" \
  "s_branch END_%=\n\t" \
  "ROWS_%=:\n\t" \
  "ds_read2st64_b32 v[40:41], v64 offset0:0 offset1:13\n\t" \
  "ds_read2st64_b32 v[42:43], v65 offset0:0 offset1:13\n\t" \
  "ds_read2st64_b32 v[44:45], v66 offset0:0 offset1:13\n\t" \
  "ds_read2st64_b32 v[46:47], v67 offset0:0 offset1:13\n\t" \
  "s_lshl_b32 s57, s46, 14\n\t" \
  "s_add_u32 s57, s57, 41947136\n\t" \
  "s_add_u32 s62, s40, s57\n\t" \
  "s_addc_u32 s63, s41, 0\n\t" \
  "s_mov_b32 s58, 60000\n\t" \
  "s_waitcnt vmcnt(0)\n\t" \
  "POLL_%=:\n\t" \
  "v_xor_b32 v8, s49, v201\n\t" \
  "v_xor_b32 v12, s49, v203\n\t" \
  "v_xor_b32 v9, s49, v205\n\t" \
  "v_xor_b32 v13, s49, v207\n\t" \
  "v_xor_b32 v10, s49, v209\n\t" \
  "v_xor_b32 v14, s49, v211\n\t" \
  "v_xor_b32 v11, s49, v213\n\t" \
  "v_xor_b32 v15, s49, v215\n\t" \
  "v_or3_b32 v8, v8, v9, v10\n\t" \
  "v_or3_b32 v11, v11, v12, v13\n\t" \
  "v_or3_b32 v8, v8, v14, v15\n\t" \
  "v_or_b32 v8, v8, v11\n\t" \
  "v_cmp_ne_u32 vcc, 0, v8\n\t" \
  "s_sub_u32 s58, s58, 1\n\t" \
  "s_cbranch_vccz GOT_%=\n\t" \
  "s_cmp_eq_u32 s58, 0\n\t" \
  "s_cbranch_scc1 GOT_%=\n\t" \
  "s_sleep 4\n\t" \
  "global_load_dwordx4 v[200:203], v6, s[60:61] offset:-4096 sc1\n\t" \
  "global_load_dwordx4 v[204:207], v6, s[60:61] offset:0 sc1\n\t" \
  "global_load_dwordx4 v[208:211], v7, s[60:61] offset:-4096 sc1\n\t" \
  "global_load_dwordx4 v[212:215], v7, s[60:61] offset:0 sc1\n\t" \
  "s_waitcnt vmcnt(0)\n\t" \
  "s_branch POLL_%=\n\t" \
  "GOT_%=:\n\t" \
  "global_load_dwordx4 v[216:219], v6, s[62:63] offset:-4096 sc1\n\t" \
  "global_load_dwordx4 v[220:223], v6, s[62:63] offset:0 sc1\n\t" \
  "global_load_dwordx4 v[224:227], v7, s[62:63] offset:-4096 sc1\n\t" \
  "global_load_dwordx4 v[228:231], v7, s[62:63] offset:0 sc1\n\t" \
  "v_and_b32 v8, 0xffff0000, v200\n\t" \
  "v_lshlrev_b32 v16, 16, v200\n\t" \
  "v_rcp_f32 v8, v8\n\t" \
  "v_and_b32 v9, 0xffff0000, v202\n\t" \
  "v_lshlrev_b32 v17, 16, v202\n\t" \
  "v_rcp_f32 v9, v9\n\t" \
  "v_and_b32 v10, 0xffff0000, v204\n\t" \
  "v_lshlrev_b32 v18, 16, v204\n\t" \
  "v_rcp_f32 v10, v10\n\t" \
  "v_and_b32 v11, 0xffff0000, v206\n\t" \
  "v_lshlrev_b32 v19, 16, v206\n\t" \
  "v_rcp_f32 v11, v11\n\t" \
  "v_and_b32 v12, 0xffff0000, v208\n\t" \
  "v_lshlrev_b32 v20, 16, v208\n\t" \
  "v_rcp_f32 v12, v12\n\t" \
  "v_and_b32 v13, 0xffff0000, v210\n\t" \
  "v_lshlrev_b32 v21, 16, v210\n\t" \
  "v_rcp_f32 v13, v13\n\t" \
  "v_and_b32 v14, 0xffff0000, v212\n\t" \
  "v_lshlrev_b32 v22, 16, v212\n\t" \
  "v_rcp_f32 v14, v14\n\t" \
  "v_and_b32 v15, 0xffff0000, v214\n\t" \
  "v_lshlrev_b32 v23, 16, v214\n\t" \
  "v_rcp_f32 v15, v15\n\t" \
  "s_waitcnt lgkmcnt(0)\n\t" \
  "v_add_f32 v40, v40, v41\n\t" \
  "v_add_f32 v42, v42, v43\n\t" \
  "v_add_f32 v44, v44, v45\n\t" \
  "v_add_f32 v46, v46, v47\n\t" \
  "v_cvt_pk_f16_f32 v48, v40, v42\n\t" \
  "v_cvt_pk_f16_f32 v49, v42, v42\n\t" \
  "v_cvt_pk_f16_f32 v50, v42, v42\n\t" \
  "v_cvt_pk_f16_f32 v51, v42, v42\n\t" \
  "v_cvt_pk_f16_f32 v52, v44, v44\n\t" \
  "v_cvt_pk_f16_f32 v53, v44, v44\n\t" \
  "v_cvt_pk_f16_f32 v54, v44, v44\n\t" \
  "v_cvt_pk_f16_f32 v55, v44, v46\n\t" \
  "ds_write_b128 v5, v[48:51] offset:14560\n\t" \
  "ds_write_b128 v5, v[52:55] offset:14576\n\t" \
  "v_cvt_pk_f16_f32 v48, v42, v42\n\t" \
  "v_cvt_pk_f16_f32 v51, v42, v44\n\t" \
  "v_cvt_pk_f16_f32 v55, v46, v46\n\t" \
  "ds_write_b128 v5, v[48:51] offset:10400\n\t" \
  "ds_write_b128 v5, v[52:55] offset:10416\n\t" \
  "v_cvt_pk_f16_f32 v51, v44, v44\n\t" \
  "v_cvt_pk_f16_f32 v54, v44, v46\n\t" \
  "ds_write_b128 v5, v[48:51] offset:6240\n\t" \
  "ds_write_b128 v5, v[52:55] offset:6256\n\t" \
  "v_cvt_pk_f16_f32 v50, v42, v44\n\t" \
  "v_cvt_pk_f16_f32 v54, v46, v46\n\t" \
  "ds_write_b128 v5, v[48:51] offset:2080\n\t" \
  "ds_write_b128 v5, v[52:55] offset:2096\n\t" \
  "s_mul_i32 s57, s47, 4608\n\t" \
  "s_add_u32 s57, s57, 136016\n\t" \
  "v_lshl_add_u32 v4, v0, 3, s57\n\t" \
  "ds_write_b64 v4, v[24:25]\n\t" \
  "s_and_b32 s55, s53, 0xffff0000\n\t" \
  "s_lshl_b32 s54, s53, 16\n\t" \
  "s_and_b32 s53, s52, 0xffff0000\n\t" \
  "s_lshl_b32 s52, s52, 16\n\t" \
  "v_lshlrev_b32 v4, 2, v3\n\t" \
  "v_add_u32 v4, 136528, v4\n\t" \
  "s_waitcnt vmcnt(3)\n\t" \
  "v_lshlrev_b32 v128, 16, v216\n\t" \
  "v_and_b32 v136, 0xffff0000, v216\n\t" \
  "v_lshlrev_b32 v144, 16, v217\n\t" \
  "v_and_b32 v152, 0xffff0000, v217\n\t" \
  "v_lshlrev_b32 v129, 16, v218\n\t" \
  "v_and_b32 v137, 0xffff0000, v218\n\t" \
  "v_lshlrev_b32 v145, 16, v219\n\t" \
  "v_and_b32 v153, 0xffff0000, v219\n\t" \
  "v_pk_add_f32 v[96:97], v[128:129], v[16:17] neg_lo:[0,1] neg_hi:[0,1]\n\t" \
  "v_pk_add_f32 v[104:105], v[136:137], v[16:17] neg_lo:[0,1] neg_hi:[0,1]\n\t" \
  "v_pk_add_f32 v[112:113], v[144:145], v[16:17] neg_lo:[0,1] neg_hi:[0,1]\n\t" \
  "v_pk_add_f32 v[120:121], v[152:153], v[16:17] neg_lo:[0,1] neg_hi:[0,1]\n\t" \
  "v_pk_mul_f32 v[96:97], v[96:97], v[8:9]\n\t" \
  "v_pk_mul_f32 v[104:105], v[104:105], v[8:9]\n\t" \
  "v_pk_mul_f32 v[112:113], v[112:113], v[8:9]\n\t" \
  "v_pk_mul_f32 v[120:121], v[120:121], v[8:9]\n\t" \
  "v_cvt_pk_f16_f32 v32, v96, v97\n\t" \
  "v_cvt_pk_f16_f32 v33, v104, v105\n\t" \
  "v_cvt_pk_f16_f32 v34, v112, v113\n\t" \
  "v_cvt_pk_f16_f32 v35, v120, v121\n\t" \
  "ds_write2st64_b32 v4, v32, v33 offset0:0 offset1:18\n\t" \
  "ds_write2st64_b32 v4, v34, v35 offset0:36 offset1:54\n\t" \
  "s_waitcnt vmcnt(2)\n\t" \
  "v_lshlrev_b32 v130, 16, v220\n\t" \
  "v_and_b32 v138, 0xffff0000, v220\n\t" \
  "v_lshlrev_b32 v146, 16, v221\n\t" \
  "v_and_b32 v154, 0xffff0000, v221\n\t" \
  "v_lshlrev_b32 v131, 16, v222\n\t" \
  "v_and_b32 v139, 0xffff0000, v222\n\t" \
  "v_lshlrev_b32 v147, 16, v223\n\t" \
  "v_and_b32 v155, 0xffff0000, v223\n\t" \
  "v_pk_add_f32 v[98:99], v[130:131], v[18:19] neg_lo:[0,1] neg_hi:[0,1]\n\t" \
  "v_pk_add_f32 v[106:107], v[138:139], v[18:19] neg_lo:[0,1] neg_hi:[0,1]\n\t" \
  "v_pk_add_f32 v[114:115], v[146:147], v[18:19] neg_lo:[0,1] neg_hi:[0,1]\n\t" \
  "v_pk_add_f32 v[122:123], v[154:155], v[18:19] neg_lo:[0,1] neg_hi:[0,1]\n\t" \
  "v_pk_mul_f32 v[98:99], v[98:99], v[10:11]\n\t" \
  "v_pk_mul_f32 v[106:107], v[106:107], v[10:11]\n\t" \
  "v_pk_mul_f32 v[114:115], v[114:115], v[10:11]\n\t" \
  "v_pk_mul_f32 v[122:123], v[122:123], v[10:11]\n\t" \
  "v_cvt_pk_f16_f32 v36, v98, v99\n\t" \
  "v_cvt_pk_f16_f32 v37, v106, v107\n\t" \
  "v_cvt_pk_f16_f32 v38, v114, v115\n\t" \
  "v_cvt_pk_f16_f32 v39, v122, v123\n\t" \
  "ds_write2st64_b32 v4, v36, v37 offset0:4 offset1:22\n\t" \
  "ds_write2st64_b32 v4, v38, v39 offset0:40 offset1:58\n\t" \
  "s_waitcnt vmcnt(1)\n\t" \
  "v_lshlrev_b32 v132, 16, v224\n\t" \
  "v_and_b32 v140, 0xffff0000, v224\n\t" \
  "v_lshlrev_b32 v148, 16, v225\n\t" \
  "v_and_b32 v156, 0xffff0000, v225\n\t" \
  "v_lshlrev_b32 v133, 16, v226\n\t" \
  "v_and_b32 v141, 0xffff0000, v226\n\t" \
  "v_lshlrev_b32 v149, 16, v227\n\t" \
  "v_and_b32 v157, 0xffff0000, v227\n\t" \
  "v_pk_add_f32 v[100:101], v[132:133], v[20:21] neg_lo:[0,1] neg_hi:[0,1]\n\t" \
  "v_pk_add_f32 v[108:109], v[140:141], v[20:21] neg_lo:[0,1] neg_hi:[0,1]\n\t" \
  "v_pk_add_f32 v[116:117], v[148:149], v[20:21] neg_lo:[0,1] neg_hi:[0,1]\n\t" \
  "v_pk_add_f32 v[124:125], v[156:157], v[20:21] neg_lo:[0,1] neg_hi:[0,1]\n\t" \
  "v_pk_mul_f32 v[100:101], v[100:101], v[12:13]\n\t" \
  "v_pk_mul_f32 v[108:109], v[108:109], v[12:13]\n\t" \
  "v_pk_mul_f32 v[116:117], v[116:117], v[12:13]\n\t" \
  "v_pk_mul_f32 v[124:125], v[124:125], v[12:13]\n\t" \
  "v_cvt_pk_f16_f32 v32, v100, v101\n\t" \
  "v_cvt_pk_f16_f32 v33, v108, v109\n\t" \
  "v_cvt_pk_f16_f32 v34, v116, v117\n\t" \
  "v_cvt_pk_f16_f32 v35, v124, v125\n\t" \
  "ds_write2st64_b32 v4, v32, v33 offset0:8 offset1:26\n\t" \
  "ds_write2st64_b32 v4, v34, v35 offset0:44 offset1:62\n\t" \
  "s_waitcnt vmcnt(0)\n\t" \
  "v_lshlrev_b32 v134, 16, v228\n\t" \
  "v_and_b32 v142, 0xffff0000, v228\n\t" \
  "v_lshlrev_b32 v150, 16, v229\n\t" \
  "v_and_b32 v158, 0xffff0000, v229\n\t" \
  "v_lshlrev_b32 v135, 16, v230\n\t" \
  "v_and_b32 v143, 0xffff0000, v230\n\t" \
  "v_lshlrev_b32 v151, 16, v231\n\t" \
  "v_and_b32 v159, 0xffff0000, v231\n\t" \
  "v_pk_add_f32 v[102:103], v[134:135], v[22:23] neg_lo:[0,1] neg_hi:[0,1]\n\t" \
  "v_pk_add_f32 v[110:111], v[142:143], v[22:23] neg_lo:[0,1] neg_hi:[0,1]\n\t" \
  "v_pk_add_f32 v[118:119], v[150:151], v[22:23] neg_lo:[0,1] neg_hi:[0,1]\n\t" \
  "v_pk_add_f32 v[126:127], v[158:159], v[22:23] neg_lo:[0,1] neg_hi:[0,1]\n\t" \
  "v_pk_mul_f32 v[102:103], v[102:103], v[14:15]\n\t" \
  "v_pk_mul_f32 v[110:111], v[110:111], v[14:15]\n\t" \
  "v_pk_mul_f32 v[118:119], v[118:119], v[14:15]\n\t" \
  "v_pk_mul_f32 v[126:127], v[126:127], v[14:15]\n\t" \
  "v_cvt_pk_f16_f32 v36, v102, v103\n\t" \
  "v_cvt_pk_f16_f32 v37, v110, v111\n\t" \
  "v_cvt_pk_f16_f32 v38, v118, v119\n\t" \
  "v_cvt_pk_f16_f32 v39, v126, v127\n\t" \
  "ds_write2st64_b32 v4, v36, v37 offset0:12 offset1:30\n\t" \
  "ds_write2st64_b32 v4, v38, v39 offset0:48 offset1:66\n\t" \
  "s_waitcnt lgkmcnt(0)\n\t" \
  "s_barrier\n\t" \
  "s_setprio 0\n\t" \
  "v_pk_fma_f32 v[128:129], v[96:97], s[52:53], v[128:129] op_sel:[0,0,0] op_sel_hi:[1,0,1]\n\t" \
  "v_pk_fma_f32 v[130:131], v[98:99], s[52:53], v[130:131] op_sel:[0,0,0] op_sel_hi:[1,0,1]\n\t" \
  "v_pk_fma_f32 v[132:133], v[100:101], s[52:53], v[132:133] op_sel:[0,0,0] op_sel_hi:[1,0,1]\n\t" \
  "v_pk_fma_f32 v[134:135], v[102:103], s[52:53], v[134:135] op_sel:[0,0,0] op_sel_hi:[1,0,1]\n\t" \
  "v_pk_fma_f32 v[136:137], v[104:105], s[52:53], v[136:137] op_sel:[0,1,0] op_sel_hi:[1,1,1]\n\t" \
  "v_pk_fma_f32 v[138:139], v[106:107], s[52:53], v[138:139] op_sel:[0,1,0] op_sel_hi:[1,1,1]\n\t" \
  "v_pk_fma_f32 v[140:141], v[108:109], s[52:53], v[140:141] op_sel:[0,1,0] op_sel_hi:[1,1,1]\n\t" \
  "v_pk_fma_f32 v[142:143], v[110:111], s[52:53], v[142:143] op_sel:[0,1,0] op_sel_hi:[1,1,1]\n\t" \
  "v_pk_fma_f32 v[144:145], v[112:113], s[54:55], v[144:145] op_sel:[0,0,0] op_sel_hi:[1,0,1]\n\t" \
  "v_pk_fma_f32 v[146:147], v[114:115], s[54:55], v[146:147] op_sel:[0,0,0] op_sel_hi:[1,0,1]\n\t" \
  "v_pk_fma_f32 v[148:149], v[116:117], s[54:55], v[148:149] op_sel:[0,0,0] op_sel_hi:[1,0,1]\n\t" \
  "v_pk_fma_f32 v[150:151], v[118:119], s[54:55], v[150:151] op_sel:[0,0,0] op_sel_hi:[1,0,1]\n\t" \
  "v_pk_fma_f32 v[152:153], v[120:121], s[54:55], v[152:153] op_sel:[0,1,0] op_sel_hi:[1,1,1]\n\t" \
  "v_pk_fma_f32 v[154:155], v[122:123], s[54:55], v[154:155] op_sel:[0,1,0] op_sel_hi:[1,1,1]\n\t" \
  "v_pk_fma_f32 v[156:157], v[124:125], s[54:55], v[156:157] op_sel:[0,1,0] op_sel_hi:[1,1,1]\n\t" \
  "v_pk_fma_f32 v[158:159], v[126:127], s[54:55], v[158:159] op_sel:[0,1,0] op_sel_hi:[1,1,1]\n\t" \
  "v_lshlrev_b32 v11, 2, v3\n\t" \
  "v_add_u32 v11, 66560, v11\n\t" \
  "v_lshrrev_b32 v5, 3, v3\n\t" \
  "v_mul_u32_u24 v5, 18432, v5\n\t" \
  "v_and_b32 v4, 7, v0\n\t" \
  "v_lshl_add_u32 v12, v4, 4, v5\n\t" \
  "v_add_u32 v13, 589824, v12\n\t" \
  "v_add_u32 v14, 1179648, v12\n\t" \
  "v_add_u32 v15, 1769472, v12\n\t" \
  "s_lshl_b32 s57, s46, 7\n\t" \
  "s_add_u32 s57, s57, 1048576\n\t" \
  "s_add_u32 s60, s40, s57\n\t" \
  "s_addc_u32 s61, s41, 0\n\t" \
  "v_mov_b32 v4, 154448\n\t" \
  "ds_read_b128 v[16:19], v4\n\t" \
  "s_or_b32 s57, s42, s47\n\t" \
  "s_cmp_lg_u32 s57, 0\n\t" \
  "s_cbranch_scc1 JOIN_%=\n\t" \
  "s_mov_b32 s58, 60000\n\t" \
  "v_and_b32 v4, 15, v0\n\t" \
  "v_lshlrev_b32 v4, 2, v4\n\t" \
  "ACKS_%=:\n\t" \
  "global_load_dword v5, v4, s[40:41] offset:1024 sc1\n\t" \
  "s_sub_u32 s58, s58, 1\n\t" \
  "s_waitcnt vmcnt(0)\n\t" \
  "v_cmp_ne_u32 vcc, s49, v5\n\t" \
  "s_cmp_eq_u32 s58, 0\n\t" \
  "s_cbranch_scc1 ACKD_%=\n\t" \
  "s_cbranch_vccnz ACKS_%=\n\t" \
  "ACKD_%=:\n\t" \
  "v_mov_b32 v4, 0\n\t" \
  "v_mov_b32 v5, s49\n\t" \
  "global_store_dword v4, v5, s[40:41]\n\t" \
  "JOIN_%=:\n\t" \
  "s_waitcnt lgkmcnt(0)\n\t" \
  "s_barrier\n\t" \
  "ds_read2st64_b32 v[32:33], v11 offset0:0 offset1:16\n\t" \
  "ds_read2st64_b32 v[34:35], v11 offset0:32 offset1:48\n\t" \
  "ds_read2st64_b32 v[36:37], v11 offset0:4 offset1:20\n\t" \
  "ds_read2st64_b32 v[38:39], v11 offset0:36 offset1:52\n\t" \
  "ds_read2st64_b32 v[40:41], v11 offset0:8 offset1:24\n\t" \
  "ds_read2st64_b32 v[42:43], v11 offset0:40 offset1:56\n\t" \
  "ds_read2st64_b32 v[44:45], v11 offset0:12 offset1:28\n\t" \
  "ds_read2st64_b32 v[46:47], v11 offset0:44 offset1:60\n\t" \
  "s_waitcnt lgkmcnt(6)\n\t" \
  "v_lshlrev_b32 v48, 16, v32\n\t" \
  "v_and_b32 v49, 0xffff0000, v32\n\t" \
  "v_lshlrev_b32 v50, 16, v33\n\t" \
  "v_and_b32 v51, 0xffff0000, v33\n\t" \
  "v_lshlrev_b32 v52, 16, v34\n\t" \
  "v_and_b32 v53, 0xffff0000, v34\n\t" \
  "v_lshlrev_b32 v54, 16, v35\n\t" \
  "v_and_b32 v55, 0xffff0000, v35\n\t" \
  "v_pk_fma_f32 v[128:129], v[48:49], v[16:17], v[128:129] op_sel:[0,0,0] op_sel_hi:[1,0,1]\n\t" \
  "v_pk_fma_f32 v[136:137], v[50:51], v[16:17], v[136:137] op_sel:[0,1,0] op_sel_hi:[1,1,1]\n\t" \
  "v_pk_fma_f32 v[144:145], v[52:53], v[18:19], v[144:145] op_sel:[0,0,0] op_sel_hi:[1,0,1]\n\t" \
  "v_pk_fma_f32 v[152:153], v[54:55], v[18:19], v[152:153] op_sel:[0,1,0] op_sel_hi:[1,1,1]\n\t" \
  "v_cvt_pk_bf16_f32 v64, v128, v136\n\t" \
  "v_cvt_pk_bf16_f32 v65, v144, v152\n\t" \
  "v_cvt_pk_bf16_f32 v66, v129, v137\n\t" \
  "v_cvt_pk_bf16_f32 v67, v145, v153\n\t" \
  "global_store_dwordx4 v12, v[64:67], s[60:61]\n\t" \
  "s_waitcnt lgkmcnt(4)\n\t" \
  "v_lshlrev_b32 v48, 16, v36\n\t" \
  "v_and_b32 v49, 0xffff0000, v36\n\t" \
  "v_lshlrev_b32 v50, 16, v37\n\t" \
  "v_and_b32 v51, 0xffff0000, v37\n\t" \
  "v_lshlrev_b32 v52, 16, v38\n\t" \
  "v_and_b32 v53, 0xffff0000, v38\n\t" \
  "v_lshlrev_b32 v54, 16, v39\n\t" \
  "v_and_b32 v55, 0xffff0000, v39\n\t" \
  "v_pk_fma_f32 v[130:131], v[48:49], v[16:17], v[130:131] op_sel:[0,0,0] op_sel_hi:[1,0,1]\n\t" \
  "v_pk_fma_f32 v[138:139], v[50:51], v[16:17], v[138:139] op_sel:[0,1,0] op_sel_hi:[1,1,1]\n\t" \
  "v_pk_fma_f32 v[146:147], v[52:53], v[18:19], v[146:147] op_sel:[0,0,0] op_sel_hi:[1,0,1]\n\t" \
  "v_pk_fma_f32 v[154:155], v[54:55], v[18:19], v[154:155] op_sel:[0,1,0] op_sel_hi:[1,1,1]\n\t" \
  "v_cvt_pk_bf16_f32 v68, v130, v138\n\t" \
  "v_cvt_pk_bf16_f32 v69, v146, v154\n\t" \
  "v_cvt_pk_bf16_f32 v70, v131, v139\n\t" \
  "v_cvt_pk_bf16_f32 v71, v147, v155\n\t" \
  "global_store_dwordx4 v13, v[68:71], s[60:61]\n\t" \
  "s_waitcnt lgkmcnt(2)\n\t" \
  "v_lshlrev_b32 v48, 16, v40\n\t" \
  "v_and_b32 v49, 0xffff0000, v40\n\t" \
  "v_lshlrev_b32 v50, 16, v41\n\t" \
  "v_and_b32 v51, 0xffff0000, v41\n\t" \
  "v_lshlrev_b32 v52, 16, v42\n\t" \
  "v_and_b32 v53, 0xffff0000, v42\n\t" \
  "v_lshlrev_b32 v54, 16, v43\n\t" \
  "v_and_b32 v55, 0xffff0000, v43\n\t" \
  "v_pk_fma_f32 v[132:133], v[48:49], v[16:17], v[132:133] op_sel:[0,0,0] op_sel_hi:[1,0,1]\n\t" \
  "v_pk_fma_f32 v[140:141], v[50:51], v[16:17], v[140:141] op_sel:[0,1,0] op_sel_hi:[1,1,1]\n\t" \
  "v_pk_fma_f32 v[148:149], v[52:53], v[18:19], v[148:149] op_sel:[0,0,0] op_sel_hi:[1,0,1]\n\t" \
  "v_pk_fma_f32 v[156:157], v[54:55], v[18:19], v[156:157] op_sel:[0,1,0] op_sel_hi:[1,1,1]\n\t" \
  "v_cvt_pk_bf16_f32 v72, v132, v140\n\t" \
  "v_cvt_pk_bf16_f32 v73, v148, v156\n\t" \
  "v_cvt_pk_bf16_f32 v74, v133, v141\n\t" \
  "v_cvt_pk_bf16_f32 v75, v149, v157\n\t" \
  "global_store_dwordx4 v14, v[72:75], s[60:61]\n\t" \
  "s_waitcnt lgkmcnt(0)\n\t" \
  "v_lshlrev_b32 v48, 16, v44\n\t" \
  "v_and_b32 v49, 0xffff0000, v44\n\t" \
  "v_lshlrev_b32 v50, 16, v45\n\t" \
  "v_and_b32 v51, 0xffff0000, v45\n\t" \
  "v_lshlrev_b32 v52, 16, v46\n\t" \
  "v_and_b32 v53, 0xffff0000, v46\n\t" \
  "v_lshlrev_b32 v54, 16, v47\n\t" \
  "v_and_b32 v55, 0xffff0000, v47\n\t" \
  "v_pk_fma_f32 v[134:135], v[48:49], v[16:17], v[134:135] op_sel:[0,0,0] op_sel_hi:[1,0,1]\n\t" \
  "v_pk_fma_f32 v[142:143], v[50:51], v[16:17], v[142:143] op_sel:[0,1,0] op_sel_hi:[1,1,1]\n\t" \
  "v_pk_fma_f32 v[150:151], v[52:53], v[18:19], v[150:151] op_sel:[0,0,0] op_sel_hi:[1,0,1]\n\t" \
  "v_pk_fma_f32 v[158:159], v[54:55], v[18:19], v[158:159] op_sel:[0,1,0] op_sel_hi:[1,1,1]\n\t" \
  "v_cvt_pk_bf16_f32 v76, v134, v142\n\t" \
  "v_cvt_pk_bf16_f32 v77, v150, v158\n\t" \
  "v_cvt_pk_bf16_f32 v78, v135, v143\n\t" \
  "v_cvt_pk_bf16_f32 v79, v151, v159\n\t" \
  "global_store_dwordx4 v15, v[76:79], s[60:61]\n\t" \
  "s_branch END_%=\n\t" \
  "IDLE_%=:\n\t" \
  "s_sub_u32 s56, s45, 18\n\t" \
  "s_lshl_b32 s56, s56, 3\n\t" \
  "s_add_u32 s56, s56, s43\n\t" \
  "s_cmp_lt_u32 s56, 90\n\t" \
  "s_cbranch_scc0 MISC_%=\n\t" \
  "s_lshl_b32 s57, s56, 6\n\t" \
  "v_add_u32 v4, s57, v0\n\t" \
  "v_mul_u32_u24 v5, 6554, v4\n\t" \
  "v_lshrrev_b32 v5, 16, v5\n\t" \
  "v_mul_u32_u24 v6, 13108, v4\n\t" \
  "v_lshrrev_b32 v6, 16, v6\n\t" \
  "v_lshlrev_b32 v4, 7, v4\n\t" \
  "s_lshr_b32 s57, s44, 1\n\t" \
  "s_and_b32 s58, s44, 1\n\t" \
  "s_mul_i32 s59, s57, 2304\n\t" \
  "s_lshl_b32 s60, s58, 10\n\t" \
  "s_add_u32 s59, s59, s60\n\t" \
  "v_mul_u32_u24 v5, 7936, v5\n\t" \
  "v_add3_u32 v5, v5, v4, s59\n\t" \
  "s_mul_i32 s59, s57, 1327104\n\t" \
  "s_lshl_b32 s60, s58, 9\n\t" \
  "s_add_u32 s59, s59, s60\n\t" \
  "v_lshlrev_b32 v6, 9, v6\n\t" \
  "v_add3_u32 v6, v6, v4, s59\n\t" \
  "s_waitcnt lgkmcnt(0)\n\t" \
  "s_sleep 12\n\t" \
  "global_load_dword v7, v5, s[30:31] nt\n\t" \
  "global_load_dword v8, v5, s[36:37] nt\n\t" \
  "global_load_dword v9, v6, s[38:39]\n\t" \
  "s_waitcnt vmcnt(1)\n\t" \
  "global_load_dword v7, v5, s[30:31]\n\t" \
  "global_load_dword v8, v5, s[36:37]\n\t" \
  "s_branch END_%=\n\t" \
  "MISC_%=:\n\t" \
  "s_sub_u32 s56, s56, 90\n\t" \
  "s_cmp_lt_u32 s56, 10\n\t" \
  "s_cbranch_scc0 CLR_%=\n\t" \
  "s_getpc_b64 s[60:61]\n\t" \
  "SELF_%=:\n\t" \
  "s_cmp_lt_u32 s56, 2\n\t" \
  "s_cbranch_scc0 NEXT_%=\n\t" \
  "s_lshl_b32 s57, s56, 13\n\t" \
  "v_lshl_add_u32 v5, v0, 7, s57\n\t" \
  "v_min_u32 v5, SELF_%=-ssm_code_begin, v5\n\t" \
  "s_sub_u32 s60, s60, SELF_%=-ssm_code_begin\n\t" \
  "s_subb_u32 s61, s61, 0\n\t" \
  "global_load_dword v7, v5, s[60:61]\n\t" \
  "s_branch END_%=\n\t" \
  "NEXT_%=:\n\t" \
  "s_getpc_b64 s[60:61]\n\t" \
  "s_add_u32 s60, s60, ffn_code_begin@rel32@lo+4\n\t" \
  "s_addc_u32 s61, s61, ffn_code_begin@rel32@hi+12\n\t" \
  "s_sub_u32 s56, s56, 2\n\t" \
  "s_lshl_b32 s57, s56, 13\n\t" \
  "v_lshl_add_u32 v5, v0, 7, s57\n\t" \
  "v_min_u32 v5, ffn_code_end-ffn_code_begin-4, v5\n\t" \
  "global_load_dword v7, v5, s[60:61]\n\t" \
  "s_branch END_%=\n\t" \
  "CLR_%=:\n\t" \
  "s_cmp_lt_u32 s56, 18\n\t" \
  "s_cbranch_scc0 END_%=\n\t" \
  "s_sub_u32 s56, s56, 10\n\t" \
  "s_lshl_b32 s57, s44, 3\n\t" \
  "s_add_u32 s56, s56, s57\n\t" \
  "s_lshl_b32 s56, s56, 10\n\t" \
  "s_add_u32 s56, s56, 65536\n\t" \
  "v_lshl_add_u32 v5, v0, 4, s56\n\t" \
  "v_mov_b32 v8, 0\n\t" \
  "v_mov_b32 v9, 0\n\t" \
  "v_mov_b32 v10, 0\n\t" \
  "v_mov_b32 v11, 0\n\t" \
  "s_waitcnt lgkmcnt(0)\n\t" \
  "global_store_dwordx4 v5, v[8:11], s[40:41]\n\t" \
  "END_%=:\n\t" \
  "s_waitcnt vmcnt(0) lgkmcnt(0)\n\t"

#define SSM_CLOB "v0", "v1", "v2", "v3", "v4", "v5", "v6", "v7", "v8", "v9", "v10", "v11", "v12", "v13", "v14", "v15", "v16", "v17", "v18", "v19", "v20", "v21", "v22", "v23", "v24", "v25", "v26", "v27", "v28", "v29", "v30", "v31", "v32", "v33", "v34", "v35", "v36", "v37", "v38", "v39", "v40", "v41", "v42", "v43", "v44", "v45", "v46", "v47", "v48", "v49", "v50", "v51", "v52", "v53", "v54", "v55", "v56", "v57", "v60", "v61", "v62", "v63", "v64", "v65", "v66", "v67", "v68", "v69", "v70", "v71", "v72", "v73", "v74", "v75", "v76", "v77", "v78", "v79", "v80", "v81", "v82", "v85", "v86", "v87", "v88", "v92", "v96", "v97", "v98", "v99", "v100", "v101", "v102", "v103", "v104", "v105", "v106", "v107", "v108", "v109", "v110", "v111", "v112", "v113", "v114", "v115", "v116", "v117", "v118", "v119", "v120", "v121", "v122", "v123", "v124", "v125", "v126", "v127", "v128", "v129", "v130", "v131", "v132", "v133", "v134", "v135", "v136", "v137", "v138", "v139", "v140", "v141", "v142", "v143", "v144", "v145", "v146", "v147", "v148", "v149", "v150", "v151", "v152", "v153", "v154", "v155", "v156", "v157", "v158", "v159", "v160", "v161", "v162", "v163", "v164", "v165", "v166", "v167", "v168", "v169", "v170", "v171", "v172", "v173", "v174", "v175", "v176", "v177", "v178", "v179", "v180", "v181", "v182", "v183", "v184", "v185", "v186", "v187", "v188", "v189", "v190", "v191", "v192", "v193", "v194", "v195", "v196", "v197", "v198", "v199", "v200", "v201", "v202", "v203", "v204", "v205", "v206", "v207", "v208", "v209", "v210", "v211", "v212", "v213", "v214", "v215", "v216", "v217", "v218", "v219", "v220", "v221", "v222", "v223", "v224", "v225", "v226", "v227", "v228", "v229", "v230", "v231", "v232", "v233", "v234", "v235", "v236", "v237", "v238", "v239", "v240", "v241", "v242", "v243", "v244", "v245", "v246", "v247", "v248", "v249", "v250", "v251", "v252", "v253", "v254", "v255", "s16", "s17", "s18", "s19", "s20", "s21", "s22", "s23", "s24", "s25", "s26", "s27", "s28", "s29", "s30", "s31", "s36", "s37", "s38", "s39", "s40", "s41", "s42", "s43", "s44", "s45", "s46", "s47", "s48", "s49", "s50", "s51", "s52", "s53", "s54", "s55", "s56", "s57", "s58", "s59", "s60", "s61", "s62", "s63", "s64", "s65"
#define SSM_LDS 154512

#define FFN_ASM \
  "ffn_code_begin:\n\t" \
  "s_load_dwordx2 s[94:95], %0, 0x0\n\t" \
  "s_load_dwordx4 s[48:51], %0, 0x8\n\t" \
  "s_load_dwordx4 s[64:67], %0, 0x18\n\t" \
  "v_mbcnt_lo_u32_b32 v13, -1, 0\n\t" \
  "v_mbcnt_hi_u32_b32 v13, -1, v13\n\t" \
  "v_lshlrev_b32 v0, 4, v13\n\t" \
  "v_lshrrev_b32 v14, 4, v13\n\t" \
  "v_and_b32 v15, 15, v13\n\t" \
  "v_lshlrev_b32 v7, 1, v14\n\t" \
  "v_xor_b32 v7, v7, v15\n\t" \
  "v_mul_u32_u24 v68, 9216, v14\n\t" \
  "v_lshl_add_u32 v68, v7, 4, v68\n\t" \
  "v_xor_b32 v69, 128, v68\n\t" \
  "s_and_b32 s25, %1, 3\n\t" \
  "s_bfe_u32 s26, %1, 0x20002\n\t" \
  "s_bfe_u32 s27, %1, 0x40004\n\t" \
  "s_bfe_u32 s20, %1, 0x80008\n\t" \
  "s_bfe_u32 s73, %1, 0x80010\n\t" \
  "s_lshr_b32 s28, %1, 24\n\t" \
  "s_add_u32 s83, s20, s28\n\t" \
  "s_mul_i32 s35, s26, 9\n\t" \
  "s_add_u32 s35, s35, s27\n\t" \
  "s_lshl_b32 s35, s35, 8\n\t" \
  "s_mul_i32 s29, s25, 73728\n\t" \
  "s_add_u32 s29, s29, s35\n\t" \
  "s_lshl_b32 s75, s25, 11\n\t" \
  "s_lshl_b32 s97, s25, 10\n\t" \
  "s_add_u32 s96, s83, s25\n\t" \
  "s_mul_i32 s96, s96, 18432\n\t" \
  "s_waitcnt lgkmcnt(0)\n\t" \
  "s_mov_b64 s[70:71], s[66:67]\n\t" \
  "s_mov_b32 s52, s50\n\t" \
  "s_mov_b32 s53, s51\n\t" \
  "s_and_b32 s53, s53, 0xffff\n\t" \
  "s_mov_b32 s54, 5308416\n\t" \
  "s_mov_b32 s55, 0x20000\n\t" \
  "s_mov_b32 s48, s48\n\t" \
  "s_mov_b32 s49, s49\n\t" \
  "s_and_b32 s49, s49, 0xffff\n\t" \
  "s_mov_b32 s50, 5308416\n\t" \
  "s_mov_b32 s51, 0x20000\n\t" \
  "s_add_u32 s36, s94, 1048576\n\t" \
  "s_addc_u32 s37, s95, 0\n\t" \
  "s_and_b32 s37, s37, 0xffff\n\t" \
  "s_mov_b32 s38, 2359296\n\t" \
  "s_mov_b32 s39, 0x20000\n\t" \
  "s_load_dword s74, s[94:95], 0x0\n\t" \
  "s_mov_b64 s[86:87], %2\n\t" \
  "s_mov_b32 s42, %3\n\t" \
  "s_add_u32 m0, s75, 18432\n\t" \
  "s_add_u32 s33, s29, 0\n\t" \
  "buffer_load_dwordx4 v68, s[48:51], s33 offen lds\n\t" \
  "s_add_u32 m0, s75, 26624\n\t" \
  "s_nop 0\n\t" \
  "buffer_load_dwordx4 v68, s[52:55], s33 offen lds\n\t" \
  "s_add_u32 m0, s75, 19456\n\t" \
  "s_add_u32 s33, s29, 36864\n\t" \
  "buffer_load_dwordx4 v69, s[48:51], s33 offen lds\n\t" \
  "s_add_u32 m0, s75, 27648\n\t" \
  "s_nop 0\n\t" \
  "buffer_load_dwordx4 v69, s[52:55], s33 offen lds\n\t" \
  "s_add_u32 m0, s97, 34816\n\t" \
  "s_add_u32 s33, s96, 0\n\t" \
  "buffer_load_dwordx4 v0, s[36:39], s33 offen lds\n\t" \
  "s_add_u32 s18, s83, s27\n\t" \
  "s_mul_i32 s34, s18, 18432\n\t" \
  "s_add_u32 s40, s36, s34\n\t" \
  "s_addc_u32 s41, s37, 0\n\t" \
  "s_mov_b32 s43, 0x20000\n\t" \
  "v_lshrrev_b32 v4, 1, v13\n\t" \
  "v_and_b32 v5, 1, v13\n\t" \
  "v_lshlrev_b32 v4, 7, v4\n\t" \
  "v_lshl_add_u32 v4, v5, 4, v4\n\t" \
  "s_lshl_b32 s34, s25, 5\n\t" \
  "v_add_u32 v4, s34, v4\n\t" \
  "buffer_load_dwordx4 v[116:119], v4, s[40:43], 0 offen\n\t" \
  "v_add_u32 v4, 4096, v4\n\t" \
  "buffer_load_dwordx4 v[120:123], v4, s[40:43], 0 offen\n\t" \
  "v_add_u32 v4, 4096, v4\n\t" \
  "buffer_load_dwordx4 v[124:127], v4, s[40:43], 0 offen\n\t" \
  "v_add_u32 v4, 4096, v4\n\t" \
  "buffer_load_dwordx4 v[128:131], v4, s[40:43], 0 offen\n\t" \
  "v_add_u32 v4, 4096, v4\n\t" \
  "buffer_load_dwordx4 v[132:135], v4, s[40:43], 0 offen\n\t" \
  "s_lshl_b32 s18, s18, 7\n\t" \
  "s_add_u32 s16, s18, s34\n\t" \
  "s_min_u32 s21, s42, 16384\n\t" \
  "v_lshrrev_b32 v4, 2, v15\n\t" \
  "v_and_b32 v5, 3, v15\n\t" \
  "v_lshl_add_u32 v6, v14, 2, v4\n\t" \
  "v_and_b32 v7, 7, v6\n\t" \
  "s_lshl_b32 s35, s25, 1\n\t" \
  "v_xor_b32 v7, s35, v7\n\t" \
  "v_lshlrev_b32 v7, 5, v7\n\t" \
  "v_lshl_add_u32 v70, v6, 8, v7\n\t" \
  "v_lshl_add_u32 v70, v5, 3, v70\n\t" \
  "v_lshlrev_b32 v1, 3, v13\n\t" \
  "v_mov_b32 v16, 0x3f803f80\n\t" \
  "v_mov_b32 v17, 0x3f803f80\n\t" \
  "v_mov_b32 v18, 0x3f803f80\n\t" \
  "v_mov_b32 v19, 0x3f803f80\n\t" \
  "s_add_u32 m0, s75, 120832\n\t" \
  "s_add_u32 s33, s29, 294912\n\t" \
  "buffer_load_dwordx4 v68, s[48:51], s33 offen lds\n\t" \
  "s_mov_b32 s64, s64\n\t" \
  "s_mov_b32 s65, s65\n\t" \
  "s_and_b32 s65, s65, 0xffff\n\t" \
  "s_mov_b32 s66, 5308416\n\t" \
  "s_mov_b32 s67, 0x20000\n\t" \
  "s_add_u32 s44, s94, 4194304\n\t" \
  "s_addc_u32 s45, s95, 0\n\t" \
  "s_and_b32 s45, s45, 0xffff\n\t" \
  "s_mov_b32 s46, 18874368\n\t" \
  "s_mov_b32 s47, 0x20000\n\t" \
  "s_add_u32 s56, s94, 65536\n\t" \
  "s_addc_u32 s57, s95, 0\n\t" \
  "s_and_b32 s57, s57, 0xffff\n\t" \
  "s_add_u32 m0, s75, 129024\n\t" \
  "s_nop 0\n\t" \
  "buffer_load_dwordx4 v68, s[52:55], s33 offen lds\n\t" \
  "s_mov_b32 s58, 0\n\t" \
  "s_mov_b32 s59, 0x20000\n\t" \
  "s_add_u32 s60, s94, 65536\n\t" \
  "s_addc_u32 s61, s95, 0\n\t" \
  "s_and_b32 s61, s61, 0xffff\n\t" \
  "s_mov_b32 s62, 65536\n\t" \
  "s_mov_b32 s63, 0x20000\n\t" \
  "s_cmp_eq_u32 s25, 0\n\t" \
  "s_cselect_b32 s58, 65536, 0\n\t" \
  "s_add_u32 s17, s20, s73\n\t" \
  "s_lshr_b32 s98, s25, 1\n\t" \
  "s_and_b32 s99, s25, 1\n\t" \
  "s_lshl_b32 s99, s99, 1\n\t" \
  "s_add_u32 m0, s75, 121856\n\t" \
  "s_add_u32 s33, s29, 331776\n\t" \
  "buffer_load_dwordx4 v69, s[48:51], s33 offen lds\n\t" \
  "s_add_u32 s99, s99, s98\n\t" \
  "s_mul_i32 s24, s98, 18432\n\t" \
  "s_lshl_b32 s34, s27, 2\n\t" \
  "s_add_u32 s35, s34, s99\n\t" \
  "s_lshl_b32 s93, s35, 10\n\t" \
  "s_sub_u32 s31, s93, 4718592\n\t" \
  "s_mul_i32 s30, s25, 9216\n\t" \
  "s_lshl_b32 s88, s25, 12\n\t" \
  "s_and_b32 s89, s25, 1\n\t" \
  "s_add_u32 s89, s89, 16\n\t" \
  "s_lshl_b32 s89, s89, 10\n\t" \
  "s_add_u32 m0, s75, 130048\n\t" \
  "s_nop 0\n\t" \
  "buffer_load_dwordx4 v69, s[52:55], s33 offen lds\n\t" \
  "s_mul_i32 s35, s26, 36864\n\t" \
  "s_add_u32 s90, s35, s30\n\t" \
  "s_add_u32 s34, s34, s25\n\t" \
  "s_lshl_b32 s34, s34, 10\n\t" \
  "s_add_u32 s91, s35, s34\n\t" \
  "s_lshl_b32 s92, s26, 7\n\t" \
  "s_mul_i32 s22, s26, 1327104\n\t" \
  "s_add_u32 s22, s22, s30\n\t" \
  "s_lshl_b32 s35, s27, 7\n\t" \
  "s_add_u32 s22, s22, s35\n\t" \
  "s_lshl_b32 s23, s25, 10\n\t" \
  "s_add_u32 m0, s97, 137216\n\t" \
  "s_add_u32 s33, s96, 1024\n\t" \
  "buffer_load_dwordx4 v0, s[36:39], s33 offen lds\n\t" \
  "v_bfe_u32 v7, v6, 1, 2\n\t" \
  "v_xor_b32 v7, s99, v7\n\t" \
  "v_lshlrev_b32 v7, 5, v7\n\t" \
  "v_lshl_add_u32 v72, v6, 7, v7\n\t" \
  "v_lshl_add_u32 v72, v5, 3, v72\n\t" \
  "s_lshl_b32 s35, s98, 12\n\t" \
  "v_add_u32 v72, s35, v72\n\t" \
  "v_lshrrev_b32 v6, 3, v13\n\t" \
  "v_and_b32 v7, 7, v13\n\t" \
  "v_lshlrev_b32 v4, 1, v14\n\t" \
  "v_xor_b32 v7, v7, v4\n\t" \
  "s_add_u32 m0, s75, 100352\n\t" \
  "s_add_u32 s33, s29, 589824\n\t" \
  "buffer_load_dwordx4 v68, s[48:51], s33 offen lds\n\t" \
  "v_mul_u32_u24 v71, 1152, v6\n\t" \
  "v_lshl_add_u32 v71, v7, 4, v71\n\t" \
  "s_lshl_b32 s35, s25, 2\n\t" \
  "v_add_u32 v4, s35, v14\n\t" \
  "v_add_u32 v5, 16, v4\n\t" \
  "s_sub_u32 s35, s73, 1\n\t" \
  "v_min_u32 v5, s35, v5\n\t" \
  "v_add_u32 v4, s28, v4\n\t" \
  "v_subrev_u32 v6, s73, v4\n\t" \
  "v_min_u32 v4, v6, v4\n\t" \
  "v_add_u32 v4, s20, v4\n\t" \
  "s_add_u32 m0, s75, 108544\n\t" \
  "s_nop 0\n\t" \
  "buffer_load_dwordx4 v68, s[52:55], s33 offen lds\n\t" \
  "v_lshl_add_u32 v4, v4, 4, v15\n\t" \
  "v_lshlrev_b32 v4, 3, v4\n\t" \
  "v_add_u32 v5, s28, v5\n\t" \
  "v_subrev_u32 v6, s73, v5\n\t" \
  "v_min_u32 v5, v6, v5\n\t" \
  "v_add_u32 v5, s20, v5\n\t" \
  "v_lshl_add_u32 v5, v5, 4, v15\n\t" \
  "v_lshlrev_b32 v5, 3, v5\n\t" \
  "s_lshl_b32 s35, s25, 9\n\t" \
  "s_add_u32 s35, s35, 156672\n\t" \
  "v_add_u32 v6, s35, v1\n\t" \
  "s_add_u32 m0, s75, 101376\n\t" \
  "s_add_u32 s33, s29, 626688\n\t" \
  "buffer_load_dwordx4 v69, s[48:51], s33 offen lds\n\t" \
  "v_lshlrev_b32 v76, 3, v15\n\t" \
  "v_add_u32 v76, 156672, v76\n\t" \
  "s_lshl_b32 s35, s25, 8\n\t" \
  "s_add_u32 s35, s35, 155648\n\t" \
  "v_lshl_add_u32 v77, v14, 4, s35\n\t" \
  "s_lshl_b32 s35, s25, 10\n\t" \
  "s_add_u32 s35, s35, 147456\n\t" \
  "v_add_u32 v79, s35, v0\n\t" \
  "s_add_u32 s82, s83, 1\n\t" \
  "s_cmp_eq_u32 s82, s17\n\t" \
  "s_cselect_b32 s82, s20, s82\n\t" \
  "s_add_u32 s81, s82, 1\n\t" \
  "s_add_u32 m0, s75, 109568\n\t" \
  "s_nop 0\n\t" \
  "buffer_load_dwordx4 v69, s[52:55], s33 offen lds\n\t" \
  "s_cmp_eq_u32 s81, s17\n\t" \
  "s_cselect_b32 s81, s20, s81\n\t" \
  "s_add_u32 s80, s81, 1\n\t" \
  "s_cmp_eq_u32 s80, s17\n\t" \
  "s_cselect_b32 s80, s20, s80\n\t" \
  "s_add_u32 s84, s80, 1\n\t" \
  "s_cmp_eq_u32 s84, s17\n\t" \
  "s_cselect_b32 s84, s20, s84\n\t" \
  "s_add_u32 s85, s84, 1\n\t" \
  "s_cmp_eq_u32 s85, s17\n\t" \
  "s_cselect_b32 s85, s20, s85\n\t" \
  "s_add_u32 s72, s85, 1\n\t" \
  "s_cmp_eq_u32 s72, s17\n\t" \
  "s_cselect_b32 s72, s20, s72\n\t" \
  "s_add_u32 s79, s72, 1\n\t" \
  "s_cmp_eq_u32 s79, s17\n\t" \
  "s_cselect_b32 s79, s20, s79\n\t" \
  "s_add_u32 m0, s97, 116736\n\t" \
  "s_add_u32 s33, s96, 2048\n\t" \
  "buffer_load_dwordx4 v0, s[36:39], s33 offen lds\n\t" \
  "s_add_u32 s78, s79, 1\n\t" \
  "s_cmp_eq_u32 s78, s17\n\t" \
  "s_cselect_b32 s78, s20, s78\n\t" \
  "s_add_u32 s77, s78, 1\n\t" \
  "s_cmp_eq_u32 s77, s17\n\t" \
  "s_cselect_b32 s77, s20, s77\n\t" \
  "s_add_u32 s76, s77, 1\n\t" \
  "s_cmp_eq_u32 s76, s17\n\t" \
  "s_cselect_b32 s76, s20, s76\n\t" \
  "s_add_u32 s68, s94, 20480\n\t" \
  "s_addc_u32 s69, s95, 0\n\t" \
  "s_and_b32 s69, s69, 0xffff\n\t" \
  "s_mov_b64 s[26:27], s[70:71]\n\t" \
  "s_mov_b32 s70, 16384\n\t" \
  "s_mov_b32 s71, 0x20000\n\t" \
  "s_add_u32 m0, s75, 79872\n\t" \
  "s_add_u32 s33, s29, 884736\n\t" \
  "buffer_load_dwordx4 v68, s[48:51], s33 offen lds\n\t" \
  "v_mov_b32 v136, 0\n\t" \
  "v_mov_b32 v137, 0\n\t" \
  "v_mov_b32 v138, 0\n\t" \
  "v_mov_b32 v139, 0\n\t" \
  "s_waitcnt vmcnt(10)\n\t" \
  "v_dot2_f32_bf16 v136, v116, v16, v136\n\t" \
  "v_dot2_f32_bf16 v138, v116, v116, v138\n\t" \
  "v_dot2_f32_bf16 v137, v118, v16, v137\n\t" \
  "v_dot2_f32_bf16 v139, v118, v118, v139\n\t" \
  "v_dot2_f32_bf16 v136, v117, v16, v136\n\t" \
  "v_dot2_f32_bf16 v138, v117, v117, v138\n\t" \
  "v_dot2_f32_bf16 v137, v119, v16, v137\n\t" \
  "v_dot2_f32_bf16 v139, v119, v119, v139\n\t" \
  "v_dot2_f32_bf16 v136, v120, v16, v136\n\t" \
  "v_dot2_f32_bf16 v138, v120, v120, v138\n\t" \
  "v_dot2_f32_bf16 v137, v122, v16, v137\n\t" \
  "v_dot2_f32_bf16 v139, v122, v122, v139\n\t" \
  "v_dot2_f32_bf16 v136, v121, v16, v136\n\t" \
  "v_dot2_f32_bf16 v138, v121, v121, v138\n\t" \
  "v_dot2_f32_bf16 v137, v123, v16, v137\n\t" \
  "v_dot2_f32_bf16 v139, v123, v123, v139\n\t" \
  "s_add_u32 m0, s75, 88064\n\t" \
  "s_nop 0\n\t" \
  "buffer_load_dwordx4 v68, s[52:55], s33 offen lds\n\t" \
  "v_dot2_f32_bf16 v136, v124, v16, v136\n\t" \
  "v_dot2_f32_bf16 v138, v124, v124, v138\n\t" \
  "v_dot2_f32_bf16 v137, v126, v16, v137\n\t" \
  "v_dot2_f32_bf16 v139, v126, v126, v139\n\t" \
  "v_dot2_f32_bf16 v136, v125, v16, v136\n\t" \
  "v_dot2_f32_bf16 v138, v125, v125, v138\n\t" \
  "v_dot2_f32_bf16 v137, v127, v16, v137\n\t" \
  "v_dot2_f32_bf16 v139, v127, v127, v139\n\t" \
  "v_dot2_f32_bf16 v136, v128, v16, v136\n\t" \
  "v_dot2_f32_bf16 v138, v128, v128, v138\n\t" \
  "v_dot2_f32_bf16 v137, v130, v16, v137\n\t" \
  "v_dot2_f32_bf16 v139, v130, v130, v139\n\t" \
  "v_dot2_f32_bf16 v136, v129, v16, v136\n\t" \
  "v_dot2_f32_bf16 v138, v129, v129, v138\n\t" \
  "v_dot2_f32_bf16 v137, v131, v16, v137\n\t" \
  "v_dot2_f32_bf16 v139, v131, v131, v139\n\t" \
  "v_dot2_f32_bf16 v136, v132, v16, v136\n\t" \
  "v_dot2_f32_bf16 v138, v132, v132, v138\n\t" \
  "v_dot2_f32_bf16 v137, v134, v16, v137\n\t" \
  "v_dot2_f32_bf16 v139, v134, v134, v139\n\t" \
  "v_dot2_f32_bf16 v136, v133, v16, v136\n\t" \
  "s_add_u32 m0, s75, 80896\n\t" \
  "s_add_u32 s33, s29, 921600\n\t" \
  "buffer_load_dwordx4 v69, s[48:51], s33 offen lds\n\t" \
  "v_dot2_f32_bf16 v138, v133, v133, v138\n\t" \
  "v_dot2_f32_bf16 v137, v135, v16, v137\n\t" \
  "v_dot2_f32_bf16 v139, v135, v135, v139\n\t" \
  "s_nop 2\n\t" \
  "v_add_f32_dpp v136, v136, v136 row_ror:2 row_mask:0xf bank_mask:0xf\n\t" \
  "v_add_f32_dpp v137, v137, v137 row_ror:2 row_mask:0xf bank_mask:0xf\n\t" \
  "v_add_f32_dpp v138, v138, v138 row_ror:2 row_mask:0xf bank_mask:0xf\n\t" \
  "v_add_f32_dpp v139, v139, v139 row_ror:2 row_mask:0xf bank_mask:0xf\n\t" \
  "v_add_f32_dpp v136, v136, v136 row_ror:4 row_mask:0xf bank_mask:0xf\n\t" \
  "v_add_f32_dpp v137, v137, v137 row_ror:4 row_mask:0xf bank_mask:0xf\n\t" \
  "v_add_f32_dpp v138, v138, v138 row_ror:4 row_mask:0xf bank_mask:0xf\n\t" \
  "v_add_f32_dpp v139, v139, v139 row_ror:4 row_mask:0xf bank_mask:0xf\n\t" \
  "v_add_f32_dpp v136, v136, v136 row_ror:8 row_mask:0xf bank_mask:0xf\n\t" \
  "v_add_f32_dpp v137, v137, v137 row_ror:8 row_mask:0xf bank_mask:0xf\n\t" \
  "v_add_f32_dpp v138, v138, v138 row_ror:8 row_mask:0xf bank_mask:0xf\n\t" \
  "v_add_f32_dpp v139, v139, v139 row_ror:8 row_mask:0xf bank_mask:0xf\n\t" \
  "v_mov_b32 v140, v136\n\t" \
  "v_mov_b32 v141, v137\n\t" \
  "v_mov_b32 v142, v138\n\t" \
  "v_mov_b32 v143, v139\n\t" \
  "v_permlane16_swap_b32 v140, v136\n\t" \
  "s_add_u32 m0, s75, 89088\n\t" \
  "s_nop 0\n\t" \
  "buffer_load_dwordx4 v69, s[52:55], s33 offen lds\n\t" \
  "v_permlane16_swap_b32 v141, v137\n\t" \
  "v_permlane16_swap_b32 v142, v138\n\t" \
  "v_permlane16_swap_b32 v143, v139\n\t" \
  "v_add_f32 v136, v140, v136\n\t" \
  "v_add_f32 v137, v141, v137\n\t" \
  "v_add_f32 v138, v142, v138\n\t" \
  "v_add_f32 v139, v143, v139\n\t" \
  "v_mov_b32 v140, v136\n\t" \
  "v_mov_b32 v141, v137\n\t" \
  "v_mov_b32 v142, v138\n\t" \
  "v_mov_b32 v143, v139\n\t" \
  "v_permlane32_swap_b32 v140, v136\n\t" \
  "v_permlane32_swap_b32 v141, v137\n\t" \
  "v_permlane32_swap_b32 v142, v138\n\t" \
  "v_permlane32_swap_b32 v143, v139\n\t" \
  "v_add_f32 v136, v140, v136\n\t" \
  "v_add_f32 v137, v141, v137\n\t" \
  "v_add_f32 v138, v142, v138\n\t" \
  "v_add_f32 v139, v143, v139\n\t" \
  "v_mul_f32 v136, 0x3ae38e39, v136\n\t" \
  "v_mul_f32 v137, 0x3ae38e39, v137\n\t" \
  "s_add_u32 m0, s97, 96256\n\t" \
  "s_add_u32 s33, s96, 3072\n\t" \
  "buffer_load_dwordx4 v0, s[36:39], s33 offen lds\n\t" \
  "v_mul_f32 v138, 0x3ae38e39, v138\n\t" \
  "v_mul_f32 v139, 0x3ae38e39, v139\n\t" \
  "v_fma_f32 v138, -v136, v136, v138\n\t" \
  "v_fma_f32 v139, -v137, v137, v139\n\t" \
  "v_max_f32 v138, 0, v138\n\t" \
  "v_max_f32 v139, 0, v139\n\t" \
  "v_cvt_pk_bf16_f32 v140, v138, v139\n\t" \
  "v_lshlrev_b32 v138, 16, v140\n\t" \
  "v_and_b32 v139, 0xffff0000, v140\n\t" \
  "v_add_f32 v138, 0x3727c5ac, v138\n\t" \
  "v_add_f32 v139, 0x3727c5ac, v139\n\t" \
  "v_cvt_pk_bf16_f32 v140, v138, v139\n\t" \
  "v_lshlrev_b32 v138, 16, v140\n\t" \
  "v_and_b32 v139, 0xffff0000, v140\n\t" \
  "v_sqrt_f32 v138, v138\n\t" \
  "v_sqrt_f32 v139, v139\n\t" \
  "s_waitcnt lgkmcnt(0)\n\t" \
  "v_mov_b32 v141, s74\n\t" \
  "v_mov_b32 v143, s74\n\t" \
  "v_cvt_pk_bf16_f32 v140, v136, v138\n\t" \
  "v_cvt_pk_bf16_f32 v142, v137, v139\n\t" \
  "s_mov_b32 s70, s21\n\t" \
  "s_mov_b64 exec, 3\n\t" \
  "buffer_store_dwordx4 v[140:143], v0, s[68:71], s16 offen sc1\n\t" \
  "s_mov_b64 exec, -1\n\t" \
  "s_mov_b32 s70, 16384\n\t" \
  "s_add_u32 s40, s94, 4194304\n\t" \
  "s_addc_u32 s41, s95, 0\n\t" \
  "s_and_b32 s41, s41, 0xffff\n\t" \
  "s_mov_b32 s42, 18874368\n\t" \
  "s_mov_b32 s43, 0x20000\n\t" \
  "s_waitcnt vmcnt(16)\n\t" \
  "s_barrier\n\t" \
  "v_add_u32 v73, 18432, v70\n\t" \
  "v_xor_b32 v74, 32, v73\n\t" \
  "v_add_u32 v75, 34816, v1\n\t" \
  "ds_read_b64_tr_b16 a[0:1], v73 offset:0\n\t" \
  "ds_read_b64_tr_b16 a[2:3], v73 offset:4096\n\t" \
  "ds_read_b64_tr_b16 a[4:5], v73 offset:8192\n\t" \
  "ds_read_b64_tr_b16 a[6:7], v73 offset:12288\n\t" \
  "ds_read_b64_tr_b16 a[8:9], v74 offset:0\n\t" \
  "ds_read_b64_tr_b16 a[10:11], v74 offset:4096\n\t" \
  "ds_read_b64_tr_b16 a[12:13], v74 offset:8192\n\t" \
  "ds_read_b64_tr_b16 a[14:15], v74 offset:12288\n\t" \
  "ds_read_b64 v[80:81], v75 offset:0\n\t" \
  "ds_read_b64 v[82:83], v75 offset:512\n\t" \
  "ds_read_b64 v[84:85], v75 offset:1024\n\t" \
  "ds_read_b64 v[86:87], v75 offset:1536\n\t" \
  "ds_read_b64 v[88:89], v75 offset:2048\n\t" \
  "ds_read_b64 v[90:91], v75 offset:2560\n\t" \
  "ds_read_b64 v[92:93], v75 offset:3072\n\t" \
  "ds_read_b64 v[94:95], v75 offset:3584\n\t" \
  "s_add_u32 m0, s75, 59392\n\t" \
  "s_add_u32 s33, s29, 1179648\n\t" \
  "buffer_load_dwordx4 v68, s[48:51], s33 offen lds\n\t" \
  "s_add_u32 m0, s75, 67584\n\t" \
  "s_nop 0\n\t" \
  "buffer_load_dwordx4 v68, s[52:55], s33 offen lds\n\t" \
  "s_add_u32 m0, s75, 60416\n\t" \
  "s_add_u32 s33, s29, 1216512\n\t" \
  "buffer_load_dwordx4 v69, s[48:51], s33 offen lds\n\t" \
  "s_add_u32 m0, s75, 68608\n\t" \
  "s_nop 0\n\t" \
  "buffer_load_dwordx4 v69, s[52:55], s33 offen lds\n\t" \
  "s_add_u32 m0, s97, 75776\n\t" \
  "s_add_u32 s33, s96, 4096\n\t" \
  "buffer_load_dwordx4 v0, s[36:39], s33 offen lds\n\t" \
  "s_waitcnt lgkmcnt(0)\n\t" \
  "s_waitcnt vmcnt(16)\n\t" \
  "s_barrier\n\t" \
  "v_add_u32 v73, 120832, v70\n\t" \
  "v_xor_b32 v74, 32, v73\n\t" \
  "v_mfma_f32_16x16x32_bf16 v[36:39], a[0:3], v[80:83], 0\n\t" \
  "s_add_u32 m0, s75, 38912\n\t" \
  "s_add_u32 s33, s29, 1474560\n\t" \
  "v_add_u32 v75, 137216, v1\n\t" \
  "v_mfma_f32_16x16x32_bf16 v[20:23], a[0:3], v[84:87], 0\n\t" \
  "buffer_load_dwordx4 v68, s[48:51], s33 offen lds\n\t" \
  "v_mfma_f32_16x16x32_bf16 v[192:195], a[0:3], v[88:91], 0\n\t" \
  "s_add_u32 m0, s75, 47104\n\t" \
  "s_nop 0\n\t" \
  "ds_read_b64_tr_b16 a[16:17], v73 offset:0\n\t" \
  "v_mfma_f32_16x16x32_bf16 v[208:211], a[0:3], v[92:95], 0\n\t" \
  "ds_read_b64_tr_b16 a[18:19], v73 offset:4096\n\t" \
  "ds_read_b64_tr_b16 a[20:21], v73 offset:8192\n\t" \
  "v_mfma_f32_16x16x32_bf16 v[52:55], a[0:3], v[16:19], 0\n\t" \
  "ds_read_b64_tr_b16 a[22:23], v73 offset:12288\n\t" \
  "ds_read_b64_tr_b16 a[24:25], v74 offset:0\n\t" \
  "v_mfma_f32_16x16x32_bf16 v[40:43], a[4:7], v[80:83], 0\n\t" \
  "buffer_load_dwordx4 v68, s[52:55], s33 offen lds\n\t" \
  "v_mfma_f32_16x16x32_bf16 v[24:27], a[4:7], v[84:87], 0\n\t" \
  "s_add_u32 m0, s75, 39936\n\t" \
  "s_add_u32 s33, s29, 1511424\n\t" \
  "ds_read_b64_tr_b16 a[26:27], v74 offset:4096\n\t" \
  "v_mfma_f32_16x16x32_bf16 v[196:199], a[4:7], v[88:91], 0\n\t" \
  "ds_read_b64_tr_b16 a[28:29], v74 offset:8192\n\t" \
  "ds_read_b64_tr_b16 a[30:31], v74 offset:12288\n\t" \
  "v_mfma_f32_16x16x32_bf16 v[212:215], a[4:7], v[92:95], 0\n\t" \
  "ds_read_b64 v[96:97], v75 offset:0\n\t" \
  "ds_read_b64 v[98:99], v75 offset:512\n\t" \
  "v_mfma_f32_16x16x32_bf16 v[56:59], a[4:7], v[16:19], 0\n\t" \
  "buffer_load_dwordx4 v69, s[48:51], s33 offen lds\n\t" \
  "v_mfma_f32_16x16x32_bf16 v[44:47], a[8:11], v[80:83], 0\n\t" \
  "s_add_u32 m0, s75, 48128\n\t" \
  "s_nop 0\n\t" \
  "ds_read_b64 v[100:101], v75 offset:1024\n\t" \
  "v_mfma_f32_16x16x32_bf16 v[28:31], a[8:11], v[84:87], 0\n\t" \
  "ds_read_b64 v[102:103], v75 offset:1536\n\t" \
  "ds_read_b64 v[104:105], v75 offset:2048\n\t" \
  "v_mfma_f32_16x16x32_bf16 v[200:203], a[8:11], v[88:91], 0\n\t" \
  "ds_read_b64 v[106:107], v75 offset:2560\n\t" \
  "ds_read_b64 v[108:109], v75 offset:3072\n\t" \
  "v_mfma_f32_16x16x32_bf16 v[216:219], a[8:11], v[92:95], 0\n\t" \
  "buffer_load_dwordx4 v69, s[52:55], s33 offen lds\n\t" \
  "v_mfma_f32_16x16x32_bf16 v[60:63], a[8:11], v[16:19], 0\n\t" \
  "s_add_u32 m0, s97, 55296\n\t" \
  "s_add_u32 s33, s96, 5120\n\t" \
  "ds_read_b64 v[110:111], v75 offset:3584\n\t" \
  "v_mfma_f32_16x16x32_bf16 v[48:51], a[12:15], v[80:83], 0\n\t" \
  "v_mfma_f32_16x16x32_bf16 v[32:35], a[12:15], v[84:87], 0\n\t" \
  "v_mfma_f32_16x16x32_bf16 v[204:207], a[12:15], v[88:91], 0\n\t" \
  "buffer_load_dwordx4 v0, s[36:39], s33 offen lds\n\t" \
  "v_mfma_f32_16x16x32_bf16 v[220:223], a[12:15], v[92:95], 0\n\t" \
  "v_mfma_f32_16x16x32_bf16 v[64:67], a[12:15], v[16:19], 0\n\t" \
  "s_waitcnt lgkmcnt(0)\n\t" \
  "s_waitcnt vmcnt(16)\n\t" \
  "s_barrier\n\t" \
  "v_add_u32 v73, 100352, v70\n\t" \
  "v_xor_b32 v74, 32, v73\n\t" \
  "v_mfma_f32_16x16x32_bf16 v[36:39], a[16:19], v[96:99], v[36:39]\n\t" \
  "s_add_u32 m0, s75, 18432\n\t" \
  "s_add_u32 s33, s29, 1769472\n\t" \
  "v_add_u32 v75, 116736, v1\n\t" \
  "v_mfma_f32_16x16x32_bf16 v[20:23], a[16:19], v[100:103], v[20:23]\n\t" \
  "buffer_load_dwordx4 v68, s[48:51], s33 offen lds\n\t" \
  "v_mfma_f32_16x16x32_bf16 v[192:195], a[16:19], v[104:107], v[192:195]\n\t" \
  "s_add_u32 m0, s75, 26624\n\t" \
  "s_nop 0\n\t" \
  "ds_read_b64_tr_b16 a[32:33], v73 offset:0\n\t" \
  "v_mfma_f32_16x16x32_bf16 v[208:211], a[16:19], v[108:111], v[208:211]\n\t" \
  "ds_read_b64_tr_b16 a[34:35], v73 offset:4096\n\t" \
  "ds_read_b64_tr_b16 a[36:37], v73 offset:8192\n\t" \
  "v_mfma_f32_16x16x32_bf16 v[52:55], a[16:19], v[16:19], v[52:55]\n\t" \
  "ds_read_b64_tr_b16 a[38:39], v73 offset:12288\n\t" \
  "ds_read_b64_tr_b16 a[40:41], v74 offset:0\n\t" \
  "v_mfma_f32_16x16x32_bf16 v[40:43], a[20:23], v[96:99], v[40:43]\n\t" \
  "buffer_load_dwordx4 v68, s[52:55], s33 offen lds\n\t" \
  "v_mfma_f32_16x16x32_bf16 v[24:27], a[20:23], v[100:103], v[24:27]\n\t" \
  "s_add_u32 m0, s75, 19456\n\t" \
  "s_add_u32 s33, s29, 1806336\n\t" \
  "ds_read_b64_tr_b16 a[42:43], v74 offset:4096\n\t" \
  "v_mfma_f32_16x16x32_bf16 v[196:199], a[20:23], v[104:107], v[196:199]\n\t" \
  "ds_read_b64_tr_b16 a[44:45], v74 offset:8192\n\t" \
  "ds_read_b64_tr_b16 a[46:47], v74 offset:12288\n\t" \
  "v_mfma_f32_16x16x32_bf16 v[212:215], a[20:23], v[108:111], v[212:215]\n\t" \
  "ds_read_b64 v[80:81], v75 offset:0\n\t" \
  "ds_read_b64 v[82:83], v75 offset:512\n\t" \
  "v_mfma_f32_16x16x32_bf16 v[56:59], a[20:23], v[16:19], v[56:59]\n\t" \
  "buffer_load_dwordx4 v69, s[48:51], s33 offen lds\n\t" \
  "v_mfma_f32_16x16x32_bf16 v[44:47], a[24:27], v[96:99], v[44:47]\n\t" \
  "s_add_u32 m0, s75, 27648\n\t" \
  "s_nop 0\n\t" \
  "ds_read_b64 v[84:85], v75 offset:1024\n\t" \
  "v_mfma_f32_16x16x32_bf16 v[28:31], a[24:27], v[100:103], v[28:31]\n\t" \
  "ds_read_b64 v[86:87], v75 offset:1536\n\t" \
  "ds_read_b64 v[88:89], v75 offset:2048\n\t" \
  "v_mfma_f32_16x16x32_bf16 v[200:203], a[24:27], v[104:107], v[200:203]\n\t" \
  "ds_read_b64 v[90:91], v75 offset:2560\n\t" \
  "ds_read_b64 v[92:93], v75 offset:3072\n\t" \
  "v_mfma_f32_16x16x32_bf16 v[216:219], a[24:27], v[108:111], v[216:219]\n\t" \
  "buffer_load_dwordx4 v69, s[52:55], s33 offen lds\n\t" \
  "v_mfma_f32_16x16x32_bf16 v[60:63], a[24:27], v[16:19], v[60:63]\n\t" \
  "s_add_u32 m0, s97, 34816\n\t" \
  "s_add_u32 s33, s96, 6144\n\t" \
  "ds_read_b64 v[94:95], v75 offset:3584\n\t" \
  "v_mfma_f32_16x16x32_bf16 v[48:51], a[28:31], v[96:99], v[48:51]\n\t" \
  "v_mfma_f32_16x16x32_bf16 v[32:35], a[28:31], v[100:103], v[32:35]\n\t" \
  "v_mfma_f32_16x16x32_bf16 v[204:207], a[28:31], v[104:107], v[204:207]\n\t" \
  "buffer_load_dwordx4 v0, s[36:39], s33 offen lds\n\t" \
  "v_mfma_f32_16x16x32_bf16 v[220:223], a[28:31], v[108:111], v[220:223]\n\t" \
  "v_mfma_f32_16x16x32_bf16 v[64:67], a[28:31], v[16:19], v[64:67]\n\t" \
  "s_waitcnt lgkmcnt(0)\n\t" \
  "s_waitcnt vmcnt(16)\n\t" \
  "s_barrier\n\t" \
  "v_add_u32 v73, 79872, v70\n\t" \
  "v_xor_b32 v74, 32, v73\n\t" \
  "v_mfma_f32_16x16x32_bf16 v[36:39], a[32:35], v[80:83], v[36:39]\n\t" \
  "s_add_u32 m0, s75, 120832\n\t" \
  "s_add_u32 s33, s29, 2064384\n\t" \
  "v_add_u32 v75, 96256, v1\n\t" \
  "v_mfma_f32_16x16x32_bf16 v[20:23], a[32:35], v[84:87], v[20:23]\n\t" \
  "buffer_load_dwordx4 v68, s[48:51], s33 offen lds\n\t" \
  "v_mfma_f32_16x16x32_bf16 v[192:195], a[32:35], v[88:91], v[192:195]\n\t" \
  "s_add_u32 m0, s75, 129024\n\t" \
  "s_nop 0\n\t" \
  "ds_read_b64_tr_b16 a[48:49], v73 offset:0\n\t" \
  "v_mfma_f32_16x16x32_bf16 v[208:211], a[32:35], v[92:95], v[208:211]\n\t" \
  "ds_read_b64_tr_b16 a[50:51], v73 offset:4096\n\t" \
  "ds_read_b64_tr_b16 a[52:53], v73 offset:8192\n\t" \
  "v_mfma_f32_16x16x32_bf16 v[52:55], a[32:35], v[16:19], v[52:55]\n\t" \
  "ds_read_b64_tr_b16 a[54:55], v73 offset:12288\n\t" \
  "ds_read_b64_tr_b16 a[56:57], v74 offset:0\n\t" \
  "v_mfma_f32_16x16x32_bf16 v[40:43], a[36:39], v[80:83], v[40:43]\n\t" \
  "buffer_load_dwordx4 v68, s[52:55], s33 offen lds\n\t" \
  "v_mfma_f32_16x16x32_bf16 v[24:27], a[36:39], v[84:87], v[24:27]\n\t" \
  "s_add_u32 m0, s75, 121856\n\t" \
  "s_add_u32 s33, s29, 2101248\n\t" \
  "ds_read_b64_tr_b16 a[58:59], v74 offset:4096\n\t" \
  "v_mfma_f32_16x16x32_bf16 v[196:199], a[36:39], v[88:91], v[196:199]\n\t" \
  "ds_read_b64_tr_b16 a[60:61], v74 offset:8192\n\t" \
  "ds_read_b64_tr_b16 a[62:63], v74 offset:12288\n\t" \
  "v_mfma_f32_16x16x32_bf16 v[212:215], a[36:39], v[92:95], v[212:215]\n\t" \
  "ds_read_b64 v[96:97], v75 offset:0\n\t" \
  "ds_read_b64 v[98:99], v75 offset:512\n\t" \
  "v_mfma_f32_16x16x32_bf16 v[56:59], a[36:39], v[16:19], v[56:59]\n\t" \
  "buffer_load_dwordx4 v69, s[48:51], s33 offen lds\n\t" \
  "v_mfma_f32_16x16x32_bf16 v[44:47], a[40:43], v[80:83], v[44:47]\n\t" \
  "s_add_u32 m0, s75, 130048\n\t" \
  "s_nop 0\n\t" \
  "ds_read_b64 v[100:101], v75 offset:1024\n\t" \
  "v_mfma_f32_16x16x32_bf16 v[28:31], a[40:43], v[84:87], v[28:31]\n\t" \
  "ds_read_b64 v[102:103], v75 offset:1536\n\t" \
  "ds_read_b64 v[104:105], v75 offset:2048\n\t" \
  "v_mfma_f32_16x16x32_bf16 v[200:203], a[40:43], v[88:91], v[200:203]\n\t" \
  "ds_read_b64 v[106:107], v75 offset:2560\n\t" \
  "ds_read_b64 v[108:109], v75 offset:3072\n\t" \
  "v_mfma_f32_16x16x32_bf16 v[216:219], a[40:43], v[92:95], v[216:219]\n\t" \
  "buffer_load_dwordx4 v69, s[52:55], s33 offen lds\n\t" \
  "v_mfma_f32_16x16x32_bf16 v[60:63], a[40:43], v[16:19], v[60:63]\n\t" \
  "s_add_u32 m0, s97, 137216\n\t" \
  "s_add_u32 s33, s96, 7168\n\t" \
  "ds_read_b64 v[110:111], v75 offset:3584\n\t" \
  "v_mfma_f32_16x16x32_bf16 v[48:51], a[44:47], v[80:83], v[48:51]\n\t" \
  "v_mfma_f32_16x16x32_bf16 v[32:35], a[44:47], v[84:87], v[32:35]\n\t" \
  "v_mfma_f32_16x16x32_bf16 v[204:207], a[44:47], v[88:91], v[204:207]\n\t" \
  "buffer_load_dwordx4 v0, s[36:39], s33 offen lds\n\t" \
  "v_mfma_f32_16x16x32_bf16 v[220:223], a[44:47], v[92:95], v[220:223]\n\t" \
  "v_mfma_f32_16x16x32_bf16 v[64:67], a[44:47], v[16:19], v[64:67]\n\t" \
  "s_waitcnt lgkmcnt(0)\n\t" \
  "s_waitcnt vmcnt(15)\n\t" \
  "s_barrier\n\t" \
  "v_add_u32 v73, 59392, v70\n\t" \
  "v_xor_b32 v74, 32, v73\n\t" \
  "v_mfma_f32_16x16x32_bf16 v[36:39], a[48:51], v[96:99], v[36:39]\n\t" \
  "s_add_u32 m0, s75, 100352\n\t" \
  "s_add_u32 s33, s29, 2359296\n\t" \
  "v_add_u32 v75, 75776, v1\n\t" \
  "v_mfma_f32_16x16x32_bf16 v[20:23], a[48:51], v[100:103], v[20:23]\n\t" \
  "buffer_load_dwordx4 v68, s[48:51], s33 offen lds\n\t" \
  "v_mfma_f32_16x16x32_bf16 v[192:195], a[48:51], v[104:107], v[192:195]\n\t" \
  "s_add_u32 m0, s75, 108544\n\t" \
  "s_nop 0\n\t" \
  "ds_read_b64_tr_b16 a[64:65], v73 offset:0\n\t" \
  "v_mfma_f32_16x16x32_bf16 v[208:211], a[48:51], v[108:111], v[208:211]\n\t" \
  "ds_read_b64_tr_b16 a[66:67], v73 offset:4096\n\t" \
  "ds_read_b64_tr_b16 a[68:69], v73 offset:8192\n\t" \
  "v_mfma_f32_16x16x32_bf16 v[52:55], a[48:51], v[16:19], v[52:55]\n\t" \
  "ds_read_b64_tr_b16 a[70:71], v73 offset:12288\n\t" \
  "ds_read_b64_tr_b16 a[72:73], v74 offset:0\n\t" \
  "v_mfma_f32_16x16x32_bf16 v[40:43], a[52:55], v[96:99], v[40:43]\n\t" \
  "buffer_load_dwordx4 v68, s[52:55], s33 offen lds\n\t" \
  "v_mfma_f32_16x16x32_bf16 v[24:27], a[52:55], v[100:103], v[24:27]\n\t" \
  "s_add_u32 m0, s75, 101376\n\t" \
  "s_add_u32 s33, s29, 2396160\n\t" \
  "ds_read_b64_tr_b16 a[74:75], v74 offset:4096\n\t" \
  "v_mfma_f32_16x16x32_bf16 v[196:199], a[52:55], v[104:107], v[196:199]\n\t" \
  "ds_read_b64_tr_b16 a[76:77], v74 offset:8192\n\t" \
  "ds_read_b64_tr_b16 a[78:79], v74 offset:12288\n\t" \
  "v_mfma_f32_16x16x32_bf16 v[212:215], a[52:55], v[108:111], v[212:215]\n\t" \
  "ds_read_b64 v[80:81], v75 offset:0\n\t" \
  "ds_read_b64 v[82:83], v75 offset:512\n\t" \
  "v_mfma_f32_16x16x32_bf16 v[56:59], a[52:55], v[16:19], v[56:59]\n\t" \
  "buffer_load_dwordx4 v69, s[48:51], s33 offen lds\n\t" \
  "v_mfma_f32_16x16x32_bf16 v[44:47], a[56:59], v[96:99], v[44:47]\n\t" \
  "s_add_u32 m0, s75, 109568\n\t" \
  "s_nop 0\n\t" \
  "ds_read_b64 v[84:85], v75 offset:1024\n\t" \
  "v_mfma_f32_16x16x32_bf16 v[28:31], a[56:59], v[100:103], v[28:31]\n\t" \
  "ds_read_b64 v[86:87], v75 offset:1536\n\t" \
  "ds_read_b64 v[88:89], v75 offset:2048\n\t" \
  "v_mfma_f32_16x16x32_bf16 v[200:203], a[56:59], v[104:107], v[200:203]\n\t" \
  "ds_read_b64 v[90:91], v75 offset:2560\n\t" \
  "ds_read_b64 v[92:93], v75 offset:3072\n\t" \
  "v_mfma_f32_16x16x32_bf16 v[216:219], a[56:59], v[108:111], v[216:219]\n\t" \
  "buffer_load_dwordx4 v69, s[52:55], s33 offen lds\n\t" \
  "v_mfma_f32_16x16x32_bf16 v[60:63], a[56:59], v[16:19], v[60:63]\n\t" \
  "s_add_u32 m0, s97, 116736\n\t" \
  "s_add_u32 s33, s96, 8192\n\t" \
  "ds_read_b64 v[94:95], v75 offset:3584\n\t" \
  "v_mfma_f32_16x16x32_bf16 v[48:51], a[60:63], v[96:99], v[48:51]\n\t" \
  "v_mfma_f32_16x16x32_bf16 v[32:35], a[60:63], v[100:103], v[32:35]\n\t" \
  "v_mfma_f32_16x16x32_bf16 v[204:207], a[60:63], v[104:107], v[204:207]\n\t" \
  "buffer_load_dwordx4 v0, s[36:39], s33 offen lds\n\t" \
  "v_mfma_f32_16x16x32_bf16 v[220:223], a[60:63], v[108:111], v[220:223]\n\t" \
  "v_mfma_f32_16x16x32_bf16 v[64:67], a[60:63], v[16:19], v[64:67]\n\t" \
  "s_waitcnt lgkmcnt(0)\n\t" \
  "s_waitcnt vmcnt(15)\n\t" \
  "s_barrier\n\t" \
  "v_add_u32 v73, 38912, v70\n\t" \
  "v_xor_b32 v74, 32, v73\n\t" \
  "v_mfma_f32_16x16x32_bf16 v[36:39], a[64:67], v[80:83], v[36:39]\n\t" \
  "s_add_u32 m0, s75, 79872\n\t" \
  "s_add_u32 s33, s29, 2654208\n\t" \
  "v_add_u32 v75, 55296, v1\n\t" \
  "v_mfma_f32_16x16x32_bf16 v[20:23], a[64:67], v[84:87], v[20:23]\n\t" \
  "buffer_load_dwordx4 v68, s[48:51], s33 offen lds\n\t" \
  "v_mfma_f32_16x16x32_bf16 v[192:195], a[64:67], v[88:91], v[192:195]\n\t" \
  "s_add_u32 m0, s75, 88064\n\t" \
  "s_nop 0\n\t" \
  "ds_read_b64_tr_b16 a[80:81], v73 offset:0\n\t" \
  "v_mfma_f32_16x16x32_bf16 v[208:211], a[64:67], v[92:95], v[208:211]\n\t" \
  "ds_read_b64_tr_b16 a[82:83], v73 offset:4096\n\t" \
  "ds_read_b64_tr_b16 a[84:85], v73 offset:8192\n\t" \
  "v_mfma_f32_16x16x32_bf16 v[52:55], a[64:67], v[16:19], v[52:55]\n\t" \
  "ds_read_b64_tr_b16 a[86:87], v73 offset:12288\n\t" \
  "ds_read_b64_tr_b16 a[88:89], v74 offset:0\n\t" \
  "v_mfma_f32_16x16x32_bf16 v[40:43], a[68:71], v[80:83], v[40:43]\n\t" \
  "buffer_load_dwordx4 v68, s[52:55], s33 offen lds\n\t" \
  "v_mfma_f32_16x16x32_bf16 v[24:27], a[68:71], v[84:87], v[24:27]\n\t" \
  "s_add_u32 m0, s75, 80896\n\t" \
  "s_add_u32 s33, s29, 2691072\n\t" \
  "ds_read_b64_tr_b16 a[90:91], v74 offset:4096\n\t" \
  "v_mfma_f32_16x16x32_bf16 v[196:199], a[68:71], v[88:91], v[196:199]\n\t" \
  "ds_read_b64_tr_b16 a[92:93], v74 offset:8192\n\t" \
  "ds_read_b64_tr_b16 a[94:95], v74 offset:12288\n\t" \
  "v_mfma_f32_16x16x32_bf16 v[212:215], a[68:71], v[92:95], v[212:215]\n\t" \
  "ds_read_b64 v[96:97], v75 offset:0\n\t" \
  "ds_read_b64 v[98:99], v75 offset:512\n\t" \
  "v_mfma_f32_16x16x32_bf16 v[56:59], a[68:71], v[16:19], v[56:59]\n\t" \
  "buffer_load_dwordx4 v69, s[48:51], s33 offen lds\n\t" \
  "v_mfma_f32_16x16x32_bf16 v[44:47], a[72:75], v[80:83], v[44:47]\n\t" \
  "s_add_u32 m0, s75, 89088\n\t" \
  "s_nop 0\n\t" \
  "ds_read_b64 v[100:101], v75 offset:1024\n\t" \
  "v_mfma_f32_16x16x32_bf16 v[28:31], a[72:75], v[84:87], v[28:31]\n\t" \
  "ds_read_b64 v[102:103], v75 offset:1536\n\t" \
  "ds_read_b64 v[104:105], v75 offset:2048\n\t" \
  "v_mfma_f32_16x16x32_bf16 v[200:203], a[72:75], v[88:91], v[200:203]\n\t" \
  "ds_read_b64 v[106:107], v75 offset:2560\n\t" \
  "ds_read_b64 v[108:109], v75 offset:3072\n\t" \
  "v_mfma_f32_16x16x32_bf16 v[216:219], a[72:75], v[92:95], v[216:219]\n\t" \
  "buffer_load_dwordx4 v69, s[52:55], s33 offen lds\n\t" \
  "v_mfma_f32_16x16x32_bf16 v[60:63], a[72:75], v[16:19], v[60:63]\n\t" \
  "s_add_u32 m0, s97, 96256\n\t" \
  "s_add_u32 s33, s96, 9216\n\t" \
  "ds_read_b64 v[110:111], v75 offset:3584\n\t" \
  "v_mfma_f32_16x16x32_bf16 v[48:51], a[76:79], v[80:83], v[48:51]\n\t" \
  "v_mfma_f32_16x16x32_bf16 v[32:35], a[76:79], v[84:87], v[32:35]\n\t" \
  "v_mfma_f32_16x16x32_bf16 v[204:207], a[76:79], v[88:91], v[204:207]\n\t" \
  "buffer_load_dwordx4 v0, s[36:39], s33 offen lds\n\t" \
  "v_mfma_f32_16x16x32_bf16 v[220:223], a[76:79], v[92:95], v[220:223]\n\t" \
  "v_mfma_f32_16x16x32_bf16 v[64:67], a[76:79], v[16:19], v[64:67]\n\t" \
  "s_waitcnt lgkmcnt(0)\n\t" \
  "s_waitcnt vmcnt(15)\n\t" \
  "s_barrier\n\t" \
  "v_add_u32 v73, 18432, v70\n\t" \
  "v_xor_b32 v74, 32, v73\n\t" \
  "v_mfma_f32_16x16x32_bf16 v[36:39], a[80:83], v[96:99], v[36:39]\n\t" \
  "s_add_u32 m0, s75, 59392\n\t" \
  "s_add_u32 s33, s29, 2949120\n\t" \
  "v_add_u32 v75, 34816, v1\n\t" \
  "v_mfma_f32_16x16x32_bf16 v[20:23], a[80:83], v[100:103], v[20:23]\n\t" \
  "buffer_load_dwordx4 v68, s[48:51], s33 offen lds\n\t" \
  "v_mfma_f32_16x16x32_bf16 v[192:195], a[80:83], v[104:107], v[192:195]\n\t" \
  "s_add_u32 m0, s75, 67584\n\t" \
  "s_nop 0\n\t" \
  "ds_read_b64_tr_b16 a[96:97], v73 offset:0\n\t" \
  "v_mfma_f32_16x16x32_bf16 v[208:211], a[80:83], v[108:111], v[208:211]\n\t" \
  "ds_read_b64_tr_b16 a[98:99], v73 offset:4096\n\t" \
  "ds_read_b64_tr_b16 a[100:101], v73 offset:8192\n\t" \
  "v_mfma_f32_16x16x32_bf16 v[52:55], a[80:83], v[16:19], v[52:55]\n\t" \
  "ds_read_b64_tr_b16 a[102:103], v73 offset:12288\n\t" \
  "ds_read_b64_tr_b16 a[104:105], v74 offset:0\n\t" \
  "v_mfma_f32_16x16x32_bf16 v[40:43], a[84:87], v[96:99], v[40:43]\n\t" \
  "buffer_load_dwordx4 v68, s[52:55], s33 offen lds\n\t" \
  "v_mfma_f32_16x16x32_bf16 v[24:27], a[84:87], v[100:103], v[24:27]\n\t" \
  "s_add_u32 m0, s75, 60416\n\t" \
  "s_add_u32 s33, s29, 2985984\n\t" \
  "ds_read_b64_tr_b16 a[106:107], v74 offset:4096\n\t" \
  "v_mfma_f32_16x16x32_bf16 v[196:199], a[84:87], v[104:107], v[196:199]\n\t" \
  "ds_read_b64_tr_b16 a[108:109], v74 offset:8192\n\t" \
  "ds_read_b64_tr_b16 a[110:111], v74 offset:12288\n\t" \
  "v_mfma_f32_16x16x32_bf16 v[212:215], a[84:87], v[108:111], v[212:215]\n\t" \
  "ds_read_b64 v[80:81], v75 offset:0\n\t" \
  "ds_read_b64 v[82:83], v75 offset:512\n\t" \
  "v_mfma_f32_16x16x32_bf16 v[56:59], a[84:87], v[16:19], v[56:59]\n\t" \
  "buffer_load_dwordx4 v69, s[48:51], s33 offen lds\n\t" \
  "v_mfma_f32_16x16x32_bf16 v[44:47], a[88:91], v[96:99], v[44:47]\n\t" \
  "s_add_u32 m0, s75, 68608\n\t" \
  "s_nop 0\n\t" \
  "ds_read_b64 v[84:85], v75 offset:1024\n\t" \
  "v_mfma_f32_16x16x32_bf16 v[28:31], a[88:91], v[100:103], v[28:31]\n\t" \
  "ds_read_b64 v[86:87], v75 offset:1536\n\t" \
  "ds_read_b64 v[88:89], v75 offset:2048\n\t" \
  "v_mfma_f32_16x16x32_bf16 v[200:203], a[88:91], v[104:107], v[200:203]\n\t" \
  "ds_read_b64 v[90:91], v75 offset:2560\n\t" \
  "ds_read_b64 v[92:93], v75 offset:3072\n\t" \
  "v_mfma_f32_16x16x32_bf16 v[216:219], a[88:91], v[108:111], v[216:219]\n\t" \
  "buffer_load_dwordx4 v69, s[52:55], s33 offen lds\n\t" \
  "v_mfma_f32_16x16x32_bf16 v[60:63], a[88:91], v[16:19], v[60:63]\n\t" \
  "s_add_u32 m0, s97, 75776\n\t" \
  "s_add_u32 s33, s96, 10240\n\t" \
  "ds_read_b64 v[94:95], v75 offset:3584\n\t" \
  "v_mfma_f32_16x16x32_bf16 v[48:51], a[92:95], v[96:99], v[48:51]\n\t" \
  "v_mfma_f32_16x16x32_bf16 v[32:35], a[92:95], v[100:103], v[32:35]\n\t" \
  "v_mfma_f32_16x16x32_bf16 v[204:207], a[92:95], v[104:107], v[204:207]\n\t" \
  "buffer_load_dwordx4 v0, s[36:39], s33 offen lds\n\t" \
  "v_mfma_f32_16x16x32_bf16 v[220:223], a[92:95], v[108:111], v[220:223]\n\t" \
  "v_mfma_f32_16x16x32_bf16 v[64:67], a[92:95], v[16:19], v[64:67]\n\t" \
  "s_waitcnt lgkmcnt(0)\n\t" \
  "s_waitcnt vmcnt(15)\n\t" \
  "s_barrier\n\t" \
  "v_add_u32 v73, 120832, v70\n\t" \
  "v_xor_b32 v74, 32, v73\n\t" \
  "v_mfma_f32_16x16x32_bf16 v[36:39], a[96:99], v[80:83], v[36:39]\n\t" \
  "s_add_u32 m0, s75, 38912\n\t" \
  "s_add_u32 s33, s29, 3244032\n\t" \
  "v_add_u32 v75, 137216, v1\n\t" \
  "v_mfma_f32_16x16x32_bf16 v[20:23], a[96:99], v[84:87], v[20:23]\n\t" \
  "buffer_load_dwordx4 v68, s[48:51], s33 offen lds\n\t" \
  "v_mfma_f32_16x16x32_bf16 v[192:195], a[96:99], v[88:91], v[192:195]\n\t" \
  "s_add_u32 m0, s75, 47104\n\t" \
  "s_nop 0\n\t" \
  "ds_read_b64_tr_b16 a[112:113], v73 offset:0\n\t" \
  "v_mfma_f32_16x16x32_bf16 v[208:211], a[96:99], v[92:95], v[208:211]\n\t" \
  "ds_read_b64_tr_b16 a[114:115], v73 offset:4096\n\t" \
  "ds_read_b64_tr_b16 a[116:117], v73 offset:8192\n\t" \
  "v_mfma_f32_16x16x32_bf16 v[52:55], a[96:99], v[16:19], v[52:55]\n\t" \
  "ds_read_b64_tr_b16 a[118:119], v73 offset:12288\n\t" \
  "ds_read_b64_tr_b16 a[120:121], v74 offset:0\n\t" \
  "v_mfma_f32_16x16x32_bf16 v[40:43], a[100:103], v[80:83], v[40:43]\n\t" \
  "buffer_load_dwordx4 v68, s[52:55], s33 offen lds\n\t" \
  "v_mfma_f32_16x16x32_bf16 v[24:27], a[100:103], v[84:87], v[24:27]\n\t" \
  "s_add_u32 m0, s75, 39936\n\t" \
  "s_add_u32 s33, s29, 3280896\n\t" \
  "ds_read_b64_tr_b16 a[122:123], v74 offset:4096\n\t" \
  "v_mfma_f32_16x16x32_bf16 v[196:199], a[100:103], v[88:91], v[196:199]\n\t" \
  "ds_read_b64_tr_b16 a[124:125], v74 offset:8192\n\t" \
  "ds_read_b64_tr_b16 a[126:127], v74 offset:12288\n\t" \
  "v_mfma_f32_16x16x32_bf16 v[212:215], a[100:103], v[92:95], v[212:215]\n\t" \
  "ds_read_b64 v[96:97], v75 offset:0\n\t" \
  "ds_read_b64 v[98:99], v75 offset:512\n\t" \
  "v_mfma_f32_16x16x32_bf16 v[56:59], a[100:103], v[16:19], v[56:59]\n\t" \
  "buffer_load_dwordx4 v69, s[48:51], s33 offen lds\n\t" \
  "v_mfma_f32_16x16x32_bf16 v[44:47], a[104:107], v[80:83], v[44:47]\n\t" \
  "s_add_u32 m0, s75, 48128\n\t" \
  "s_nop 0\n\t" \
  "ds_read_b64 v[100:101], v75 offset:1024\n\t" \
  "v_mfma_f32_16x16x32_bf16 v[28:31], a[104:107], v[84:87], v[28:31]\n\t" \
  "ds_read_b64 v[102:103], v75 offset:1536\n\t" \
  "ds_read_b64 v[104:105], v75 offset:2048\n\t" \
  "v_mfma_f32_16x16x32_bf16 v[200:203], a[104:107], v[88:91], v[200:203]\n\t" \
  "ds_read_b64 v[106:107], v75 offset:2560\n\t" \
  "ds_read_b64 v[108:109], v75 offset:3072\n\t" \
  "v_mfma_f32_16x16x32_bf16 v[216:219], a[104:107], v[92:95], v[216:219]\n\t" \
  "buffer_load_dwordx4 v69, s[52:55], s33 offen lds\n\t" \
  "v_mfma_f32_16x16x32_bf16 v[60:63], a[104:107], v[16:19], v[60:63]\n\t" \
  "s_add_u32 m0, s97, 55296\n\t" \
  "s_add_u32 s33, s96, 11264\n\t" \
  "ds_read_b64 v[110:111], v75 offset:3584\n\t" \
  "v_mfma_f32_16x16x32_bf16 v[48:51], a[108:111], v[80:83], v[48:51]\n\t" \
  "v_mfma_f32_16x16x32_bf16 v[32:35], a[108:111], v[84:87], v[32:35]\n\t" \
  "v_mfma_f32_16x16x32_bf16 v[204:207], a[108:111], v[88:91], v[204:207]\n\t" \
  "buffer_load_dwordx4 v0, s[36:39], s33 offen lds\n\t" \
  "v_mfma_f32_16x16x32_bf16 v[220:223], a[108:111], v[92:95], v[220:223]\n\t" \
  "v_mfma_f32_16x16x32_bf16 v[64:67], a[108:111], v[16:19], v[64:67]\n\t" \
  "s_waitcnt lgkmcnt(0)\n\t" \
  "s_waitcnt vmcnt(15)\n\t" \
  "s_barrier\n\t" \
  "v_add_u32 v73, 100352, v70\n\t" \
  "v_xor_b32 v74, 32, v73\n\t" \
  "v_mfma_f32_16x16x32_bf16 v[36:39], a[112:115], v[96:99], v[36:39]\n\t" \
  "s_add_u32 m0, s75, 18432\n\t" \
  "s_add_u32 s33, s29, 3538944\n\t" \
  "v_add_u32 v75, 116736, v1\n\t" \
  "v_mfma_f32_16x16x32_bf16 v[20:23], a[112:115], v[100:103], v[20:23]\n\t" \
  "buffer_load_dwordx4 v68, s[48:51], s33 offen lds\n\t" \
  "v_mfma_f32_16x16x32_bf16 v[192:195], a[112:115], v[104:107], v[192:195]\n\t" \
  "s_add_u32 m0, s75, 26624\n\t" \
  "s_nop 0\n\t" \
  "ds_read_b64_tr_b16 a[128:129], v73 offset:0\n\t" \
  "v_mfma_f32_16x16x32_bf16 v[208:211], a[112:115], v[108:111], v[208:211]\n\t" \
  "ds_read_b64_tr_b16 a[130:131], v73 offset:4096\n\t" \
  "ds_read_b64_tr_b16 a[132:133], v73 offset:8192\n\t" \
  "v_mfma_f32_16x16x32_bf16 v[52:55], a[112:115], v[16:19], v[52:55]\n\t" \
  "ds_read_b64_tr_b16 a[134:135], v73 offset:12288\n\t" \
  "ds_read_b64_tr_b16 a[136:137], v74 offset:0\n\t" \
  "v_mfma_f32_16x16x32_bf16 v[40:43], a[116:119], v[96:99], v[40:43]\n\t" \
  "buffer_load_dwordx4 v68, s[52:55], s33 offen lds\n\t" \
  "v_mfma_f32_16x16x32_bf16 v[24:27], a[116:119], v[100:103], v[24:27]\n\t" \
  "s_add_u32 m0, s75, 19456\n\t" \
  "s_add_u32 s33, s29, 3575808\n\t" \
  "ds_read_b64_tr_b16 a[138:139], v74 offset:4096\n\t" \
  "v_mfma_f32_16x16x32_bf16 v[196:199], a[116:119], v[104:107], v[196:199]\n\t" \
  "ds_read_b64_tr_b16 a[140:141], v74 offset:8192\n\t" \
  "ds_read_b64_tr_b16 a[142:143], v74 offset:12288\n\t" \
  "v_mfma_f32_16x16x32_bf16 v[212:215], a[116:119], v[108:111], v[212:215]\n\t" \
  "ds_read_b64 v[80:81], v75 offset:0\n\t" \
  "ds_read_b64 v[82:83], v75 offset:512\n\t" \
  "v_mfma_f32_16x16x32_bf16 v[56:59], a[116:119], v[16:19], v[56:59]\n\t" \
  "buffer_load_dwordx4 v69, s[48:51], s33 offen lds\n\t" \
  "v_mfma_f32_16x16x32_bf16 v[44:47], a[120:123], v[96:99], v[44:47]\n\t" \
  "s_add_u32 m0, s75, 27648\n\t" \
  "s_nop 0\n\t" \
  "ds_read_b64 v[84:85], v75 offset:1024\n\t" \
  "v_mfma_f32_16x16x32_bf16 v[28:31], a[120:123], v[100:103], v[28:31]\n\t" \
  "ds_read_b64 v[86:87], v75 offset:1536\n\t" \
  "ds_read_b64 v[88:89], v75 offset:2048\n\t" \
  "v_mfma_f32_16x16x32_bf16 v[200:203], a[120:123], v[104:107], v[200:203]\n\t" \
  "ds_read_b64 v[90:91], v75 offset:2560\n\t" \
  "ds_read_b64 v[92:93], v75 offset:3072\n\t" \
  "v_mfma_f32_16x16x32_bf16 v[216:219], a[120:123], v[108:111], v[216:219]\n\t" \
  "buffer_load_dwordx4 v69, s[52:55], s33 offen lds\n\t" \
  "v_mfma_f32_16x16x32_bf16 v[60:63], a[120:123], v[16:19], v[60:63]\n\t" \
  "s_add_u32 m0, s97, 34816\n\t" \
  "s_add_u32 s33, s96, 12288\n\t" \
  "ds_read_b64 v[94:95], v75 offset:3584\n\t" \
  "v_mfma_f32_16x16x32_bf16 v[48:51], a[124:127], v[96:99], v[48:51]\n\t" \
  "v_mfma_f32_16x16x32_bf16 v[32:35], a[124:127], v[100:103], v[32:35]\n\t" \
  "v_mfma_f32_16x16x32_bf16 v[204:207], a[124:127], v[104:107], v[204:207]\n\t" \
  "buffer_load_dwordx4 v0, s[36:39], s33 offen lds\n\t" \
  "v_mfma_f32_16x16x32_bf16 v[220:223], a[124:127], v[108:111], v[220:223]\n\t" \
  "v_mfma_f32_16x16x32_bf16 v[64:67], a[124:127], v[16:19], v[64:67]\n\t" \
  "s_waitcnt lgkmcnt(0)\n\t" \
  "s_waitcnt vmcnt(15)\n\t" \
  "s_barrier\n\t" \
  "v_add_u32 v73, 79872, v70\n\t" \
  "v_xor_b32 v74, 32, v73\n\t" \
  "v_mfma_f32_16x16x32_bf16 v[36:39], a[128:131], v[80:83], v[36:39]\n\t" \
  "s_add_u32 m0, s75, 120832\n\t" \
  "s_add_u32 s33, s29, 3833856\n\t" \
  "v_add_u32 v75, 96256, v1\n\t" \
  "v_mfma_f32_16x16x32_bf16 v[20:23], a[128:131], v[84:87], v[20:23]\n\t" \
  "buffer_load_dwordx4 v68, s[48:51], s33 offen lds\n\t" \
  "v_mfma_f32_16x16x32_bf16 v[192:195], a[128:131], v[88:91], v[192:195]\n\t" \
  "s_add_u32 m0, s75, 129024\n\t" \
  "s_nop 0\n\t" \
  "ds_read_b64_tr_b16 a[144:145], v73 offset:0\n\t" \
  "v_mfma_f32_16x16x32_bf16 v[208:211], a[128:131], v[92:95], v[208:211]\n\t" \
  "ds_read_b64_tr_b16 a[146:147], v73 offset:4096\n\t" \
  "ds_read_b64_tr_b16 a[148:149], v73 offset:8192\n\t" \
  "v_mfma_f32_16x16x32_bf16 v[52:55], a[128:131], v[16:19], v[52:55]\n\t" \
  "ds_read_b64_tr_b16 a[150:151], v73 offset:12288\n\t" \
  "ds_read_b64_tr_b16 a[152:153], v74 offset:0\n\t" \
  "v_mfma_f32_16x16x32_bf16 v[40:43], a[132:135], v[80:83], v[40:43]\n\t" \
  "buffer_load_dwordx4 v68, s[52:55], s33 offen lds\n\t" \
  "v_mfma_f32_16x16x32_bf16 v[24:27], a[132:135], v[84:87], v[24:27]\n\t" \
  "s_add_u32 m0, s75, 121856\n\t" \
  "s_add_u32 s33, s29, 3870720\n\t" \
  "ds_read_b64_tr_b16 a[154:155], v74 offset:4096\n\t" \
  "v_mfma_f32_16x16x32_bf16 v[196:199], a[132:135], v[88:91], v[196:199]\n\t" \
  "ds_read_b64_tr_b16 a[156:157], v74 offset:8192\n\t" \
  "ds_read_b64_tr_b16 a[158:159], v74 offset:12288\n\t" \
  "v_mfma_f32_16x16x32_bf16 v[212:215], a[132:135], v[92:95], v[212:215]\n\t" \
  "ds_read_b64 v[96:97], v75 offset:0\n\t" \
  "ds_read_b64 v[98:99], v75 offset:512\n\t" \
  "v_mfma_f32_16x16x32_bf16 v[56:59], a[132:135], v[16:19], v[56:59]\n\t" \
  "buffer_load_dwordx4 v69, s[48:51], s33 offen lds\n\t" \
  "v_mfma_f32_16x16x32_bf16 v[44:47], a[136:139], v[80:83], v[44:47]\n\t" \
  "s_add_u32 m0, s75, 130048\n\t" \
  "s_nop 0\n\t" \
  "ds_read_b64 v[100:101], v75 offset:1024\n\t" \
  "v_mfma_f32_16x16x32_bf16 v[28:31], a[136:139], v[84:87], v[28:31]\n\t" \
  "ds_read_b64 v[102:103], v75 offset:1536\n\t" \
  "ds_read_b64 v[104:105], v75 offset:2048\n\t" \
  "v_mfma_f32_16x16x32_bf16 v[200:203], a[136:139], v[88:91], v[200:203]\n\t" \
  "ds_read_b64 v[106:107], v75 offset:2560\n\t" \
  "ds_read_b64 v[108:109], v75 offset:3072\n\t" \
  "v_mfma_f32_16x16x32_bf16 v[216:219], a[136:139], v[92:95], v[216:219]\n\t" \
  "buffer_load_dwordx4 v69, s[52:55], s33 offen lds\n\t" \
  "v_mfma_f32_16x16x32_bf16 v[60:63], a[136:139], v[16:19], v[60:63]\n\t" \
  "s_add_u32 m0, s97, 137216\n\t" \
  "s_add_u32 s33, s96, 13312\n\t" \
  "ds_read_b64 v[110:111], v75 offset:3584\n\t" \
  "v_mfma_f32_16x16x32_bf16 v[48:51], a[140:143], v[80:83], v[48:51]\n\t" \
  "v_mfma_f32_16x16x32_bf16 v[32:35], a[140:143], v[84:87], v[32:35]\n\t" \
  "v_mfma_f32_16x16x32_bf16 v[204:207], a[140:143], v[88:91], v[204:207]\n\t" \
  "buffer_load_dwordx4 v0, s[36:39], s33 offen lds\n\t" \
  "v_mfma_f32_16x16x32_bf16 v[220:223], a[140:143], v[92:95], v[220:223]\n\t" \
  "v_mfma_f32_16x16x32_bf16 v[64:67], a[140:143], v[16:19], v[64:67]\n\t" \
  "s_waitcnt lgkmcnt(0)\n\t" \
  "s_waitcnt vmcnt(15)\n\t" \
  "s_barrier\n\t" \
  "v_add_u32 v73, 59392, v70\n\t" \
  "v_xor_b32 v74, 32, v73\n\t" \
  "v_mfma_f32_16x16x32_bf16 v[36:39], a[144:147], v[96:99], v[36:39]\n\t" \
  "s_add_u32 m0, s75, 100352\n\t" \
  "s_add_u32 s33, s29, 4128768\n\t" \
  "v_add_u32 v75, 75776, v1\n\t" \
  "v_mfma_f32_16x16x32_bf16 v[20:23], a[144:147], v[100:103], v[20:23]\n\t" \
  "buffer_load_dwordx4 v68, s[48:51], s33 offen lds\n\t" \
  "v_mfma_f32_16x16x32_bf16 v[192:195], a[144:147], v[104:107], v[192:195]\n\t" \
  "s_add_u32 m0, s75, 108544\n\t" \
  "s_nop 0\n\t" \
  "ds_read_b64_tr_b16 a[160:161], v73 offset:0\n\t" \
  "v_mfma_f32_16x16x32_bf16 v[208:211], a[144:147], v[108:111], v[208:211]\n\t" \
  "ds_read_b64_tr_b16 a[162:163], v73 offset:4096\n\t" \
  "ds_read_b64_tr_b16 a[164:165], v73 offset:8192\n\t" \
  "v_mfma_f32_16x16x32_bf16 v[52:55], a[144:147], v[16:19], v[52:55]\n\t" \
  "ds_read_b64_tr_b16 a[166:167], v73 offset:12288\n\t" \
  "ds_read_b64_tr_b16 a[168:169], v74 offset:0\n\t" \
  "v_mfma_f32_16x16x32_bf16 v[40:43], a[148:151], v[96:99], v[40:43]\n\t" \
  "buffer_load_dwordx4 v68, s[52:55], s33 offen lds\n\t" \
  "v_mfma_f32_16x16x32_bf16 v[24:27], a[148:151], v[100:103], v[24:27]\n\t" \
  "s_add_u32 m0, s75, 101376\n\t" \
  "s_add_u32 s33, s29, 4165632\n\t" \
  "ds_read_b64_tr_b16 a[170:171], v74 offset:4096\n\t" \
  "v_mfma_f32_16x16x32_bf16 v[196:199], a[148:151], v[104:107], v[196:199]\n\t" \
  "ds_read_b64_tr_b16 a[172:173], v74 offset:8192\n\t" \
  "ds_read_b64_tr_b16 a[174:175], v74 offset:12288\n\t" \
  "v_mfma_f32_16x16x32_bf16 v[212:215], a[148:151], v[108:111], v[212:215]\n\t" \
  "ds_read_b64 v[80:81], v75 offset:0\n\t" \
  "ds_read_b64 v[82:83], v75 offset:512\n\t" \
  "v_mfma_f32_16x16x32_bf16 v[56:59], a[148:151], v[16:19], v[56:59]\n\t" \
  "buffer_load_dwordx4 v69, s[48:51], s33 offen lds\n\t" \
  "v_mfma_f32_16x16x32_bf16 v[44:47], a[152:155], v[96:99], v[44:47]\n\t" \
  "s_add_u32 m0, s75, 109568\n\t" \
  "s_nop 0\n\t" \
  "ds_read_b64 v[84:85], v75 offset:1024\n\t" \
  "v_mfma_f32_16x16x32_bf16 v[28:31], a[152:155], v[100:103], v[28:31]\n\t" \
  "ds_read_b64 v[86:87], v75 offset:1536\n\t" \
  "ds_read_b64 v[88:89], v75 offset:2048\n\t" \
  "v_mfma_f32_16x16x32_bf16 v[200:203], a[152:155], v[104:107], v[200:203]\n\t" \
  "ds_read_b64 v[90:91], v75 offset:2560\n\t" \
  "ds_read_b64 v[92:93], v75 offset:3072\n\t" \
  "v_mfma_f32_16x16x32_bf16 v[216:219], a[152:155], v[108:111], v[216:219]\n\t" \
  "buffer_load_dwordx4 v69, s[52:55], s33 offen lds\n\t" \
  "v_mfma_f32_16x16x32_bf16 v[60:63], a[152:155], v[16:19], v[60:63]\n\t" \
  "s_add_u32 m0, s97, 116736\n\t" \
  "s_add_u32 s33, s96, 14336\n\t" \
  "ds_read_b64 v[94:95], v75 offset:3584\n\t" \
  "v_mfma_f32_16x16x32_bf16 v[48:51], a[156:159], v[96:99], v[48:51]\n\t" \
  "v_mfma_f32_16x16x32_bf16 v[32:35], a[156:159], v[100:103], v[32:35]\n\t" \
  "v_mfma_f32_16x16x32_bf16 v[204:207], a[156:159], v[104:107], v[204:207]\n\t" \
  "buffer_load_dwordx4 v0, s[36:39], s33 offen lds\n\t" \
  "v_mfma_f32_16x16x32_bf16 v[220:223], a[156:159], v[108:111], v[220:223]\n\t" \
  "v_mfma_f32_16x16x32_bf16 v[64:67], a[156:159], v[16:19], v[64:67]\n\t" \
  "s_waitcnt lgkmcnt(0)\n\t" \
  "s_waitcnt vmcnt(15)\n\t" \
  "s_barrier\n\t" \
  "v_add_u32 v73, 38912, v70\n\t" \
  "v_xor_b32 v74, 32, v73\n\t" \
  "v_mfma_f32_16x16x32_bf16 v[36:39], a[160:163], v[80:83], v[36:39]\n\t" \
  "s_add_u32 m0, s75, 79872\n\t" \
  "s_add_u32 s33, s29, 4423680\n\t" \
  "v_add_u32 v75, 55296, v1\n\t" \
  "v_mfma_f32_16x16x32_bf16 v[20:23], a[160:163], v[84:87], v[20:23]\n\t" \
  "buffer_load_dwordx4 v68, s[48:51], s33 offen lds\n\t" \
  "v_mfma_f32_16x16x32_bf16 v[192:195], a[160:163], v[88:91], v[192:195]\n\t" \
  "s_add_u32 m0, s75, 88064\n\t" \
  "s_nop 0\n\t" \
  "ds_read_b64_tr_b16 a[176:177], v73 offset:0\n\t" \
  "v_mfma_f32_16x16x32_bf16 v[208:211], a[160:163], v[92:95], v[208:211]\n\t" \
  "ds_read_b64_tr_b16 a[178:179], v73 offset:4096\n\t" \
  "ds_read_b64_tr_b16 a[180:181], v73 offset:8192\n\t" \
  "v_mfma_f32_16x16x32_bf16 v[52:55], a[160:163], v[16:19], v[52:55]\n\t" \
  "ds_read_b64_tr_b16 a[182:183], v73 offset:12288\n\t" \
  "ds_read_b64_tr_b16 a[184:185], v74 offset:0\n\t" \
  "v_mfma_f32_16x16x32_bf16 v[40:43], a[164:167], v[80:83], v[40:43]\n\t" \
  "buffer_load_dwordx4 v68, s[52:55], s33 offen lds\n\t" \
  "v_mfma_f32_16x16x32_bf16 v[24:27], a[164:167], v[84:87], v[24:27]\n\t" \
  "s_add_u32 m0, s75, 80896\n\t" \
  "s_add_u32 s33, s29, 4460544\n\t" \
  "ds_read_b64_tr_b16 a[186:187], v74 offset:4096\n\t" \
  "v_mfma_f32_16x16x32_bf16 v[196:199], a[164:167], v[88:91], v[196:199]\n\t" \
  "ds_read_b64_tr_b16 a[188:189], v74 offset:8192\n\t" \
  "ds_read_b64_tr_b16 a[190:191], v74 offset:12288\n\t" \
  "v_mfma_f32_16x16x32_bf16 v[212:215], a[164:167], v[92:95], v[212:215]\n\t" \
  "ds_read_b64 v[96:97], v75 offset:0\n\t" \
  "ds_read_b64 v[98:99], v75 offset:512\n\t" \
  "v_mfma_f32_16x16x32_bf16 v[56:59], a[164:167], v[16:19], v[56:59]\n\t" \
  "buffer_load_dwordx4 v69, s[48:51], s33 offen lds\n\t" \
  "v_mfma_f32_16x16x32_bf16 v[44:47], a[168:171], v[80:83], v[44:47]\n\t" \
  "s_add_u32 m0, s75, 89088\n\t" \
  "s_nop 0\n\t" \
  "ds_read_b64 v[100:101], v75 offset:1024\n\t" \
  "v_mfma_f32_16x16x32_bf16 v[28:31], a[168:171], v[84:87], v[28:31]\n\t" \
  "ds_read_b64 v[102:103], v75 offset:1536\n\t" \
  "ds_read_b64 v[104:105], v75 offset:2048\n\t" \
  "v_mfma_f32_16x16x32_bf16 v[200:203], a[168:171], v[88:91], v[200:203]\n\t" \
  "ds_read_b64 v[106:107], v75 offset:2560\n\t" \
  "ds_read_b64 v[108:109], v75 offset:3072\n\t" \
  "v_mfma_f32_16x16x32_bf16 v[216:219], a[168:171], v[92:95], v[216:219]\n\t" \
  "buffer_load_dwordx4 v69, s[52:55], s33 offen lds\n\t" \
  "v_mfma_f32_16x16x32_bf16 v[60:63], a[168:171], v[16:19], v[60:63]\n\t" \
  "s_add_u32 m0, s97, 96256\n\t" \
  "s_add_u32 s33, s96, 15360\n\t" \
  "ds_read_b64 v[110:111], v75 offset:3584\n\t" \
  "v_mfma_f32_16x16x32_bf16 v[48:51], a[172:175], v[80:83], v[48:51]\n\t" \
  "v_mfma_f32_16x16x32_bf16 v[32:35], a[172:175], v[84:87], v[32:35]\n\t" \
  "v_mfma_f32_16x16x32_bf16 v[204:207], a[172:175], v[88:91], v[204:207]\n\t" \
  "buffer_load_dwordx4 v0, s[36:39], s33 offen lds\n\t" \
  "v_mfma_f32_16x16x32_bf16 v[220:223], a[172:175], v[92:95], v[220:223]\n\t" \
  "v_mfma_f32_16x16x32_bf16 v[64:67], a[172:175], v[16:19], v[64:67]\n\t" \
  "s_waitcnt lgkmcnt(0)\n\t" \
  "s_waitcnt vmcnt(15)\n\t" \
  "s_barrier\n\t" \
  "v_add_u32 v73, 18432, v70\n\t" \
  "v_xor_b32 v74, 32, v73\n\t" \
  "v_mfma_f32_16x16x32_bf16 v[36:39], a[176:179], v[96:99], v[36:39]\n\t" \
  "s_add_u32 m0, s75, 59392\n\t" \
  "s_add_u32 s33, s29, 4718592\n\t" \
  "v_add_u32 v75, 34816, v1\n\t" \
  "v_mfma_f32_16x16x32_bf16 v[20:23], a[176:179], v[100:103], v[20:23]\n\t" \
  "buffer_load_dwordx4 v68, s[48:51], s33 offen lds\n\t" \
  "v_mfma_f32_16x16x32_bf16 v[192:195], a[176:179], v[104:107], v[192:195]\n\t" \
  "s_add_u32 m0, s75, 67584\n\t" \
  "s_nop 0\n\t" \
  "ds_read_b64_tr_b16 a[192:193], v73 offset:0\n\t" \
  "v_mfma_f32_16x16x32_bf16 v[208:211], a[176:179], v[108:111], v[208:211]\n\t" \
  "ds_read_b64_tr_b16 a[194:195], v73 offset:4096\n\t" \
  "ds_read_b64_tr_b16 a[196:197], v73 offset:8192\n\t" \
  "v_mfma_f32_16x16x32_bf16 v[52:55], a[176:179], v[16:19], v[52:55]\n\t" \
  "ds_read_b64_tr_b16 a[198:199], v73 offset:12288\n\t" \
  "ds_read_b64_tr_b16 a[200:201], v74 offset:0\n\t" \
  "v_mfma_f32_16x16x32_bf16 v[40:43], a[180:183], v[96:99], v[40:43]\n\t" \
  "buffer_load_dwordx4 v68, s[52:55], s33 offen lds\n\t" \
  "v_mfma_f32_16x16x32_bf16 v[24:27], a[180:183], v[100:103], v[24:27]\n\t" \
  "s_add_u32 m0, s75, 60416\n\t" \
  "s_add_u32 s33, s29, 4755456\n\t" \
  "ds_read_b64_tr_b16 a[202:203], v74 offset:4096\n\t" \
  "v_mfma_f32_16x16x32_bf16 v[196:199], a[180:183], v[104:107], v[196:199]\n\t" \
  "ds_read_b64_tr_b16 a[204:205], v74 offset:8192\n\t" \
  "ds_read_b64_tr_b16 a[206:207], v74 offset:12288\n\t" \
  "v_mfma_f32_16x16x32_bf16 v[212:215], a[180:183], v[108:111], v[212:215]\n\t" \
  "ds_read_b64 v[80:81], v75 offset:0\n\t" \
  "ds_read_b64 v[82:83], v75 offset:512\n\t" \
  "v_mfma_f32_16x16x32_bf16 v[56:59], a[180:183], v[16:19], v[56:59]\n\t" \
  "buffer_load_dwordx4 v69, s[48:51], s33 offen lds\n\t" \
  "v_mfma_f32_16x16x32_bf16 v[44:47], a[184:187], v[96:99], v[44:47]\n\t" \
  "s_add_u32 m0, s75, 68608\n\t" \
  "s_nop 0\n\t" \
  "ds_read_b64 v[84:85], v75 offset:1024\n\t" \
  "v_mfma_f32_16x16x32_bf16 v[28:31], a[184:187], v[100:103], v[28:31]\n\t" \
  "ds_read_b64 v[86:87], v75 offset:1536\n\t" \
  "ds_read_b64 v[88:89], v75 offset:2048\n\t" \
  "v_mfma_f32_16x16x32_bf16 v[200:203], a[184:187], v[104:107], v[200:203]\n\t" \
  "ds_read_b64 v[90:91], v75 offset:2560\n\t" \
  "ds_read_b64 v[92:93], v75 offset:3072\n\t" \
  "v_mfma_f32_16x16x32_bf16 v[216:219], a[184:187], v[108:111], v[216:219]\n\t" \
  "buffer_load_dwordx4 v69, s[52:55], s33 offen lds\n\t" \
  "v_mfma_f32_16x16x32_bf16 v[60:63], a[184:187], v[16:19], v[60:63]\n\t" \
  "s_add_u32 m0, s97, 75776\n\t" \
  "s_add_u32 s33, s96, 16384\n\t" \
  "ds_read_b64 v[94:95], v75 offset:3584\n\t" \
  "v_mfma_f32_16x16x32_bf16 v[48:51], a[188:191], v[96:99], v[48:51]\n\t" \
  "v_mfma_f32_16x16x32_bf16 v[32:35], a[188:191], v[100:103], v[32:35]\n\t" \
  "v_mfma_f32_16x16x32_bf16 v[204:207], a[188:191], v[104:107], v[204:207]\n\t" \
  "buffer_load_dwordx4 v0, s[36:39], s33 offen lds\n\t" \
  "v_mfma_f32_16x16x32_bf16 v[220:223], a[188:191], v[108:111], v[220:223]\n\t" \
  "buffer_load_dwordx2 v[112:113], v4, s[68:71], 0 offen sc1\n\t" \
  "v_mfma_f32_16x16x32_bf16 v[64:67], a[188:191], v[16:19], v[64:67]\n\t" \
  "buffer_load_dwordx2 v[114:115], v5, s[68:71], 0 offen sc1\n\t" \
  "s_waitcnt lgkmcnt(0)\n\t" \
  "s_waitcnt vmcnt(17)\n\t" \
  "s_barrier\n\t" \
  "v_add_u32 v73, 120832, v70\n\t" \
  "v_xor_b32 v74, 32, v73\n\t" \
  "v_mfma_f32_16x16x32_bf16 v[36:39], a[192:195], v[80:83], v[36:39]\n\t" \
  "s_add_u32 m0, s75, 38912\n\t" \
  "s_add_u32 s33, s29, 5013504\n\t" \
  "v_add_u32 v75, 137216, v1\n\t" \
  "v_mfma_f32_16x16x32_bf16 v[20:23], a[192:195], v[84:87], v[20:23]\n\t" \
  "buffer_load_dwordx4 v68, s[48:51], s33 offen lds\n\t" \
  "v_mfma_f32_16x16x32_bf16 v[192:195], a[192:195], v[88:91], v[192:195]\n\t" \
  "s_add_u32 m0, s75, 47104\n\t" \
  "s_nop 0\n\t" \
  "ds_read_b64_tr_b16 a[208:209], v73 offset:0\n\t" \
  "v_mfma_f32_16x16x32_bf16 v[208:211], a[192:195], v[92:95], v[208:211]\n\t" \
  "ds_read_b64_tr_b16 a[210:211], v73 offset:4096\n\t" \
  "ds_read_b64_tr_b16 a[212:213], v73 offset:8192\n\t" \
  "v_mfma_f32_16x16x32_bf16 v[52:55], a[192:195], v[16:19], v[52:55]\n\t" \
  "ds_read_b64_tr_b16 a[214:215], v73 offset:12288\n\t" \
  "ds_read_b64_tr_b16 a[216:217], v74 offset:0\n\t" \
  "v_mfma_f32_16x16x32_bf16 v[40:43], a[196:199], v[80:83], v[40:43]\n\t" \
  "buffer_load_dwordx4 v68, s[52:55], s33 offen lds\n\t" \
  "v_mfma_f32_16x16x32_bf16 v[24:27], a[196:199], v[84:87], v[24:27]\n\t" \
  "s_add_u32 m0, s75, 39936\n\t" \
  "s_add_u32 s33, s29, 5050368\n\t" \
  "ds_read_b64_tr_b16 a[218:219], v74 offset:4096\n\t" \
  "v_mfma_f32_16x16x32_bf16 v[196:199], a[196:199], v[88:91], v[196:199]\n\t" \
  "ds_read_b64_tr_b16 a[220:221], v74 offset:8192\n\t" \
  "ds_read_b64_tr_b16 a[222:223], v74 offset:12288\n\t" \
  "v_mfma_f32_16x16x32_bf16 v[212:215], a[196:199], v[92:95], v[212:215]\n\t" \
  "ds_read_b64 v[96:97], v75 offset:0\n\t" \
  "ds_read_b64 v[98:99], v75 offset:512\n\t" \
  "v_mfma_f32_16x16x32_bf16 v[56:59], a[196:199], v[16:19], v[56:59]\n\t" \
  "buffer_load_dwordx4 v69, s[48:51], s33 offen lds\n\t" \
  "v_mfma_f32_16x16x32_bf16 v[44:47], a[200:203], v[80:83], v[44:47]\n\t" \
  "s_add_u32 m0, s75, 48128\n\t" \
  "s_nop 0\n\t" \
  "ds_read_b64 v[100:101], v75 offset:1024\n\t" \
  "v_mfma_f32_16x16x32_bf16 v[28:31], a[200:203], v[84:87], v[28:31]\n\t" \
  "ds_read_b64 v[102:103], v75 offset:1536\n\t" \
  "ds_read_b64 v[104:105], v75 offset:2048\n\t" \
  "v_mfma_f32_16x16x32_bf16 v[200:203], a[200:203], v[88:91], v[200:203]\n\t" \
  "ds_read_b64 v[106:107], v75 offset:2560\n\t" \
  "ds_read_b64 v[108:109], v75 offset:3072\n\t" \
  "v_mfma_f32_16x16x32_bf16 v[216:219], a[200:203], v[92:95], v[216:219]\n\t" \
  "buffer_load_dwordx4 v69, s[52:55], s33 offen lds\n\t" \
  "v_mfma_f32_16x16x32_bf16 v[60:63], a[200:203], v[16:19], v[60:63]\n\t" \
  "s_add_u32 m0, s97, 55296\n\t" \
  "s_add_u32 s33, s96, 17408\n\t" \
  "ds_read_b64 v[110:111], v75 offset:3584\n\t" \
  "v_mfma_f32_16x16x32_bf16 v[48:51], a[204:207], v[80:83], v[48:51]\n\t" \
  "v_mfma_f32_16x16x32_bf16 v[32:35], a[204:207], v[84:87], v[32:35]\n\t" \
  "v_mfma_f32_16x16x32_bf16 v[204:207], a[204:207], v[88:91], v[204:207]\n\t" \
  "buffer_load_dwordx4 v0, s[36:39], s33 offen lds\n\t" \
  "v_mfma_f32_16x16x32_bf16 v[220:223], a[204:207], v[92:95], v[220:223]\n\t" \
  "v_mfma_f32_16x16x32_bf16 v[64:67], a[204:207], v[16:19], v[64:67]\n\t" \
  "s_waitcnt lgkmcnt(0)\n\t" \
  "s_waitcnt vmcnt(17)\n\t" \
  "s_barrier\n\t" \
  "v_add_u32 v73, 100352, v70\n\t" \
  "v_xor_b32 v74, 32, v73\n\t" \
  "v_mfma_f32_16x16x32_bf16 v[36:39], a[208:211], v[96:99], v[36:39]\n\t" \
  "s_mul_i32 s33, s84, 18432\n\t" \
  "s_add_u32 m0, s88, 0\n\t" \
  "s_add_u32 s34, s33, s88\n\t" \
  "v_mfma_f32_16x16x32_bf16 v[20:23], a[208:211], v[100:103], v[20:23]\n\t" \
  "buffer_load_dwordx4 v0, s[36:39], s34 offen offset:0 lds\n\t" \
  "v_mfma_f32_16x16x32_bf16 v[192:195], a[208:211], v[104:107], v[192:195]\n\t" \
  "v_add_u32 v75, 116736, v1\n\t" \
  "ds_read_b64_tr_b16 a[224:225], v73 offset:0\n\t" \
  "v_mfma_f32_16x16x32_bf16 v[208:211], a[208:211], v[108:111], v[208:211]\n\t" \
  "ds_read_b64_tr_b16 a[226:227], v73 offset:4096\n\t" \
  "ds_read_b64_tr_b16 a[228:229], v73 offset:8192\n\t" \
  "v_mfma_f32_16x16x32_bf16 v[52:55], a[208:211], v[16:19], v[52:55]\n\t" \
  "ds_read_b64_tr_b16 a[230:231], v73 offset:12288\n\t" \
  "ds_read_b64_tr_b16 a[232:233], v74 offset:0\n\t" \
  "v_mfma_f32_16x16x32_bf16 v[40:43], a[212:215], v[96:99], v[40:43]\n\t" \
  "buffer_load_dwordx4 v0, s[36:39], s34 offen offset:1024 lds\n\t" \
  "v_mfma_f32_16x16x32_bf16 v[24:27], a[212:215], v[100:103], v[24:27]\n\t" \
  "ds_read_b64_tr_b16 a[234:235], v74 offset:4096\n\t" \
  "ds_read_b64_tr_b16 a[236:237], v74 offset:8192\n\t" \
  "v_mfma_f32_16x16x32_bf16 v[196:199], a[212:215], v[104:107], v[196:199]\n\t" \
  "ds_read_b64_tr_b16 a[238:239], v74 offset:12288\n\t" \
  "ds_read_b64 v[80:81], v75 offset:0\n\t" \
  "v_mfma_f32_16x16x32_bf16 v[212:215], a[212:215], v[108:111], v[212:215]\n\t" \
  "ds_read_b64 v[82:83], v75 offset:512\n\t" \
  "ds_read_b64 v[84:85], v75 offset:1024\n\t" \
  "v_mfma_f32_16x16x32_bf16 v[56:59], a[212:215], v[16:19], v[56:59]\n\t" \
  "buffer_load_dwordx4 v0, s[36:39], s34 offen offset:2048 lds\n\t" \
  "v_mfma_f32_16x16x32_bf16 v[44:47], a[216:219], v[96:99], v[44:47]\n\t" \
  "ds_read_b64 v[86:87], v75 offset:1536\n\t" \
  "ds_read_b64 v[88:89], v75 offset:2048\n\t" \
  "v_mfma_f32_16x16x32_bf16 v[28:31], a[216:219], v[100:103], v[28:31]\n\t" \
  "ds_read_b64 v[90:91], v75 offset:2560\n\t" \
  "ds_read_b64 v[92:93], v75 offset:3072\n\t" \
  "v_mfma_f32_16x16x32_bf16 v[200:203], a[216:219], v[104:107], v[200:203]\n\t" \
  "ds_read_b64 v[94:95], v75 offset:3584\n\t" \
  "v_mfma_f32_16x16x32_bf16 v[216:219], a[216:219], v[108:111], v[216:219]\n\t" \
  "buffer_load_dwordx4 v0, s[36:39], s34 offen offset:3072 lds\n\t" \
  "v_mfma_f32_16x16x32_bf16 v[60:63], a[216:219], v[16:19], v[60:63]\n\t" \
  "s_add_u32 m0, s89, 0\n\t" \
  "s_add_u32 s34, s33, s89\n\t" \
  "v_mfma_f32_16x16x32_bf16 v[48:51], a[220:223], v[96:99], v[48:51]\n\t" \
  "v_mfma_f32_16x16x32_bf16 v[32:35], a[220:223], v[100:103], v[32:35]\n\t" \
  "v_mfma_f32_16x16x32_bf16 v[204:207], a[220:223], v[104:107], v[204:207]\n\t" \
  "buffer_load_dwordx4 v0, s[36:39], s34 offen lds\n\t" \
  "v_mfma_f32_16x16x32_bf16 v[220:223], a[220:223], v[108:111], v[220:223]\n\t" \
  "v_mfma_f32_16x16x32_bf16 v[64:67], a[220:223], v[16:19], v[64:67]\n\t" \
  "s_waitcnt lgkmcnt(0)\n\t" \
  "s_waitcnt vmcnt(17)\n\t" \
  "s_barrier\n\t" \
  "v_add_u32 v73, 79872, v70\n\t" \
  "v_xor_b32 v74, 32, v73\n\t" \
  "v_mfma_f32_16x16x32_bf16 v[36:39], a[224:227], v[80:83], v[36:39]\n\t" \
  "s_mul_i32 s33, s85, 18432\n\t" \
  "s_add_u32 m0, s88, 18432\n\t" \
  "s_add_u32 s34, s33, s88\n\t" \
  "v_mfma_f32_16x16x32_bf16 v[20:23], a[224:227], v[84:87], v[20:23]\n\t" \
  "buffer_load_dwordx4 v0, s[36:39], s34 offen offset:0 lds\n\t" \
  "v_mfma_f32_16x16x32_bf16 v[192:195], a[224:227], v[88:91], v[192:195]\n\t" \
  "v_add_u32 v75, 96256, v1\n\t" \
  "ds_read_b64_tr_b16 a[240:241], v73 offset:0\n\t" \
  "v_mfma_f32_16x16x32_bf16 v[208:211], a[224:227], v[92:95], v[208:211]\n\t" \
  "ds_read_b64_tr_b16 a[242:243], v73 offset:4096\n\t" \
  "ds_read_b64_tr_b16 a[244:245], v73 offset:8192\n\t" \
  "v_mfma_f32_16x16x32_bf16 v[52:55], a[224:227], v[16:19], v[52:55]\n\t" \
  "ds_read_b64_tr_b16 a[246:247], v73 offset:12288\n\t" \
  "ds_read_b64_tr_b16 a[248:249], v74 offset:0\n\t" \
  "v_mfma_f32_16x16x32_bf16 v[40:43], a[228:231], v[80:83], v[40:43]\n\t" \
  "buffer_load_dwordx4 v0, s[36:39], s34 offen offset:1024 lds\n\t" \
  "v_mfma_f32_16x16x32_bf16 v[24:27], a[228:231], v[84:87], v[24:27]\n\t" \
  "ds_read_b64_tr_b16 a[250:251], v74 offset:4096\n\t" \
  "ds_read_b64_tr_b16 a[252:253], v74 offset:8192\n\t" \
  "v_mfma_f32_16x16x32_bf16 v[196:199], a[228:231], v[88:91], v[196:199]\n\t" \
  "ds_read_b64_tr_b16 a[254:255], v74 offset:12288\n\t" \
  "ds_read_b64 v[96:97], v75 offset:0\n\t" \
  "v_mfma_f32_16x16x32_bf16 v[212:215], a[228:231], v[92:95], v[212:215]\n\t" \
  "ds_read_b64 v[98:99], v75 offset:512\n\t" \
  "ds_read_b64 v[100:101], v75 offset:1024\n\t" \
  "v_mfma_f32_16x16x32_bf16 v[56:59], a[228:231], v[16:19], v[56:59]\n\t" \
  "buffer_load_dwordx4 v0, s[36:39], s34 offen offset:2048 lds\n\t" \
  "v_mfma_f32_16x16x32_bf16 v[44:47], a[232:235], v[80:83], v[44:47]\n\t" \
  "ds_read_b64 v[102:103], v75 offset:1536\n\t" \
  "ds_read_b64 v[104:105], v75 offset:2048\n\t" \
  "v_mfma_f32_16x16x32_bf16 v[28:31], a[232:235], v[84:87], v[28:31]\n\t" \
  "ds_read_b64 v[106:107], v75 offset:2560\n\t" \
  "ds_read_b64 v[108:109], v75 offset:3072\n\t" \
  "v_mfma_f32_16x16x32_bf16 v[200:203], a[232:235], v[88:91], v[200:203]\n\t" \
  "ds_read_b64 v[110:111], v75 offset:3584\n\t" \
  "v_mfma_f32_16x16x32_bf16 v[216:219], a[232:235], v[92:95], v[216:219]\n\t" \
  "buffer_load_dwordx4 v0, s[36:39], s34 offen offset:3072 lds\n\t" \
  "v_mfma_f32_16x16x32_bf16 v[60:63], a[232:235], v[16:19], v[60:63]\n\t" \
  "s_add_u32 m0, s89, 18432\n\t" \
  "s_add_u32 s34, s33, s89\n\t" \
  "v_mfma_f32_16x16x32_bf16 v[48:51], a[236:239], v[80:83], v[48:51]\n\t" \
  "v_mfma_f32_16x16x32_bf16 v[32:35], a[236:239], v[84:87], v[32:35]\n\t" \
  "v_mfma_f32_16x16x32_bf16 v[204:207], a[236:239], v[88:91], v[204:207]\n\t" \
  "buffer_load_dwordx4 v0, s[36:39], s34 offen lds\n\t" \
  "v_mfma_f32_16x16x32_bf16 v[220:223], a[236:239], v[92:95], v[220:223]\n\t" \
  "v_mfma_f32_16x16x32_bf16 v[64:67], a[236:239], v[16:19], v[64:67]\n\t" \
  "s_waitcnt lgkmcnt(0)\n\t" \
  "s_waitcnt vmcnt(17)\n\t" \
  "s_barrier\n\t" \
  "v_add_u32 v73, 59392, v70\n\t" \
  "v_xor_b32 v74, 32, v73\n\t" \
  "v_mfma_f32_16x16x32_bf16 v[36:39], a[240:243], v[96:99], v[36:39]\n\t" \
  "s_add_u32 m0, s23, 147456\n\t" \
  "s_add_u32 s33, s22, 0\n\t" \
  "v_add_u32 v75, 75776, v1\n\t" \
  "v_mfma_f32_16x16x32_bf16 v[20:23], a[240:243], v[100:103], v[20:23]\n\t" \
  "buffer_load_dwordx4 v71, s[64:67], s33 offen lds\n\t" \
  "v_mfma_f32_16x16x32_bf16 v[192:195], a[240:243], v[104:107], v[192:195]\n\t" \
  "s_add_u32 m0, s23, 151552\n\t" \
  "s_add_u32 s33, s22, 663552\n\t" \
  "ds_read_b64_tr_b16 v[224:225], v73 offset:0\n\t" \
  "v_mfma_f32_16x16x32_bf16 v[208:211], a[240:243], v[108:111], v[208:211]\n\t" \
  "ds_read_b64_tr_b16 v[226:227], v73 offset:4096\n\t" \
  "ds_read_b64_tr_b16 v[228:229], v73 offset:8192\n\t" \
  "v_mfma_f32_16x16x32_bf16 v[52:55], a[240:243], v[16:19], v[52:55]\n\t" \
  "buffer_load_dwordx4 v71, s[64:67], s33 offen lds\n\t" \
  "v_mfma_f32_16x16x32_bf16 v[40:43], a[244:247], v[96:99], v[40:43]\n\t" \
  "s_add_u32 m0, s23, 131072\n\t" \
  "s_add_u32 s33, s22, 36864\n\t" \
  "ds_read_b64_tr_b16 v[230:231], v73 offset:12288\n\t" \
  "v_mfma_f32_16x16x32_bf16 v[24:27], a[244:247], v[100:103], v[24:27]\n\t" \
  "ds_read_b64_tr_b16 v[232:233], v74 offset:0\n\t" \
  "ds_read_b64_tr_b16 v[234:235], v74 offset:4096\n\t" \
  "v_mfma_f32_16x16x32_bf16 v[196:199], a[244:247], v[104:107], v[196:199]\n\t" \
  "buffer_load_dwordx4 v71, s[64:67], s33 offen lds\n\t" \
  "v_mfma_f32_16x16x32_bf16 v[212:215], a[244:247], v[108:111], v[212:215]\n\t" \
  "s_add_u32 m0, s23, 135168\n\t" \
  "s_add_u32 s33, s22, 700416\n\t" \
  "ds_read_b64_tr_b16 v[236:237], v74 offset:8192\n\t" \
  "v_mfma_f32_16x16x32_bf16 v[56:59], a[244:247], v[16:19], v[56:59]\n\t" \
  "ds_read_b64_tr_b16 v[238:239], v74 offset:12288\n\t" \
  "ds_read_b64 v[80:81], v75 offset:0\n\t" \
  "v_mfma_f32_16x16x32_bf16 v[44:47], a[248:251], v[96:99], v[44:47]\n\t" \
  "buffer_load_dwordx4 v71, s[64:67], s33 offen lds\n\t" \
  "v_mfma_f32_16x16x32_bf16 v[28:31], a[248:251], v[100:103], v[28:31]\n\t" \
  "s_add_u32 m0, s23, 139264\n\t" \
  "s_add_u32 s33, s22, 73728\n\t" \
  "ds_read_b64 v[82:83], v75 offset:512\n\t" \
  "v_mfma_f32_16x16x32_bf16 v[200:203], a[248:251], v[104:107], v[200:203]\n\t" \
  "ds_read_b64 v[84:85], v75 offset:1024\n\t" \
  "ds_read_b64 v[86:87], v75 offset:1536\n\t" \
  "v_mfma_f32_16x16x32_bf16 v[216:219], a[248:251], v[108:111], v[216:219]\n\t" \
  "buffer_load_dwordx4 v71, s[64:67], s33 offen lds\n\t" \
  "v_mfma_f32_16x16x32_bf16 v[60:63], a[248:251], v[16:19], v[60:63]\n\t" \
  "s_add_u32 m0, s23, 143360\n\t" \
  "s_add_u32 s33, s22, 737280\n\t" \
  "ds_read_b64 v[88:89], v75 offset:2048\n\t" \
  "v_mfma_f32_16x16x32_bf16 v[48:51], a[252:255], v[96:99], v[48:51]\n\t" \
  "ds_read_b64 v[90:91], v75 offset:2560\n\t" \
  "ds_read_b64 v[92:93], v75 offset:3072\n\t" \
  "ds_read_b64 v[94:95], v75 offset:3584\n\t" \
  "v_mfma_f32_16x16x32_bf16 v[32:35], a[252:255], v[100:103], v[32:35]\n\t" \
  "buffer_load_dwordx4 v71, s[64:67], s33 offen lds\n\t" \
  "v_mfma_f32_16x16x32_bf16 v[204:207], a[252:255], v[104:107], v[204:207]\n\t" \
  "v_mfma_f32_16x16x32_bf16 v[220:223], a[252:255], v[108:111], v[220:223]\n\t" \
  "v_mfma_f32_16x16x32_bf16 v[64:67], a[252:255], v[16:19], v[64:67]\n\t" \
  "s_mov_b32 s21, 40000\n\t" \
  "s_waitcnt vmcnt(21)\n\t" \
  "SG_%=:\n\t" \
  "v_cmp_ne_u32 vcc, s74, v113\n\t" \
  "v_cmp_ne_u32 s[34:35], s74, v115\n\t" \
  "s_or_b64 vcc, vcc, s[34:35]\n\t" \
  "s_cbranch_vccz SD_%=\n\t" \
  "s_sub_u32 s21, s21, 1\n\t" \
  "s_cmp_eq_u32 s21, 0\n\t" \
  "s_cbranch_scc1 SD_%=\n\t" \
  "buffer_load_dwordx2 v[112:113], v4, s[68:71], 0 offen sc1\n\t" \
  "buffer_load_dwordx2 v[114:115], v5, s[68:71], 0 offen sc1\n\t" \
  "s_waitcnt vmcnt(0)\n\t" \
  "s_branch SG_%=\n\t" \
  "SD_%=:\n\t" \
  "v_and_b32 v113, 0xffff0000, v112\n\t" \
  "v_lshlrev_b32 v112, 16, v112\n\t" \
  "v_mul_f32 v113, v113, v113\n\t" \
  "v_xor_b32 v112, 0x80000000, v112\n\t" \
  "v_rcp_f32 v113, v113\n\t" \
  "s_nop 0\n\t" \
  "ds_write_b64 v6, v[112:113] offset:0\n\t" \
  "v_and_b32 v115, 0xffff0000, v114\n\t" \
  "v_lshlrev_b32 v114, 16, v114\n\t" \
  "v_mul_f32 v115, v115, v115\n\t" \
  "v_xor_b32 v114, 0x80000000, v114\n\t" \
  "v_rcp_f32 v115, v115\n\t" \
  "s_nop 0\n\t" \
  "ds_write_b64 v6, v[114:115] offset:2048\n\t" \
  "s_waitcnt lgkmcnt(0)\n\t" \
  "s_waitcnt vmcnt(16)\n\t" \
  "s_barrier\n\t" \
  "v_add_u32 v73, 38912, v70\n\t" \
  "v_xor_b32 v74, 32, v73\n\t" \
  "v_mfma_f32_16x16x32_bf16 v[36:39], v[224:227], v[80:83], v[36:39]\n\t" \
  "s_add_u32 m0, s23, 114688\n\t" \
  "s_add_u32 s33, s22, 110592\n\t" \
  "v_add_u32 v75, 55296, v1\n\t" \
  "v_mfma_f32_16x16x32_bf16 v[20:23], v[224:227], v[84:87], v[20:23]\n\t" \
  "buffer_load_dwordx4 v71, s[64:67], s33 offen lds\n\t" \
  "v_mfma_f32_16x16x32_bf16 v[192:195], v[224:227], v[88:91], v[192:195]\n\t" \
  "s_add_u32 m0, s23, 118784\n\t" \
  "s_add_u32 s33, s22, 774144\n\t" \
  "ds_read_b64_tr_b16 v[240:241], v73 offset:0\n\t" \
  "v_mfma_f32_16x16x32_bf16 v[208:211], v[224:227], v[92:95], v[208:211]\n\t" \
  "ds_read_b64_tr_b16 v[242:243], v73 offset:4096\n\t" \
  "ds_read_b64_tr_b16 v[244:245], v73 offset:8192\n\t" \
  "v_mfma_f32_16x16x32_bf16 v[52:55], v[224:227], v[16:19], v[52:55]\n\t" \
  "buffer_load_dwordx4 v71, s[64:67], s33 offen lds\n\t" \
  "v_mfma_f32_16x16x32_bf16 v[40:43], v[228:231], v[80:83], v[40:43]\n\t" \
  "s_add_u32 m0, s23, 122880\n\t" \
  "s_add_u32 s33, s22, 147456\n\t" \
  "ds_read_b64_tr_b16 v[246:247], v73 offset:12288\n\t" \
  "v_mfma_f32_16x16x32_bf16 v[24:27], v[228:231], v[84:87], v[24:27]\n\t" \
  "ds_read_b64_tr_b16 v[248:249], v74 offset:0\n\t" \
  "ds_read_b64_tr_b16 v[250:251], v74 offset:4096\n\t" \
  "v_mfma_f32_16x16x32_bf16 v[196:199], v[228:231], v[88:91], v[196:199]\n\t" \
  "buffer_load_dwordx4 v71, s[64:67], s33 offen lds\n\t" \
  "v_mfma_f32_16x16x32_bf16 v[212:215], v[228:231], v[92:95], v[212:215]\n\t" \
  "s_add_u32 m0, s23, 126976\n\t" \
  "s_add_u32 s33, s22, 811008\n\t" \
  "ds_read_b64_tr_b16 v[252:253], v74 offset:8192\n\t" \
  "v_mfma_f32_16x16x32_bf16 v[56:59], v[228:231], v[16:19], v[56:59]\n\t" \
  "ds_read_b64_tr_b16 v[254:255], v74 offset:12288\n\t" \
  "ds_read_b64 v[96:97], v75 offset:0\n\t" \
  "v_mfma_f32_16x16x32_bf16 v[44:47], v[232:235], v[80:83], v[44:47]\n\t" \
  "buffer_load_dwordx4 v71, s[64:67], s33 offen lds\n\t" \
  "v_mfma_f32_16x16x32_bf16 v[28:31], v[232:235], v[84:87], v[28:31]\n\t" \
  "ds_read_b64 v[98:99], v75 offset:512\n\t" \
  "ds_read_b64 v[100:101], v75 offset:1024\n\t" \
  "v_mfma_f32_16x16x32_bf16 v[200:203], v[232:235], v[88:91], v[200:203]\n\t" \
  "ds_read_b64 v[102:103], v75 offset:1536\n\t" \
  "ds_read_b64 v[104:105], v75 offset:2048\n\t" \
  "v_mfma_f32_16x16x32_bf16 v[216:219], v[232:235], v[92:95], v[216:219]\n\t" \
  "ds_read_b64 v[106:107], v75 offset:2560\n\t" \
  "ds_read_b64 v[108:109], v75 offset:3072\n\t" \
  "v_mfma_f32_16x16x32_bf16 v[60:63], v[232:235], v[16:19], v[60:63]\n\t" \
  "ds_read_b64 v[110:111], v75 offset:3584\n\t" \
  "v_mfma_f32_16x16x32_bf16 v[48:51], v[236:239], v[80:83], v[48:51]\n\t" \
  "v_mfma_f32_16x16x32_bf16 v[32:35], v[236:239], v[84:87], v[32:35]\n\t" \
  "v_mfma_f32_16x16x32_bf16 v[204:207], v[236:239], v[88:91], v[204:207]\n\t" \
  "v_mfma_f32_16x16x32_bf16 v[220:223], v[236:239], v[92:95], v[220:223]\n\t" \
  "v_mfma_f32_16x16x32_bf16 v[64:67], v[236:239], v[16:19], v[64:67]\n\t" \
  "s_waitcnt lgkmcnt(0)\n\t" \
  "v_mfma_f32_16x16x32_bf16 v[36:39], v[240:243], v[96:99], v[36:39]\n\t" \
  "v_mfma_f32_16x16x32_bf16 v[20:23], v[240:243], v[100:103], v[20:23]\n\t" \
  "v_mfma_f32_16x16x32_bf16 v[192:195], v[240:243], v[104:107], v[192:195]\n\t" \
  "v_mfma_f32_16x16x32_bf16 v[208:211], v[240:243], v[108:111], v[208:211]\n\t" \
  "v_mfma_f32_16x16x32_bf16 v[52:55], v[240:243], v[16:19], v[52:55]\n\t" \
  "v_mfma_f32_16x16x32_bf16 v[40:43], v[244:247], v[96:99], v[40:43]\n\t" \
  "v_mfma_f32_16x16x32_bf16 v[24:27], v[244:247], v[100:103], v[24:27]\n\t" \
  "v_mfma_f32_16x16x32_bf16 v[196:199], v[244:247], v[104:107], v[196:199]\n\t" \
  "v_mfma_f32_16x16x32_bf16 v[212:215], v[244:247], v[108:111], v[212:215]\n\t" \
  "v_mfma_f32_16x16x32_bf16 v[56:59], v[244:247], v[16:19], v[56:59]\n\t" \
  "v_mfma_f32_16x16x32_bf16 v[44:47], v[248:251], v[96:99], v[44:47]\n\t" \
  "v_mfma_f32_16x16x32_bf16 v[28:31], v[248:251], v[100:103], v[28:31]\n\t" \
  "s_add_u32 s48, s94, 25165824\n\t" \
  "s_addc_u32 s49, s95, 0\n\t" \
  "s_and_b32 s49, s49, 0xffff\n\t" \
  "s_mov_b32 s50, 0\n\t" \
  "s_mov_b32 s51, 0x20000\n\t" \
  "s_add_u32 s52, s94, 25165824\n\t" \
  "s_addc_u32 s53, s95, 0\n\t" \
  "s_and_b32 s53, s53, 0xffff\n\t" \
  "s_mov_b32 s54, 0\n\t" \
  "s_mov_b32 s55, 0x20000\n\t" \
  "s_mov_b32 s97, 144\n\t" \
  "s_waitcnt vmcnt(15)\n\t" \
  "s_barrier\n\t" \
  "ds_read2st64_b64 v[12:15], v1 offset0:0 offset1:1\n\t" \
  "ds_read2st64_b64 v[4:7], v1 offset0:2 offset1:3\n\t" \
  "v_mfma_f32_16x16x32_bf16 v[200:203], v[248:251], v[104:107], v[200:203]\n\t" \
  "v_mfma_f32_16x16x32_bf16 v[216:219], v[248:251], v[108:111], v[216:219]\n\t" \
  "v_mfma_f32_16x16x32_bf16 v[60:63], v[248:251], v[16:19], v[60:63]\n\t" \
  "v_mfma_f32_16x16x32_bf16 v[48:51], v[252:255], v[96:99], v[48:51]\n\t" \
  "s_mul_i32 s35, s83, 147456\n\t" \
  "s_add_u32 s35, s35, s91\n\t" \
  "v_mfma_f32_16x16x32_bf16 v[32:35], v[252:255], v[100:103], v[32:35]\n\t" \
  "v_mfma_f32_16x16x32_bf16 v[204:207], v[252:255], v[104:107], v[204:207]\n\t" \
  "v_mfma_f32_16x16x32_bf16 v[220:223], v[252:255], v[108:111], v[220:223]\n\t" \
  "v_mfma_f32_16x16x32_bf16 v[64:67], v[252:255], v[16:19], v[64:67]\n\t" \
  "ds_read2st64_b64 v[8:11], v1 offset0:4 offset1:5\n\t" \
  "s_waitcnt lgkmcnt(2)\n\t" \
  "v_mfma_f32_16x16x32_bf16 v[176:179], a[0:3], v[12:15], 0\n\t" \
  "v_mfma_f32_16x16x32_bf16 v[180:183], a[4:7], v[12:15], 0\n\t" \
  "v_mfma_f32_16x16x32_bf16 v[184:187], a[8:11], v[12:15], 0\n\t" \
  "v_mfma_f32_16x16x32_bf16 v[188:191], a[12:15], v[12:15], 0\n\t" \
  "ds_read2st64_b64 v[12:15], v1 offset0:6 offset1:7\n\t" \
  "s_waitcnt lgkmcnt(2)\n\t" \
  "v_mfma_f32_16x16x32_bf16 v[176:179], a[16:19], v[4:7], v[176:179]\n\t" \
  "v_mfma_f32_16x16x32_bf16 v[180:183], a[20:23], v[4:7], v[180:183]\n\t" \
  "ds_read_b32 v3, v76 offset:0\n\t" \
  "ds_write_b128 v77, v[52:55] offset:0\n\t" \
  "v_mfma_f32_16x16x32_bf16 v[184:187], a[24:27], v[4:7], v[184:187]\n\t" \
  "s_waitcnt lgkmcnt(1)\n\t" \
  "v_fmac_f32 v36, v3, v52\n\t" \
  "v_fmac_f32 v37, v3, v53\n\t" \
  "v_mfma_f32_16x16x32_bf16 v[188:191], a[28:31], v[4:7], v[188:191]\n\t" \
  "ds_read2st64_b64 v[4:7], v1 offset0:8 offset1:9\n\t" \
  "v_fmac_f32 v38, v3, v54\n\t" \
  "v_mfma_f32_16x16x32_bf16 v[176:179], a[32:35], v[8:11], v[176:179]\n\t" \
  "v_fmac_f32 v39, v3, v55\n\t" \
  "v_fmac_f32 v40, v3, v56\n\t" \
  "v_mfma_f32_16x16x32_bf16 v[180:183], a[36:39], v[8:11], v[180:183]\n\t" \
  "v_fmac_f32 v41, v3, v57\n\t" \
  "v_fmac_f32 v42, v3, v58\n\t" \
  "v_mfma_f32_16x16x32_bf16 v[184:187], a[40:43], v[8:11], v[184:187]\n\t" \
  "v_fmac_f32 v43, v3, v59\n\t" \
  "v_fmac_f32 v44, v3, v60\n\t" \
  "v_mfma_f32_16x16x32_bf16 v[188:191], a[44:47], v[8:11], v[188:191]\n\t" \
  "ds_read2st64_b64 v[8:11], v1 offset0:10 offset1:11\n\t" \
  "v_fmac_f32 v45, v3, v61\n\t" \
  "v_mfma_f32_16x16x32_bf16 v[176:179], a[48:51], v[12:15], v[176:179]\n\t" \
  "v_fmac_f32 v46, v3, v62\n\t" \
  "v_fmac_f32 v47, v3, v63\n\t" \
  "v_mfma_f32_16x16x32_bf16 v[180:183], a[52:55], v[12:15], v[180:183]\n\t" \
  "v_fmac_f32 v48, v3, v64\n\t" \
  "v_fmac_f32 v49, v3, v65\n\t" \
  "v_mfma_f32_16x16x32_bf16 v[184:187], a[56:59], v[12:15], v[184:187]\n\t" \
  "v_fmac_f32 v50, v3, v66\n\t" \
  "v_fmac_f32 v51, v3, v67\n\t" \
  "v_mfma_f32_16x16x32_bf16 v[188:191], a[60:63], v[12:15], v[188:191]\n\t" \
  "ds_read2st64_b64 v[12:15], v1 offset0:12 offset1:13\n\t" \
  "v_max_f32 v36, 0, v36\n\t" \
  "s_waitcnt lgkmcnt(2)\n\t" \
  "v_mfma_f32_16x16x32_bf16 v[176:179], a[64:67], v[4:7], v[176:179]\n\t" \
  "v_max_f32 v37, 0, v37\n\t" \
  "v_max_f32 v38, 0, v38\n\t" \
  "v_mfma_f32_16x16x32_bf16 v[180:183], a[68:71], v[4:7], v[180:183]\n\t" \
  "v_max_f32 v39, 0, v39\n\t" \
  "v_mul_f32 v36, v36, v40\n\t" \
  "v_mfma_f32_16x16x32_bf16 v[184:187], a[72:75], v[4:7], v[184:187]\n\t" \
  "v_mul_f32 v37, v37, v41\n\t" \
  "v_mul_f32 v38, v38, v42\n\t" \
  "v_mfma_f32_16x16x32_bf16 v[188:191], a[76:79], v[4:7], v[188:191]\n\t" \
  "ds_read2st64_b64 v[4:7], v1 offset0:14 offset1:15\n\t" \
  "v_mul_f32 v39, v39, v43\n\t" \
  "s_waitcnt lgkmcnt(2)\n\t" \
  "v_mfma_f32_16x16x32_bf16 v[176:179], a[80:83], v[8:11], v[176:179]\n\t" \
  "v_max_f32 v44, 0, v44\n\t" \
  "v_max_f32 v45, 0, v45\n\t" \
  "v_mfma_f32_16x16x32_bf16 v[180:183], a[84:87], v[8:11], v[180:183]\n\t" \
  "v_max_f32 v46, 0, v46\n\t" \
  "v_max_f32 v47, 0, v47\n\t" \
  "v_mfma_f32_16x16x32_bf16 v[184:187], a[88:91], v[8:11], v[184:187]\n\t" \
  "v_mul_f32 v44, v44, v48\n\t" \
  "v_mul_f32 v45, v45, v49\n\t" \
  "v_mfma_f32_16x16x32_bf16 v[188:191], a[92:95], v[8:11], v[188:191]\n\t" \
  "ds_read2st64_b64 v[8:11], v1 offset0:16 offset1:17\n\t" \
  "v_mul_f32 v46, v46, v50\n\t" \
  "s_waitcnt lgkmcnt(2)\n\t" \
  "v_mfma_f32_16x16x32_bf16 v[176:179], a[96:99], v[12:15], v[176:179]\n\t" \
  "v_mul_f32 v47, v47, v51\n\t" \
  "v_cvt_pk_bf16_f32 v40, v36, v37\n\t" \
  "v_mfma_f32_16x16x32_bf16 v[180:183], a[100:103], v[12:15], v[180:183]\n\t" \
  "v_cvt_pk_bf16_f32 v41, v38, v39\n\t" \
  "v_cvt_pk_bf16_f32 v42, v44, v45\n\t" \
  "v_mfma_f32_16x16x32_bf16 v[184:187], a[104:107], v[12:15], v[184:187]\n\t" \
  "v_cvt_pk_bf16_f32 v43, v46, v47\n\t" \
  "ds_write_b128 v77, v[56:59] offset:64\n\t" \
  "v_mfma_f32_16x16x32_bf16 v[188:191], a[108:111], v[12:15], v[188:191]\n\t" \
  "ds_read2st64_b64 v[12:15], v1 offset0:18 offset1:19\n\t" \
  "ds_read_b32 v3, v76 offset:128\n\t" \
  "s_waitcnt lgkmcnt(4)\n\t" \
  "v_mfma_f32_16x16x32_bf16 v[176:179], a[112:115], v[4:7], v[176:179]\n\t" \
  "buffer_store_dwordx4 v[40:43], v0, s[44:47], s35 offen sc1\n\t" \
  "v_mfma_f32_16x16x32_bf16 v[180:183], a[116:119], v[4:7], v[180:183]\n\t" \
  "s_add_u32 m0, s23, 36864\n\t" \
  "s_add_u32 s33, s22, 184320\n\t" \
  "s_waitcnt lgkmcnt(0)\n\t" \
  "v_fmac_f32 v20, v3, v52\n\t" \
  "v_mfma_f32_16x16x32_bf16 v[184:187], a[120:123], v[4:7], v[184:187]\n\t" \
  "s_mul_i32 s35, s82, 147456\n\t" \
  "v_fmac_f32 v21, v3, v53\n\t" \
  "v_fmac_f32 v22, v3, v54\n\t" \
  "ds_read_b32 v17, v76 offset:640\n\t" \
  "v_mfma_f32_16x16x32_bf16 v[188:191], a[124:127], v[4:7], v[188:191]\n\t" \
  "s_add_u32 s35, s35, s91\n\t" \
  "ds_read2st64_b64 v[4:7], v1 offset0:20 offset1:21\n\t" \
  "v_fmac_f32 v23, v3, v55\n\t" \
  "s_waitcnt lgkmcnt(1)\n\t" \
  "v_mul_f32 v36, v17, v52\n\t" \
  "v_mfma_f32_16x16x32_bf16 v[176:179], a[128:131], v[8:11], v[176:179]\n\t" \
  "buffer_load_dwordx4 v71, s[64:67], s33 offen lds\n\t" \
  "v_mul_f32 v37, v17, v53\n\t" \
  "v_mfma_f32_16x16x32_bf16 v[180:183], a[132:135], v[8:11], v[180:183]\n\t" \
  "s_add_u32 m0, s23, 40960\n\t" \
  "s_add_u32 s33, s22, 847872\n\t" \
  "v_fmac_f32 v24, v3, v56\n\t" \
  "v_mul_f32 v38, v17, v54\n\t" \
  "v_mfma_f32_16x16x32_bf16 v[184:187], a[136:139], v[8:11], v[184:187]\n\t" \
  "v_fmac_f32 v25, v3, v57\n\t" \
  "v_fmac_f32 v26, v3, v58\n\t" \
  "v_mul_f32 v39, v17, v55\n\t" \
  "v_mfma_f32_16x16x32_bf16 v[188:191], a[140:143], v[8:11], v[188:191]\n\t" \
  "ds_read2st64_b64 v[8:11], v1 offset0:22 offset1:23\n\t" \
  "v_fmac_f32 v27, v3, v59\n\t" \
  "v_mul_f32 v40, v17, v56\n\t" \
  "v_mfma_f32_16x16x32_bf16 v[176:179], a[144:147], v[12:15], v[176:179]\n\t" \
  "buffer_load_dwordx4 v71, s[64:67], s33 offen lds\n\t" \
  "v_mul_f32 v41, v17, v57\n\t" \
  "v_mfma_f32_16x16x32_bf16 v[180:183], a[148:151], v[12:15], v[180:183]\n\t" \
  "s_add_u32 m0, s23, 45056\n\t" \
  "s_add_u32 s33, s22, 221184\n\t" \
  "v_fmac_f32 v28, v3, v60\n\t" \
  "v_mul_f32 v42, v17, v58\n\t" \
  "v_mfma_f32_16x16x32_bf16 v[184:187], a[152:155], v[12:15], v[184:187]\n\t" \
  "v_fmac_f32 v29, v3, v61\n\t" \
  "v_fmac_f32 v30, v3, v62\n\t" \
  "v_mul_f32 v43, v17, v59\n\t" \
  "v_mfma_f32_16x16x32_bf16 v[188:191], a[156:159], v[12:15], v[188:191]\n\t" \
  "ds_read2st64_b64 v[12:15], v1 offset0:24 offset1:25\n\t" \
  "v_fmac_f32 v31, v3, v63\n\t" \
  "v_mul_f32 v44, v17, v60\n\t" \
  "s_waitcnt lgkmcnt(2)\n\t" \
  "v_mfma_f32_16x16x32_bf16 v[176:179], a[160:163], v[4:7], v[176:179]\n\t" \
  "buffer_load_dwordx4 v71, s[64:67], s33 offen lds\n\t" \
  "v_mul_f32 v45, v17, v61\n\t" \
  "v_mfma_f32_16x16x32_bf16 v[180:183], a[164:167], v[4:7], v[180:183]\n\t" \
  "s_add_u32 m0, s23, 49152\n\t" \
  "s_add_u32 s33, s22, 884736\n\t" \
  "v_fmac_f32 v32, v3, v64\n\t" \
  "v_mul_f32 v46, v17, v62\n\t" \
  "v_mfma_f32_16x16x32_bf16 v[184:187], a[168:171], v[4:7], v[184:187]\n\t" \
  "v_fmac_f32 v33, v3, v65\n\t" \
  "v_fmac_f32 v34, v3, v66\n\t" \
  "v_mul_f32 v47, v17, v63\n\t" \
  "v_mfma_f32_16x16x32_bf16 v[188:191], a[172:175], v[4:7], v[188:191]\n\t" \
  "ds_read2st64_b64 v[4:7], v1 offset0:26 offset1:27\n\t" \
  "v_fmac_f32 v35, v3, v67\n\t" \
  "v_mul_f32 v48, v17, v64\n\t" \
  "s_waitcnt lgkmcnt(2)\n\t" \
  "v_mfma_f32_16x16x32_bf16 v[176:179], a[176:179], v[8:11], v[176:179]\n\t" \
  "buffer_load_dwordx4 v71, s[64:67], s33 offen lds\n\t" \
  "v_mul_f32 v49, v17, v65\n\t" \
  "v_mfma_f32_16x16x32_bf16 v[180:183], a[180:183], v[8:11], v[180:183]\n\t" \
  "s_add_u32 m0, s23, 53248\n\t" \
  "s_add_u32 s33, s22, 258048\n\t" \
  "v_max_f32 v20, 0, v20\n\t" \
  "v_mul_f32 v50, v17, v66\n\t" \
  "v_mfma_f32_16x16x32_bf16 v[184:187], a[184:187], v[8:11], v[184:187]\n\t" \
  "v_max_f32 v21, 0, v21\n\t" \
  "v_max_f32 v22, 0, v22\n\t" \
  "v_mul_f32 v51, v17, v67\n\t" \
  "v_mfma_f32_16x16x32_bf16 v[188:191], a[188:191], v[8:11], v[188:191]\n\t" \
  "ds_read2st64_b64 v[8:11], v1 offset0:28 offset1:29\n\t" \
  "v_max_f32 v23, 0, v23\n\t" \
  "s_waitcnt lgkmcnt(2)\n\t" \
  "v_mfma_f32_16x16x32_bf16 v[176:179], a[192:195], v[12:15], v[176:179]\n\t" \
  "buffer_load_dwordx4 v71, s[64:67], s33 offen lds\n\t" \
  "v_mfma_f32_16x16x32_bf16 v[180:183], a[196:199], v[12:15], v[180:183]\n\t" \
  "s_add_u32 m0, s23, 57344\n\t" \
  "s_add_u32 s33, s22, 921600\n\t" \
  "v_mul_f32 v20, v20, v24\n\t" \
  "v_mfma_f32_16x16x32_bf16 v[184:187], a[200:203], v[12:15], v[184:187]\n\t" \
  "v_mul_f32 v21, v21, v25\n\t" \
  "v_mul_f32 v22, v22, v26\n\t" \
  "v_mfma_f32_16x16x32_bf16 v[188:191], a[204:207], v[12:15], v[188:191]\n\t" \
  "ds_read2st64_b64 v[12:15], v1 offset0:30 offset1:31\n\t" \
  "v_mul_f32 v23, v23, v27\n\t" \
  "s_waitcnt lgkmcnt(2)\n\t" \
  "v_mfma_f32_16x16x32_bf16 v[176:179], a[208:211], v[4:7], v[176:179]\n\t" \
  "buffer_load_dwordx4 v71, s[64:67], s33 offen lds\n\t" \
  "v_mfma_f32_16x16x32_bf16 v[180:183], a[212:215], v[4:7], v[180:183]\n\t" \
  "s_add_u32 m0, s23, 61440\n\t" \
  "s_add_u32 s33, s22, 294912\n\t" \
  "v_max_f32 v28, 0, v28\n\t" \
  "v_mfma_f32_16x16x32_bf16 v[184:187], a[216:219], v[4:7], v[184:187]\n\t" \
  "v_max_f32 v29, 0, v29\n\t" \
  "v_max_f32 v30, 0, v30\n\t" \
  "v_mfma_f32_16x16x32_bf16 v[188:191], a[220:223], v[4:7], v[188:191]\n\t" \
  "ds_read2st64_b64 v[4:7], v1 offset0:32 offset1:33\n\t" \
  "v_max_f32 v31, 0, v31\n\t" \
  "s_waitcnt lgkmcnt(2)\n\t" \
  "v_mfma_f32_16x16x32_bf16 v[176:179], a[224:227], v[8:11], v[176:179]\n\t" \
  "buffer_load_dwordx4 v71, s[64:67], s33 offen lds\n\t" \
  "v_mfma_f32_16x16x32_bf16 v[180:183], a[228:231], v[8:11], v[180:183]\n\t" \
  "s_add_u32 m0, s23, 65536\n\t" \
  "s_add_u32 s33, s22, 958464\n\t" \
  "v_mul_f32 v28, v28, v32\n\t" \
  "v_mfma_f32_16x16x32_bf16 v[184:187], a[232:235], v[8:11], v[184:187]\n\t" \
  "v_mul_f32 v29, v29, v33\n\t" \
  "v_mul_f32 v30, v30, v34\n\t" \
  "v_mfma_f32_16x16x32_bf16 v[188:191], a[236:239], v[8:11], v[188:191]\n\t" \
  "ds_read2st64_b64 v[8:11], v1 offset0:34 offset1:35\n\t" \
  "v_mul_f32 v31, v31, v35\n\t" \
  "s_waitcnt lgkmcnt(2)\n\t" \
  "v_mfma_f32_16x16x32_bf16 v[176:179], a[240:243], v[12:15], v[176:179]\n\t" \
  "buffer_load_dwordx4 v71, s[64:67], s33 offen lds\n\t" \
  "v_mfma_f32_16x16x32_bf16 v[180:183], a[244:247], v[12:15], v[180:183]\n\t" \
  "s_add_u32 m0, s23, 106496\n\t" \
  "s_add_u32 s33, s22, 331776\n\t" \
  "v_cvt_pk_bf16_f32 v24, v20, v21\n\t" \
  "v_xor_b32 v1, 18432, v1\n\t" \
  "v_mfma_f32_16x16x32_bf16 v[184:187], a[248:251], v[12:15], v[184:187]\n\t" \
  "v_cvt_pk_bf16_f32 v25, v22, v23\n\t" \
  "v_cvt_pk_bf16_f32 v26, v28, v29\n\t" \
  "v_mfma_f32_16x16x32_bf16 v[188:191], a[252:255], v[12:15], v[188:191]\n\t" \
  "v_cvt_pk_bf16_f32 v27, v30, v31\n\t" \
  "ds_write_b128 v77, v[60:63] offset:128\n\t" \
  "s_waitcnt lgkmcnt(2)\n\t" \
  "v_mfma_f32_16x16x32_bf16 v[176:179], v[224:227], v[4:7], v[176:179]\n\t" \
  "buffer_load_dwordx4 v71, s[64:67], s33 offen lds\n\t" \
  "v_mfma_f32_16x16x32_bf16 v[180:183], v[228:231], v[4:7], v[180:183]\n\t" \
  "s_add_u32 m0, s23, 110592\n\t" \
  "s_add_u32 s33, s22, 995328\n\t" \
  "ds_write_b128 v77, v[64:67] offset:192\n\t" \
  "v_mfma_f32_16x16x32_bf16 v[184:187], v[232:235], v[4:7], v[184:187]\n\t" \
  "buffer_store_dwordx4 v[24:27], v0, s[44:47], s35 offen sc1\n\t" \
  "v_mfma_f32_16x16x32_bf16 v[188:191], v[236:239], v[4:7], v[188:191]\n\t" \
  "buffer_load_dwordx4 v71, s[64:67], s33 offen lds\n\t" \
  "s_waitcnt vmcnt(11) lgkmcnt(0)\n\t" \
  "s_barrier\n\t" \
  "ds_read2st64_b64 v[12:15], v1 offset0:0 offset1:1\n\t" \
  "ds_read2st64_b64 v[4:7], v1 offset0:2 offset1:3\n\t" \
  "v_mfma_f32_16x16x32_bf16 v[176:179], v[240:243], v[8:11], v[176:179]\n\t" \
  "s_mul_i32 s33, s72, 18432\n\t" \
  "s_lshl_b32 s18, s83, 9\n\t" \
  "s_add_u32 s18, s18, s92\n\t" \
  "v_mfma_f32_16x16x32_bf16 v[180:183], v[244:247], v[8:11], v[180:183]\n\t" \
  "s_mov_b64 exec, 2\n\t" \
  "buffer_atomic_add v0, v0, s[56:59], s18 offen\n\t" \
  "s_mov_b64 exec, -1\n\t" \
  "v_mfma_f32_16x16x32_bf16 v[184:187], v[248:251], v[8:11], v[184:187]\n\t" \
  "s_add_u32 m0, s88, 0\n\t" \
  "s_add_u32 s34, s33, s88\n\t" \
  "v_mfma_f32_16x16x32_bf16 v[188:191], v[252:255], v[8:11], v[188:191]\n\t" \
  "s_mul_i32 s35, s81, 147456\n\t" \
  "ds_read2st64_b64 v[8:11], v1 offset0:4 offset1:5\n\t" \
  "s_add_u32 s35, s35, s91\n\t" \
  "s_waitcnt lgkmcnt(2)\n\t" \
  "v_mfma_f32_16x16x32_bf16 v[36:39], a[0:3], v[12:15], v[36:39]\n\t" \
  "buffer_load_dwordx4 v0, s[36:39], s34 offen offset:0 lds\n\t" \
  "v_mfma_f32_16x16x32_bf16 v[40:43], a[4:7], v[12:15], v[40:43]\n\t" \
  "v_mfma_f32_16x16x32_bf16 v[44:47], a[8:11], v[12:15], v[44:47]\n\t" \
  "v_mfma_f32_16x16x32_bf16 v[48:51], a[12:15], v[12:15], v[48:51]\n\t" \
  "ds_read2st64_b64 v[12:15], v1 offset0:6 offset1:7\n\t" \
  "v_add_u32 v73, 147456, v72\n\t" \
  "s_waitcnt lgkmcnt(2)\n\t" \
  "v_mfma_f32_16x16x32_bf16 v[36:39], a[16:19], v[4:7], v[36:39]\n\t" \
  "v_xor_b32 v74, 32, v73\n\t" \
  "ds_read_b64_tr_b16 v[80:81], v73 offset:0\n\t" \
  "ds_read_b64_tr_b16 v[82:83], v73 offset:2048\n\t" \
  "v_mfma_f32_16x16x32_bf16 v[40:43], a[20:23], v[4:7], v[40:43]\n\t" \
  "buffer_load_dwordx4 v0, s[36:39], s34 offen offset:1024 lds\n\t" \
  "ds_read_b64_tr_b16 v[84:85], v74 offset:0\n\t" \
  "v_mfma_f32_16x16x32_bf16 v[44:47], a[24:27], v[4:7], v[44:47]\n\t" \
  "ds_read_b32 v3, v76 offset:256\n\t" \
  "ds_read_b32 v17, v76 offset:768\n\t" \
  "ds_read_b64_tr_b16 v[86:87], v74 offset:2048\n\t" \
  "v_mfma_f32_16x16x32_bf16 v[48:51], a[28:31], v[4:7], v[48:51]\n\t" \
  "ds_read2st64_b64 v[4:7], v1 offset0:8 offset1:9\n\t" \
  "s_waitcnt lgkmcnt(3)\n\t" \
  "v_fmac_f32 v192, v3, v52\n\t" \
  "s_waitcnt lgkmcnt(2)\n\t" \
  "v_mul_f32 v20, v17, v52\n\t" \
  "v_mfma_f32_16x16x32_bf16 v[36:39], a[32:35], v[8:11], v[36:39]\n\t" \
  "v_fmac_f32 v193, v3, v53\n\t" \
  "v_fmac_f32 v194, v3, v54\n\t" \
  "v_mul_f32 v21, v17, v53\n\t" \
  "v_mfma_f32_16x16x32_bf16 v[40:43], a[36:39], v[8:11], v[40:43]\n\t" \
  "v_fmac_f32 v195, v3, v55\n\t" \
  "v_fmac_f32 v196, v3, v56\n\t" \
  "v_mul_f32 v22, v17, v54\n\t" \
  "v_mfma_f32_16x16x32_bf16 v[44:47], a[40:43], v[8:11], v[44:47]\n\t" \
  "buffer_load_dwordx4 v0, s[36:39], s34 offen offset:2048 lds\n\t" \
  "v_mul_f32 v23, v17, v55\n\t" \
  "v_mfma_f32_16x16x32_bf16 v[48:51], a[44:47], v[8:11], v[48:51]\n\t" \
  "ds_read2st64_b64 v[8:11], v1 offset0:10 offset1:11\n\t" \
  "v_fmac_f32 v197, v3, v57\n\t" \
  "v_mul_f32 v24, v17, v56\n\t" \
  "v_mfma_f32_16x16x32_bf16 v[36:39], a[48:51], v[12:15], v[36:39]\n\t" \
  "v_fmac_f32 v198, v3, v58\n\t" \
  "v_fmac_f32 v199, v3, v59\n\t" \
  "v_mul_f32 v25, v17, v57\n\t" \
  "v_mfma_f32_16x16x32_bf16 v[40:43], a[52:55], v[12:15], v[40:43]\n\t" \
  "v_fmac_f32 v200, v3, v60\n\t" \
  "v_fmac_f32 v201, v3, v61\n\t" \
  "v_mul_f32 v26, v17, v58\n\t" \
  "v_mfma_f32_16x16x32_bf16 v[44:47], a[56:59], v[12:15], v[44:47]\n\t" \
  "v_fmac_f32 v202, v3, v62\n\t" \
  "v_fmac_f32 v203, v3, v63\n\t" \
  "v_mul_f32 v27, v17, v59\n\t" \
  "v_mfma_f32_16x16x32_bf16 v[48:51], a[60:63], v[12:15], v[48:51]\n\t" \
  "ds_read2st64_b64 v[12:15], v1 offset0:12 offset1:13\n\t" \
  "v_fmac_f32 v204, v3, v64\n\t" \
  "v_mul_f32 v28, v17, v60\n\t" \
  "s_waitcnt lgkmcnt(2)\n\t" \
  "v_mfma_f32_16x16x32_bf16 v[36:39], a[64:67], v[4:7], v[36:39]\n\t" \
  "buffer_load_dwordx4 v0, s[36:39], s34 offen offset:3072 lds\n\t" \
  "v_mul_f32 v29, v17, v61\n\t" \
  "v_mfma_f32_16x16x32_bf16 v[40:43], a[68:71], v[4:7], v[40:43]\n\t" \
  "s_add_u32 m0, s89, 0\n\t" \
  "s_add_u32 s34, s33, s89\n\t" \
  "v_fmac_f32 v205, v3, v65\n\t" \
  "v_mul_f32 v30, v17, v62\n\t" \
  "v_mfma_f32_16x16x32_bf16 v[44:47], a[72:75], v[4:7], v[44:47]\n\t" \
  "v_fmac_f32 v206, v3, v66\n\t" \
  "v_fmac_f32 v207, v3, v67\n\t" \
  "v_mul_f32 v31, v17, v63\n\t" \
  "v_mfma_f32_16x16x32_bf16 v[48:51], a[76:79], v[4:7], v[48:51]\n\t" \
  "ds_read2st64_b64 v[4:7], v1 offset0:14 offset1:15\n\t" \
  "v_max_f32 v192, 0, v192\n\t" \
  "v_mul_f32 v32, v17, v64\n\t" \
  "s_waitcnt lgkmcnt(2)\n\t" \
  "v_mfma_f32_16x16x32_bf16 v[36:39], a[80:83], v[8:11], v[36:39]\n\t" \
  "v_max_f32 v193, 0, v193\n\t" \
  "v_max_f32 v194, 0, v194\n\t" \
  "v_mul_f32 v33, v17, v65\n\t" \
  "v_mfma_f32_16x16x32_bf16 v[40:43], a[84:87], v[8:11], v[40:43]\n\t" \
  "buffer_load_dwordx4 v0, s[36:39], s34 offen lds\n\t" \
  "v_mul_f32 v34, v17, v66\n\t" \
  "v_mfma_f32_16x16x32_bf16 v[44:47], a[88:91], v[8:11], v[44:47]\n\t" \
  "s_add_u32 m0, s23, 73728\n\t" \
  "s_add_u32 s33, s22, 368640\n\t" \
  "v_max_f32 v195, 0, v195\n\t" \
  "v_mul_f32 v35, v17, v67\n\t" \
  "v_mfma_f32_16x16x32_bf16 v[48:51], a[92:95], v[8:11], v[48:51]\n\t" \
  "ds_read2st64_b64 v[8:11], v1 offset0:16 offset1:17\n\t" \
  "v_mul_f32 v192, v192, v196\n\t" \
  "v_add_u32 v73, 131072, v72\n\t" \
  "s_waitcnt lgkmcnt(2)\n\t" \
  "v_mfma_f32_16x16x32_bf16 v[36:39], a[96:99], v[12:15], v[36:39]\n\t" \
  "v_mul_f32 v193, v193, v197\n\t" \
  "v_mul_f32 v194, v194, v198\n\t" \
  "v_xor_b32 v74, 32, v73\n\t" \
  "v_mfma_f32_16x16x32_bf16 v[40:43], a[100:103], v[12:15], v[40:43]\n\t" \
  "v_mul_f32 v195, v195, v199\n\t" \
  "v_max_f32 v200, 0, v200\n\t" \
  "ds_read_b64_tr_b16 v[88:89], v73 offset:0\n\t" \
  "v_mfma_f32_16x16x32_bf16 v[44:47], a[104:107], v[12:15], v[44:47]\n\t" \
  "buffer_load_dwordx4 v71, s[64:67], s33 offen lds\n\t" \
  "ds_read_b64_tr_b16 v[90:91], v73 offset:2048\n\t" \
  "v_mfma_f32_16x16x32_bf16 v[48:51], a[108:111], v[12:15], v[48:51]\n\t" \
  "s_add_u32 m0, s23, 77824\n\t" \
  "ds_read2st64_b64 v[12:15], v1 offset0:18 offset1:19\n\t" \
  "s_add_u32 s33, s22, 1032192\n\t" \
  "ds_read_b64_tr_b16 v[92:93], v74 offset:0\n\t" \
  "s_waitcnt lgkmcnt(5)\n\t" \
  "v_mfma_f32_16x16x32_bf16 v[36:39], a[112:115], v[4:7], v[36:39]\n\t" \
  "v_max_f32 v201, 0, v201\n\t" \
  "v_max_f32 v202, 0, v202\n\t" \
  "ds_read_b64_tr_b16 v[94:95], v74 offset:2048\n\t" \
  "v_mfma_f32_16x16x32_bf16 v[40:43], a[116:119], v[4:7], v[40:43]\n\t" \
  "v_max_f32 v203, 0, v203\n\t" \
  "v_mul_f32 v200, v200, v204\n\t" \
  "v_add_u32 v73, 139264, v72\n\t" \
  "v_mfma_f32_16x16x32_bf16 v[44:47], a[120:123], v[4:7], v[44:47]\n\t" \
  "v_mul_f32 v201, v201, v205\n\t" \
  "v_mul_f32 v202, v202, v206\n\t" \
  "v_xor_b32 v74, 32, v73\n\t" \
  "v_mfma_f32_16x16x32_bf16 v[48:51], a[124:127], v[4:7], v[48:51]\n\t" \
  "ds_read2st64_b64 v[4:7], v1 offset0:20 offset1:21\n\t" \
  "v_mul_f32 v203, v203, v207\n\t" \
  "ds_read_b64_tr_b16 v[96:97], v73 offset:0\n\t" \
  "s_waitcnt lgkmcnt(7)\n\t" \
  "v_mfma_f32_16x16x32_bf16 v[36:39], a[128:131], v[8:11], v[36:39]\n\t" \
  "buffer_load_dwordx4 v71, s[64:67], s33 offen lds\n\t" \
  "ds_read_b64_tr_b16 v[98:99], v73 offset:2048\n\t" \
  "v_mfma_f32_16x16x32_bf16 v[40:43], a[132:135], v[8:11], v[40:43]\n\t" \
  "s_add_u32 m0, s23, 81920\n\t" \
  "s_add_u32 s33, s22, 405504\n\t" \
  "v_cvt_pk_bf16_f32 v196, v192, v193\n\t" \
  "ds_read_b64_tr_b16 v[100:101], v74 offset:0\n\t" \
  "v_mfma_f32_16x16x32_bf16 v[44:47], a[136:139], v[8:11], v[44:47]\n\t" \
  "v_cvt_pk_bf16_f32 v197, v194, v195\n\t" \
  "v_cvt_pk_bf16_f32 v198, v200, v201\n\t" \
  "ds_read_b64_tr_b16 v[102:103], v74 offset:2048\n\t" \
  "v_mfma_f32_16x16x32_bf16 v[48:51], a[140:143], v[8:11], v[48:51]\n\t" \
  "ds_read2st64_b64 v[8:11], v1 offset0:22 offset1:23\n\t" \
  "v_cvt_pk_bf16_f32 v199, v202, v203\n\t" \
  "v_add_u32 v73, 114688, v72\n\t" \
  "s_waitcnt lgkmcnt(8)\n\t" \
  "v_mfma_f32_16x16x32_bf16 v[36:39], a[144:147], v[12:15], v[36:39]\n\t" \
  "buffer_store_dwordx4 v[196:199], v0, s[44:47], s35 offen sc1\n\t" \
  "v_xor_b32 v74, 32, v73\n\t" \
  "v_mfma_f32_16x16x32_bf16 v[40:43], a[148:151], v[12:15], v[40:43]\n\t" \
  "buffer_load_dwordx4 v71, s[64:67], s33 offen lds\n\t" \
  "ds_read_b64_tr_b16 v[104:105], v73 offset:0\n\t" \
  "v_mfma_f32_16x16x32_bf16 v[44:47], a[152:155], v[12:15], v[44:47]\n\t" \
  "s_add_u32 m0, s23, 86016\n\t" \
  "s_add_u32 s33, s22, 1069056\n\t" \
  "ds_read_b32 v3, v76 offset:384\n\t" \
  "ds_read_b64_tr_b16 v[106:107], v73 offset:2048\n\t" \
  "v_mfma_f32_16x16x32_bf16 v[48:51], a[156:159], v[12:15], v[48:51]\n\t" \
  "s_mul_i32 s35, s80, 147456\n\t" \
  "ds_read2st64_b64 v[12:15], v1 offset0:24 offset1:25\n\t" \
  "s_waitcnt lgkmcnt(2)\n\t" \
  "v_fmac_f32 v208, v3, v52\n\t" \
  "ds_read_b64_tr_b16 v[108:109], v74 offset:0\n\t" \
  "v_mfma_f32_16x16x32_bf16 v[36:39], a[160:163], v[4:7], v[36:39]\n\t" \
  "s_add_u32 s35, s35, s91\n\t" \
  "v_fmac_f32 v209, v3, v53\n\t" \
  "v_fmac_f32 v210, v3, v54\n\t" \
  "ds_read_b64_tr_b16 v[110:111], v74 offset:2048\n\t" \
  "v_mfma_f32_16x16x32_bf16 v[40:43], a[164:167], v[4:7], v[40:43]\n\t" \
  "v_fmac_f32 v211, v3, v55\n\t" \
  "v_fmac_f32 v212, v3, v56\n\t" \
  "v_add_u32 v73, 122880, v72\n\t" \
  "v_mfma_f32_16x16x32_bf16 v[44:47], a[168:171], v[4:7], v[44:47]\n\t" \
  "buffer_load_dwordx4 v71, s[64:67], s33 offen lds\n\t" \
  "v_xor_b32 v74, 32, v73\n\t" \
  "v_mfma_f32_16x16x32_bf16 v[48:51], a[172:175], v[4:7], v[48:51]\n\t" \
  "s_add_u32 m0, s23, 90112\n\t" \
  "ds_read2st64_b64 v[4:7], v1 offset0:26 offset1:27\n\t" \
  "s_add_u32 s33, s22, 442368\n\t" \
  "ds_read_b64_tr_b16 v[112:113], v73 offset:0\n\t" \
  "v_mfma_f32_16x16x32_bf16 v[36:39], a[176:179], v[8:11], v[36:39]\n\t" \
  "v_fmac_f32 v213, v3, v57\n\t" \
  "v_fmac_f32 v214, v3, v58\n\t" \
  "ds_read_b64_tr_b16 v[114:115], v73 offset:2048\n\t" \
  "v_mfma_f32_16x16x32_bf16 v[40:43], a[180:183], v[8:11], v[40:43]\n\t" \
  "v_fmac_f32 v215, v3, v59\n\t" \
  "v_fmac_f32 v216, v3, v60\n\t" \
  "ds_read_b64_tr_b16 v[116:117], v74 offset:0\n\t" \
  "v_mfma_f32_16x16x32_bf16 v[44:47], a[184:187], v[8:11], v[44:47]\n\t" \
  "v_fmac_f32 v217, v3, v61\n\t" \
  "v_fmac_f32 v218, v3, v62\n\t" \
  "ds_read_b64_tr_b16 v[118:119], v74 offset:2048\n\t" \
  "v_mfma_f32_16x16x32_bf16 v[48:51], a[188:191], v[8:11], v[48:51]\n\t" \
  "ds_read2st64_b64 v[8:11], v1 offset0:28 offset1:29\n\t" \
  "v_fmac_f32 v219, v3, v63\n\t" \
  "s_waitcnt lgkmcnt(8)\n\t" \
  "v_mfma_f32_16x16x32_bf16 v[36:39], a[192:195], v[12:15], v[36:39]\n\t" \
  "buffer_load_dwordx4 v71, s[64:67], s33 offen lds\n\t" \
  "v_mfma_f32_16x16x32_bf16 v[40:43], a[196:199], v[12:15], v[40:43]\n\t" \
  "s_add_u32 m0, s23, 94208\n\t" \
  "s_add_u32 s33, s22, 1105920\n\t" \
  "v_fmac_f32 v220, v3, v64\n\t" \
  "v_mfma_f32_16x16x32_bf16 v[44:47], a[200:203], v[12:15], v[44:47]\n\t" \
  "v_fmac_f32 v221, v3, v65\n\t" \
  "v_fmac_f32 v222, v3, v66\n\t" \
  "v_mfma_f32_16x16x32_bf16 v[48:51], a[204:207], v[12:15], v[48:51]\n\t" \
  "ds_read2st64_b64 v[12:15], v1 offset0:30 offset1:31\n\t" \
  "v_fmac_f32 v223, v3, v67\n\t" \
  "s_waitcnt lgkmcnt(6)\n\t" \
  "v_mfma_f32_16x16x32_bf16 v[36:39], a[208:211], v[4:7], v[36:39]\n\t" \
  "v_max_f32 v208, 0, v208\n\t" \
  "v_max_f32 v209, 0, v209\n\t" \
  "v_mfma_f32_16x16x32_bf16 v[40:43], a[212:215], v[4:7], v[40:43]\n\t" \
  "buffer_load_dwordx4 v71, s[64:67], s33 offen lds\n\t" \
  "v_mfma_f32_16x16x32_bf16 v[44:47], a[216:219], v[4:7], v[44:47]\n\t" \
  "s_add_u32 m0, s23, 98304\n\t" \
  "s_add_u32 s33, s22, 479232\n\t" \
  "v_max_f32 v210, 0, v210\n\t" \
  "v_mfma_f32_16x16x32_bf16 v[48:51], a[220:223], v[4:7], v[48:51]\n\t" \
  "ds_read2st64_b64 v[4:7], v1 offset0:32 offset1:33\n\t" \
  "v_max_f32 v211, 0, v211\n\t" \
  "s_waitcnt lgkmcnt(2)\n\t" \
  "v_mfma_f32_16x16x32_bf16 v[36:39], a[224:227], v[8:11], v[36:39]\n\t" \
  "v_mul_f32 v208, v208, v212\n\t" \
  "v_mul_f32 v209, v209, v213\n\t" \
  "v_mfma_f32_16x16x32_bf16 v[40:43], a[228:231], v[8:11], v[40:43]\n\t" \
  "v_mul_f32 v210, v210, v214\n\t" \
  "v_mul_f32 v211, v211, v215\n\t" \
  "v_mfma_f32_16x16x32_bf16 v[44:47], a[232:235], v[8:11], v[44:47]\n\t" \
  "buffer_load_dwordx4 v71, s[64:67], s33 offen lds\n\t" \
  "v_mfma_f32_16x16x32_bf16 v[48:51], a[236:239], v[8:11], v[48:51]\n\t" \
  "s_add_u32 m0, s23, 102400\n\t" \
  "ds_read2st64_b64 v[8:11], v1 offset0:34 offset1:35\n\t" \
  "s_add_u32 s33, s22, 1142784\n\t" \
  "s_waitcnt lgkmcnt(2)\n\t" \
  "v_mfma_f32_16x16x32_bf16 v[36:39], a[240:243], v[12:15], v[36:39]\n\t" \
  "v_max_f32 v216, 0, v216\n\t" \
  "v_max_f32 v217, 0, v217\n\t" \
  "v_mfma_f32_16x16x32_bf16 v[40:43], a[244:247], v[12:15], v[40:43]\n\t" \
  "v_max_f32 v218, 0, v218\n\t" \
  "v_max_f32 v219, 0, v219\n\t" \
  "v_xor_b32 v1, 18432, v1\n\t" \
  "v_mfma_f32_16x16x32_bf16 v[44:47], a[248:251], v[12:15], v[44:47]\n\t" \
  "v_mul_f32 v216, v216, v220\n\t" \
  "v_mul_f32 v217, v217, v221\n\t" \
  "v_mfma_f32_16x16x32_bf16 v[48:51], a[252:255], v[12:15], v[48:51]\n\t" \
  "buffer_load_dwordx4 v71, s[64:67], s33 offen lds\n\t" \
  "s_waitcnt lgkmcnt(1)\n\t" \
  "v_mfma_f32_16x16x32_bf16 v[36:39], v[224:227], v[4:7], v[36:39]\n\t" \
  "v_mul_f32 v218, v218, v222\n\t" \
  "v_mul_f32 v219, v219, v223\n\t" \
  "v_mfma_f32_16x16x32_bf16 v[40:43], v[228:231], v[4:7], v[40:43]\n\t" \
  "v_cvt_pk_bf16_f32 v212, v208, v209\n\t" \
  "v_cvt_pk_bf16_f32 v213, v210, v211\n\t" \
  "v_mfma_f32_16x16x32_bf16 v[44:47], v[232:235], v[4:7], v[44:47]\n\t" \
  "v_cvt_pk_bf16_f32 v214, v216, v217\n\t" \
  "v_cvt_pk_bf16_f32 v215, v218, v219\n\t" \
  "v_mfma_f32_16x16x32_bf16 v[48:51], v[236:239], v[4:7], v[48:51]\n\t" \
  "buffer_store_dwordx4 v[212:215], v0, s[44:47], s35 offen sc1\n\t" \
  "s_waitcnt vmcnt(10) lgkmcnt(0)\n\t" \
  "s_barrier\n\t" \
  "ds_read2st64_b64 v[12:15], v1 offset0:0 offset1:1\n\t" \
  "ds_read2st64_b64 v[4:7], v1 offset0:2 offset1:3\n\t" \
  "v_mfma_f32_16x16x32_bf16 v[36:39], v[240:243], v[8:11], v[36:39]\n\t" \
  "s_mul_i32 s33, s79, 18432\n\t" \
  "s_lshl_b32 s18, s82, 9\n\t" \
  "s_add_u32 s18, s18, s92\n\t" \
  "v_mfma_f32_16x16x32_bf16 v[40:43], v[244:247], v[8:11], v[40:43]\n\t" \
  "s_mov_b64 exec, 2\n\t" \
  "buffer_atomic_add v0, v0, s[56:59], s18 offen\n\t" \
  "s_mov_b64 exec, -1\n\t" \
  "v_mfma_f32_16x16x32_bf16 v[44:47], v[248:251], v[8:11], v[44:47]\n\t" \
  "s_add_u32 m0, s88, 18432\n\t" \
  "s_add_u32 s34, s33, s88\n\t" \
  "v_mfma_f32_16x16x32_bf16 v[48:51], v[252:255], v[8:11], v[48:51]\n\t" \
  "s_mul_i32 s35, s85, 147456\n\t" \
  "ds_read2st64_b64 v[8:11], v1 offset0:4 offset1:5\n\t" \
  "s_add_u32 s35, s35, s91\n\t" \
  "s_waitcnt lgkmcnt(2)\n\t" \
  "v_mfma_f32_16x16x32_bf16 v[20:23], a[0:3], v[12:15], v[20:23]\n\t" \
  "buffer_load_dwordx4 v0, s[36:39], s34 offen offset:0 lds\n\t" \
  "v_mfma_f32_16x16x32_bf16 v[24:27], a[4:7], v[12:15], v[24:27]\n\t" \
  "v_mfma_f32_16x16x32_bf16 v[28:31], a[8:11], v[12:15], v[28:31]\n\t" \
  "v_mfma_f32_16x16x32_bf16 v[32:35], a[12:15], v[12:15], v[32:35]\n\t" \
  "ds_read2st64_b64 v[12:15], v1 offset0:6 offset1:7\n\t" \
  "v_add_u32 v73, 36864, v72\n\t" \
  "s_waitcnt lgkmcnt(2)\n\t" \
  "v_mfma_f32_16x16x32_bf16 v[20:23], a[16:19], v[4:7], v[20:23]\n\t" \
  "v_xor_b32 v74, 32, v73\n\t" \
  "ds_read_b64_tr_b16 v[120:121], v73 offset:0\n\t" \
  "v_mfma_f32_16x16x32_bf16 v[24:27], a[20:23], v[4:7], v[24:27]\n\t" \
  "buffer_load_dwordx4 v0, s[36:39], s34 offen offset:1024 lds\n\t" \
  "v_mfma_f32_16x16x32_bf16 v[28:31], a[24:27], v[4:7], v[28:31]\n\t" \
  "v_max_f32 v36, 0, v36\n\t" \
  "v_max_f32 v37, 0, v37\n\t" \
  "v_mfma_f32_16x16x32_bf16 v[32:35], a[28:31], v[4:7], v[32:35]\n\t" \
  "ds_read2st64_b64 v[4:7], v1 offset0:8 offset1:9\n\t" \
  "v_max_f32 v38, 0, v38\n\t" \
  "s_waitcnt lgkmcnt(3)\n\t" \
  "v_mfma_f32_16x16x32_bf16 v[20:23], a[32:35], v[8:11], v[20:23]\n\t" \
  "v_max_f32 v39, 0, v39\n\t" \
  "v_mul_f32 v36, v36, v40\n\t" \
  "v_mfma_f32_16x16x32_bf16 v[24:27], a[36:39], v[8:11], v[24:27]\n\t" \
  "v_mul_f32 v37, v37, v41\n\t" \
  "v_mul_f32 v38, v38, v42\n\t" \
  "v_mfma_f32_16x16x32_bf16 v[28:31], a[40:43], v[8:11], v[28:31]\n\t" \
  "buffer_load_dwordx4 v0, s[36:39], s34 offen offset:2048 lds\n\t" \
  "v_mfma_f32_16x16x32_bf16 v[32:35], a[44:47], v[8:11], v[32:35]\n\t" \
  "ds_read2st64_b64 v[8:11], v1 offset0:10 offset1:11\n\t" \
  "v_mul_f32 v39, v39, v43\n\t" \
  "s_waitcnt lgkmcnt(3)\n\t" \
  "v_mfma_f32_16x16x32_bf16 v[20:23], a[48:51], v[12:15], v[20:23]\n\t" \
  "v_max_f32 v44, 0, v44\n\t" \
  "v_max_f32 v45, 0, v45\n\t" \
  "v_mfma_f32_16x16x32_bf16 v[24:27], a[52:55], v[12:15], v[24:27]\n\t" \
  "v_max_f32 v46, 0, v46\n\t" \
  "v_max_f32 v47, 0, v47\n\t" \
  "v_mfma_f32_16x16x32_bf16 v[28:31], a[56:59], v[12:15], v[28:31]\n\t" \
  "v_mul_f32 v44, v44, v48\n\t" \
  "v_mul_f32 v45, v45, v49\n\t" \
  "v_mfma_f32_16x16x32_bf16 v[32:35], a[60:63], v[12:15], v[32:35]\n\t" \
  "ds_read2st64_b64 v[12:15], v1 offset0:12 offset1:13\n\t" \
  "v_mul_f32 v46, v46, v50\n\t" \
  "s_waitcnt lgkmcnt(2)\n\t" \
  "v_mfma_f32_16x16x32_bf16 v[20:23], a[64:67], v[4:7], v[20:23]\n\t" \
  "buffer_load_dwordx4 v0, s[36:39], s34 offen offset:3072 lds\n\t" \
  "v_mfma_f32_16x16x32_bf16 v[24:27], a[68:71], v[4:7], v[24:27]\n\t" \
  "s_add_u32 m0, s89, 18432\n\t" \
  "s_add_u32 s34, s33, s89\n\t" \
  "v_mul_f32 v47, v47, v51\n\t" \
  "v_mfma_f32_16x16x32_bf16 v[28:31], a[72:75], v[4:7], v[28:31]\n\t" \
  "v_cvt_pk_bf16_f32 v40, v36, v37\n\t" \
  "v_cvt_pk_bf16_f32 v41, v38, v39\n\t" \
  "v_mfma_f32_16x16x32_bf16 v[32:35], a[76:79], v[4:7], v[32:35]\n\t" \
  "ds_read2st64_b64 v[4:7], v1 offset0:14 offset1:15\n\t" \
  "v_cvt_pk_bf16_f32 v42, v44, v45\n\t" \
  "s_waitcnt lgkmcnt(2)\n\t" \
  "v_mfma_f32_16x16x32_bf16 v[20:23], a[80:83], v[8:11], v[20:23]\n\t" \
  "v_cvt_pk_bf16_f32 v43, v46, v47\n\t" \
  "ds_read_b64_tr_b16 v[122:123], v73 offset:2048\n\t" \
  "v_mfma_f32_16x16x32_bf16 v[24:27], a[84:87], v[8:11], v[24:27]\n\t" \
  "buffer_load_dwordx4 v0, s[36:39], s34 offen lds\n\t" \
  "v_mfma_f32_16x16x32_bf16 v[28:31], a[88:91], v[8:11], v[28:31]\n\t" \
  "s_lshl_b32 s16, s83, 9\n\t" \
  "s_add_u32 s16, s16, s92\n\t" \
  "ds_read_b32 v3, v76 offset:512\n\t" \
  "v_mfma_f32_16x16x32_bf16 v[32:35], a[92:95], v[8:11], v[32:35]\n\t" \
  "ds_read2st64_b64 v[8:11], v1 offset0:16 offset1:17\n\t" \
  "s_waitcnt lgkmcnt(1)\n\t" \
  "v_fmac_f32 v176, v3, v52\n\t" \
  "v_mfma_f32_16x16x32_bf16 v[20:23], a[96:99], v[12:15], v[20:23]\n\t" \
  "buffer_load_dword v78, off, s[60:63], s16 offset:16 sc1\n\t" \
  "v_mfma_f32_16x16x32_bf16 v[24:27], a[100:103], v[12:15], v[24:27]\n\t" \
  "s_add_u32 m0, s23, 114688\n\t" \
  "s_add_u32 s33, s22, 516096\n\t" \
  "v_fmac_f32 v177, v3, v53\n\t" \
  "v_mfma_f32_16x16x32_bf16 v[28:31], a[104:107], v[12:15], v[28:31]\n\t" \
  "buffer_store_dwordx4 v[40:43], v0, s[44:47], s35 offen sc1\n\t" \
  "v_mfma_f32_16x16x32_bf16 v[32:35], a[108:111], v[12:15], v[32:35]\n\t" \
  "s_mul_i32 s35, s84, 147456\n\t" \
  "ds_read2st64_b64 v[12:15], v1 offset0:18 offset1:19\n\t" \
  "v_fmac_f32 v178, v3, v54\n\t" \
  "v_mfma_f32_16x16x32_bf16 v[20:23], a[112:115], v[4:7], v[20:23]\n\t" \
  "s_add_u32 s35, s35, s91\n\t" \
  "v_fmac_f32 v179, v3, v55\n\t" \
  "v_fmac_f32 v180, v3, v56\n\t" \
  "v_mfma_f32_16x16x32_bf16 v[24:27], a[116:119], v[4:7], v[24:27]\n\t" \
  "buffer_load_dwordx4 v71, s[64:67], s33 offen lds\n\t" \
  "v_mfma_f32_16x16x32_bf16 v[28:31], a[120:123], v[4:7], v[28:31]\n\t" \
  "s_add_u32 m0, s23, 118784\n\t" \
  "s_add_u32 s33, s22, 1179648\n\t" \
  "v_fmac_f32 v181, v3, v57\n\t" \
  "v_mfma_f32_16x16x32_bf16 v[32:35], a[124:127], v[4:7], v[32:35]\n\t" \
  "ds_read2st64_b64 v[4:7], v1 offset0:20 offset1:21\n\t" \
  "v_fmac_f32 v182, v3, v58\n\t" \
  "s_waitcnt lgkmcnt(2)\n\t" \
  "v_mfma_f32_16x16x32_bf16 v[20:23], a[128:131], v[8:11], v[20:23]\n\t" \
  "v_fmac_f32 v183, v3, v59\n\t" \
  "v_fmac_f32 v184, v3, v60\n\t" \
  "v_mfma_f32_16x16x32_bf16 v[24:27], a[132:135], v[8:11], v[24:27]\n\t" \
  "v_fmac_f32 v185, v3, v61\n\t" \
  "v_fmac_f32 v186, v3, v62\n\t" \
  "v_mfma_f32_16x16x32_bf16 v[28:31], a[136:139], v[8:11], v[28:31]\n\t" \
  "buffer_load_dwordx4 v71, s[64:67], s33 offen lds\n\t" \
  "v_mfma_f32_16x16x32_bf16 v[32:35], a[140:143], v[8:11], v[32:35]\n\t" \
  "s_add_u32 m0, s23, 122880\n\t" \
  "ds_read2st64_b64 v[8:11], v1 offset0:22 offset1:23\n\t" \
  "s_add_u32 s33, s22, 552960\n\t" \
  "s_waitcnt lgkmcnt(2)\n\t" \
  "v_mfma_f32_16x16x32_bf16 v[20:23], a[144:147], v[12:15], v[20:23]\n\t" \
  "v_fmac_f32 v187, v3, v63\n\t" \
  "v_fmac_f32 v188, v3, v64\n\t" \
  "v_mfma_f32_16x16x32_bf16 v[24:27], a[148:151], v[12:15], v[24:27]\n\t" \
  "v_fmac_f32 v189, v3, v65\n\t" \
  "v_fmac_f32 v190, v3, v66\n\t" \
  "v_mfma_f32_16x16x32_bf16 v[28:31], a[152:155], v[12:15], v[28:31]\n\t" \
  "v_fmac_f32 v191, v3, v67\n\t" \
  "v_max_f32 v176, 0, v176\n\t" \
  "v_mfma_f32_16x16x32_bf16 v[32:35], a[156:159], v[12:15], v[32:35]\n\t" \
  "ds_read2st64_b64 v[12:15], v1 offset0:24 offset1:25\n\t" \
  "v_max_f32 v177, 0, v177\n\t" \
  "s_waitcnt lgkmcnt(2)\n\t" \
  "v_mfma_f32_16x16x32_bf16 v[20:23], a[160:163], v[4:7], v[20:23]\n\t" \
  "buffer_load_dwordx4 v71, s[64:67], s33 offen lds\n\t" \
  "v_mfma_f32_16x16x32_bf16 v[24:27], a[164:167], v[4:7], v[24:27]\n\t" \
  "s_add_u32 m0, s23, 126976\n\t" \
  "s_add_u32 s33, s22, 1216512\n\t" \
  "v_max_f32 v178, 0, v178\n\t" \
  "v_mfma_f32_16x16x32_bf16 v[28:31], a[168:171], v[4:7], v[28:31]\n\t" \
  "v_max_f32 v179, 0, v179\n\t" \
  "v_mul_f32 v176, v176, v180\n\t" \
  "v_mfma_f32_16x16x32_bf16 v[32:35], a[172:175], v[4:7], v[32:35]\n\t" \
  "ds_read2st64_b64 v[4:7], v1 offset0:26 offset1:27\n\t" \
  "v_mul_f32 v177, v177, v181\n\t" \
  "s_waitcnt lgkmcnt(2)\n\t" \
  "v_mfma_f32_16x16x32_bf16 v[20:23], a[176:179], v[8:11], v[20:23]\n\t" \
  "v_mul_f32 v178, v178, v182\n\t" \
  "v_mul_f32 v179, v179, v183\n\t" \
  "v_mfma_f32_16x16x32_bf16 v[24:27], a[180:183], v[8:11], v[24:27]\n\t" \
  "buffer_load_dwordx4 v71, s[64:67], s33 offen lds\n\t" \
  "v_mfma_f32_16x16x32_bf16 v[28:31], a[184:187], v[8:11], v[28:31]\n\t" \
  "s_add_u32 m0, s23, 131072\n\t" \
  "s_add_u32 s33, s22, 589824\n\t" \
  "v_max_f32 v184, 0, v184\n\t" \
  "v_mfma_f32_16x16x32_bf16 v[32:35], a[188:191], v[8:11], v[32:35]\n\t" \
  "ds_read2st64_b64 v[8:11], v1 offset0:28 offset1:29\n\t" \
  "v_max_f32 v185, 0, v185\n\t" \
  "s_waitcnt lgkmcnt(2)\n\t" \
  "v_mfma_f32_16x16x32_bf16 v[20:23], a[192:195], v[12:15], v[20:23]\n\t" \
  "v_max_f32 v186, 0, v186\n\t" \
  "v_max_f32 v187, 0, v187\n\t" \
  "v_mfma_f32_16x16x32_bf16 v[24:27], a[196:199], v[12:15], v[24:27]\n\t" \
  "v_mul_f32 v184, v184, v188\n\t" \
  "v_mul_f32 v185, v185, v189\n\t" \
  "v_mfma_f32_16x16x32_bf16 v[28:31], a[200:203], v[12:15], v[28:31]\n\t" \
  "buffer_load_dwordx4 v71, s[64:67], s33 offen lds\n\t" \
  "v_mfma_f32_16x16x32_bf16 v[32:35], a[204:207], v[12:15], v[32:35]\n\t" \
  "s_add_u32 m0, s23, 135168\n\t" \
  "ds_read2st64_b64 v[12:15], v1 offset0:30 offset1:31\n\t" \
  "s_add_u32 s33, s22, 1253376\n\t" \
  "s_waitcnt lgkmcnt(2)\n\t" \
  "v_mfma_f32_16x16x32_bf16 v[20:23], a[208:211], v[4:7], v[20:23]\n\t" \
  "v_mul_f32 v186, v186, v190\n\t" \
  "v_mul_f32 v187, v187, v191\n\t" \
  "v_mfma_f32_16x16x32_bf16 v[24:27], a[212:215], v[4:7], v[24:27]\n\t" \
  "v_cvt_pk_bf16_f32 v180, v176, v177\n\t" \
  "v_cvt_pk_bf16_f32 v181, v178, v179\n\t" \
  "v_mfma_f32_16x16x32_bf16 v[28:31], a[216:219], v[4:7], v[28:31]\n\t" \
  "v_cvt_pk_bf16_f32 v182, v184, v185\n\t" \
  "v_cvt_pk_bf16_f32 v183, v186, v187\n\t" \
  "v_mfma_f32_16x16x32_bf16 v[32:35], a[220:223], v[4:7], v[32:35]\n\t" \
  "ds_read2st64_b64 v[4:7], v1 offset0:32 offset1:33\n\t" \
  "ds_read_b32 v17, v76 offset:896\n\t" \
  "s_waitcnt lgkmcnt(3)\n\t" \
  "v_mfma_f32_16x16x32_bf16 v[20:23], a[224:227], v[8:11], v[20:23]\n\t" \
  "buffer_load_dwordx4 v71, s[64:67], s33 offen lds\n\t" \
  "v_mfma_f32_16x16x32_bf16 v[24:27], a[228:231], v[8:11], v[24:27]\n\t" \
  "s_add_u32 m0, s23, 139264\n\t" \
  "s_add_u32 s33, s22, 626688\n\t" \
  "s_waitcnt lgkmcnt(0)\n\t" \
  "v_mul_f32 v36, v17, v52\n\t" \
  "v_mfma_f32_16x16x32_bf16 v[28:31], a[232:235], v[8:11], v[28:31]\n\t" \
  "buffer_store_dwordx4 v[180:183], v0, s[44:47], s35 offen sc1\n\t" \
  "v_mfma_f32_16x16x32_bf16 v[32:35], a[236:239], v[8:11], v[32:35]\n\t" \
  "ds_read2st64_b64 v[8:11], v1 offset0:34 offset1:35\n\t" \
  "v_mul_f32 v37, v17, v53\n\t" \
  "v_mfma_f32_16x16x32_bf16 v[20:23], a[240:243], v[12:15], v[20:23]\n\t" \
  "v_mul_f32 v38, v17, v54\n\t" \
  "v_mul_f32 v39, v17, v55\n\t" \
  "v_mfma_f32_16x16x32_bf16 v[24:27], a[244:247], v[12:15], v[24:27]\n\t" \
  "buffer_load_dwordx4 v71, s[64:67], s33 offen lds\n\t" \
  "v_xor_b32 v1, 18432, v1\n\t" \
  "v_mfma_f32_16x16x32_bf16 v[28:31], a[248:251], v[12:15], v[28:31]\n\t" \
  "s_add_u32 m0, s23, 143360\n\t" \
  "s_add_u32 s33, s22, 1290240\n\t" \
  "v_mul_f32 v40, v17, v56\n\t" \
  "v_mfma_f32_16x16x32_bf16 v[32:35], a[252:255], v[12:15], v[32:35]\n\t" \
  "v_mul_f32 v41, v17, v57\n\t" \
  "v_mul_f32 v42, v17, v58\n\t" \
  "s_waitcnt vmcnt(9)\n\t" \
  "v_cmp_gt_u32 vcc, s97, v78\n\t" \
  "v_mfma_f32_16x16x32_bf16 v[20:23], v[224:227], v[4:7], v[20:23]\n\t" \
  "v_mul_f32 v43, v17, v59\n\t" \
  "v_mul_f32 v44, v17, v60\n\t" \
  "v_mfma_f32_16x16x32_bf16 v[24:27], v[228:231], v[4:7], v[24:27]\n\t" \
  "v_mul_f32 v45, v17, v61\n\t" \
  "v_mul_f32 v46, v17, v62\n\t" \
  "v_mul_f32 v47, v17, v63\n\t" \
  "v_mul_f32 v48, v17, v64\n\t" \
  "v_mul_f32 v49, v17, v65\n\t" \
  "v_mul_f32 v50, v17, v66\n\t" \
  "v_mul_f32 v51, v17, v67\n\t" \
  "v_mfma_f32_16x16x32_bf16 v[28:31], v[232:235], v[4:7], v[28:31]\n\t" \
  "buffer_load_dwordx4 v71, s[64:67], s33 offen lds\n\t" \
  "v_mfma_f32_16x16x32_bf16 v[32:35], v[236:239], v[4:7], v[32:35]\n\t" \
  "ds_read_b64_tr_b16 v[124:125], v74 offset:0\n\t" \
  "ds_read_b64_tr_b16 v[126:127], v74 offset:2048\n\t" \
  "v_add_u32 v73, 45056, v72\n\t" \
  "v_xor_b32 v74, 32, v73\n\t" \
  "ds_read_b64_tr_b16 v[128:129], v73 offset:0\n\t" \
  "ds_read_b64_tr_b16 v[130:131], v73 offset:2048\n\t" \
  "ds_read_b64_tr_b16 v[132:133], v74 offset:0\n\t" \
  "ds_read_b64_tr_b16 v[134:135], v74 offset:2048\n\t" \
  "v_add_u32 v73, 53248, v72\n\t" \
  "v_xor_b32 v74, 32, v73\n\t" \
  "ds_read_b64_tr_b16 v[136:137], v73 offset:0\n\t" \
  "ds_read_b64_tr_b16 v[138:139], v73 offset:2048\n\t" \
  "ds_read_b64_tr_b16 v[140:141], v74 offset:0\n\t" \
  "ds_read_b64_tr_b16 v[142:143], v74 offset:2048\n\t" \
  "v_add_u32 v73, 61440, v72\n\t" \
  "v_xor_b32 v74, 32, v73\n\t" \
  "ds_read_b64_tr_b16 v[144:145], v73 offset:0\n\t" \
  "ds_read_b64_tr_b16 v[146:147], v73 offset:2048\n\t" \
  "ds_read_b64_tr_b16 v[148:149], v74 offset:0\n\t" \
  "ds_read_b64_tr_b16 v[150:151], v74 offset:2048\n\t" \
  "v_add_u32 v73, 106496, v72\n\t" \
  "v_xor_b32 v74, 32, v73\n\t" \
  "ds_read_b64_tr_b16 v[152:153], v73 offset:0\n\t" \
  "ds_read_b64_tr_b16 v[154:155], v73 offset:2048\n\t" \
  "ds_read_b64_tr_b16 v[156:157], v74 offset:0\n\t" \
  "ds_read_b64_tr_b16 v[158:159], v74 offset:2048\n\t" \
  "s_waitcnt vmcnt(11) lgkmcnt(0)\n\t" \
  "s_barrier\n\t" \
  "s_cbranch_vccnz SL_r2_%=\n\t" \
  "BK_r2_%=:\n\t" \
  "ds_read2st64_b64 v[12:15], v1 offset0:0 offset1:1\n\t" \
  "ds_read2st64_b64 v[4:7], v1 offset0:2 offset1:3\n\t" \
  "v_mfma_f32_16x16x32_bf16 v[20:23], v[240:243], v[8:11], v[20:23]\n\t" \
  "s_mul_i32 s33, s78, 18432\n\t" \
  "s_lshl_b32 s18, s81, 9\n\t" \
  "s_add_u32 s18, s18, s92\n\t" \
  "v_mfma_f32_16x16x32_bf16 v[24:27], v[244:247], v[8:11], v[24:27]\n\t" \
  "s_mov_b64 exec, 2\n\t" \
  "buffer_atomic_add v0, v0, s[56:59], s18 offen\n\t" \
  "s_mov_b64 exec, -1\n\t" \
  "v_mfma_f32_16x16x32_bf16 v[28:31], v[248:251], v[8:11], v[28:31]\n\t" \
  "s_add_u32 m0, s88, 0\n\t" \
  "s_lshl_b32 s18, s80, 9\n\t" \
  "s_add_u32 s18, s18, s92\n\t" \
  "v_mfma_f32_16x16x32_bf16 v[32:35], v[252:255], v[8:11], v[32:35]\n\t" \
  "s_add_u32 s34, s33, s88\n\t" \
  "ds_read2st64_b64 v[8:11], v1 offset0:4 offset1:5\n\t" \
  "s_waitcnt lgkmcnt(2)\n\t" \
  "v_mfma_f32_16x16x32_bf16 v[36:39], a[0:3], v[12:15], v[36:39]\n\t" \
  "s_mov_b64 exec, 2\n\t" \
  "buffer_atomic_add v0, v0, s[56:59], s18 offen\n\t" \
  "s_mov_b64 exec, -1\n\t" \
  "v_mfma_f32_16x16x32_bf16 v[40:43], a[4:7], v[12:15], v[40:43]\n\t" \
  "buffer_load_dwordx4 v0, s[36:39], s34 offen offset:0 lds\n\t" \
  "v_mfma_f32_16x16x32_bf16 v[44:47], a[8:11], v[12:15], v[44:47]\n\t" \
  "v_mfma_f32_16x16x32_bf16 v[48:51], a[12:15], v[12:15], v[48:51]\n\t" \
  "ds_read2st64_b64 v[12:15], v1 offset0:6 offset1:7\n\t" \
  "v_add_u32 v73, 73728, v72\n\t" \
  "s_waitcnt lgkmcnt(2)\n\t" \
  "v_mfma_f32_16x16x32_bf16 v[36:39], a[16:19], v[4:7], v[36:39]\n\t" \
  "v_xor_b32 v74, 32, v73\n\t" \
  "ds_read_b64_tr_b16 v[160:161], v73 offset:0\n\t" \
  "v_mfma_f32_16x16x32_bf16 v[40:43], a[20:23], v[4:7], v[40:43]\n\t" \
  "v_max_f32 v20, 0, v20\n\t" \
  "v_max_f32 v21, 0, v21\n\t" \
  "v_mfma_f32_16x16x32_bf16 v[44:47], a[24:27], v[4:7], v[44:47]\n\t" \
  "buffer_load_dwordx4 v0, s[36:39], s34 offen offset:1024 lds\n\t" \
  "v_mfma_f32_16x16x32_bf16 v[48:51], a[28:31], v[4:7], v[48:51]\n\t" \
  "ds_read2st64_b64 v[4:7], v1 offset0:8 offset1:9\n\t" \
  "v_max_f32 v22, 0, v22\n\t" \
  "s_waitcnt lgkmcnt(3)\n\t" \
  "v_mfma_f32_16x16x32_bf16 v[36:39], a[32:35], v[8:11], v[36:39]\n\t" \
  "v_max_f32 v23, 0, v23\n\t" \
  "v_mul_f32 v20, v20, v24\n\t" \
  "v_mfma_f32_16x16x32_bf16 v[40:43], a[36:39], v[8:11], v[40:43]\n\t" \
  "v_mul_f32 v21, v21, v25\n\t" \
  "v_mul_f32 v22, v22, v26\n\t" \
  "v_mfma_f32_16x16x32_bf16 v[44:47], a[40:43], v[8:11], v[44:47]\n\t" \
  "v_mul_f32 v23, v23, v27\n\t" \
  "v_max_f32 v28, 0, v28\n\t" \
  "v_mfma_f32_16x16x32_bf16 v[48:51], a[44:47], v[8:11], v[48:51]\n\t" \
  "ds_read2st64_b64 v[8:11], v1 offset0:10 offset1:11\n\t" \
  "v_max_f32 v29, 0, v29\n\t" \
  "s_waitcnt lgkmcnt(3)\n\t" \
  "v_mfma_f32_16x16x32_bf16 v[36:39], a[48:51], v[12:15], v[36:39]\n\t" \
  "buffer_load_dwordx4 v0, s[36:39], s34 offen offset:2048 lds\n\t" \
  "v_mfma_f32_16x16x32_bf16 v[40:43], a[52:55], v[12:15], v[40:43]\n\t" \
  "v_max_f32 v30, 0, v30\n\t" \
  "v_max_f32 v31, 0, v31\n\t" \
  "v_mfma_f32_16x16x32_bf16 v[44:47], a[56:59], v[12:15], v[44:47]\n\t" \
  "v_mul_f32 v28, v28, v32\n\t" \
  "v_mul_f32 v29, v29, v33\n\t" \
  "v_mfma_f32_16x16x32_bf16 v[48:51], a[60:63], v[12:15], v[48:51]\n\t" \
  "ds_read2st64_b64 v[12:15], v1 offset0:12 offset1:13\n\t" \
  "v_mul_f32 v30, v30, v34\n\t" \
  "s_waitcnt lgkmcnt(2)\n\t" \
  "v_mfma_f32_16x16x32_bf16 v[36:39], a[64:67], v[4:7], v[36:39]\n\t" \
  "v_mul_f32 v31, v31, v35\n\t" \
  "v_cvt_pk_bf16_f32 v24, v20, v21\n\t" \
  "v_mfma_f32_16x16x32_bf16 v[40:43], a[68:71], v[4:7], v[40:43]\n\t" \
  "buffer_load_dwordx4 v0, s[36:39], s34 offen offset:3072 lds\n\t" \
  "v_mfma_f32_16x16x32_bf16 v[44:47], a[72:75], v[4:7], v[44:47]\n\t" \
  "s_add_u32 m0, s89, 0\n\t" \
  "s_add_u32 s34, s33, s89\n\t" \
  "v_cvt_pk_bf16_f32 v25, v22, v23\n\t" \
  "v_mfma_f32_16x16x32_bf16 v[48:51], a[76:79], v[4:7], v[48:51]\n\t" \
  "ds_read2st64_b64 v[4:7], v1 offset0:14 offset1:15\n\t" \
  "v_cvt_pk_bf16_f32 v26, v28, v29\n\t" \
  "s_waitcnt lgkmcnt(2)\n\t" \
  "v_mfma_f32_16x16x32_bf16 v[36:39], a[80:83], v[8:11], v[36:39]\n\t" \
  "v_cvt_pk_bf16_f32 v27, v30, v31\n\t" \
  "ds_read_b64_tr_b16 v[162:163], v73 offset:2048\n\t" \
  "v_mfma_f32_16x16x32_bf16 v[40:43], a[84:87], v[8:11], v[40:43]\n\t" \
  "ds_read_b64_tr_b16 v[164:165], v74 offset:0\n\t" \
  "ds_read_b64_tr_b16 v[166:167], v74 offset:2048\n\t" \
  "v_mfma_f32_16x16x32_bf16 v[44:47], a[88:91], v[8:11], v[44:47]\n\t" \
  "buffer_load_dwordx4 v0, s[36:39], s34 offen lds\n\t" \
  "v_mfma_f32_16x16x32_bf16 v[48:51], a[92:95], v[8:11], v[48:51]\n\t" \
  "s_mul_i32 s35, s72, 147456\n\t" \
  "ds_read2st64_b64 v[8:11], v1 offset0:16 offset1:17\n\t" \
  "s_add_u32 s35, s35, s91\n\t" \
  "s_waitcnt lgkmcnt(5)\n\t" \
  "v_mfma_f32_16x16x32_bf16 v[36:39], a[96:99], v[12:15], v[36:39]\n\t" \
  "buffer_store_dwordx4 v[24:27], v0, s[44:47], s35 offen sc1\n\t" \
  "v_mfma_f32_16x16x32_bf16 v[40:43], a[100:103], v[12:15], v[40:43]\n\t" \
  "s_mul_i32 s34, s83, 147456\n\t" \
  "s_lshl_b32 s16, s82, 9\n\t" \
  "s_add_u32 s16, s16, s92\n\t" \
  "v_mfma_f32_16x16x32_bf16 v[44:47], a[104:107], v[12:15], v[44:47]\n\t" \
  "s_add_u32 s34, s34, s90\n\t" \
  "ds_read_b32 v17, v76 offset:1024\n\t" \
  "s_add_u32 m0, s30, 36864\n\t" \
  "v_mfma_f32_16x16x32_bf16 v[48:51], a[108:111], v[12:15], v[48:51]\n\t" \
  "ds_read2st64_b64 v[12:15], v1 offset0:18 offset1:19\n\t" \
  "s_waitcnt lgkmcnt(1)\n\t" \
  "v_mul_f32 v20, v17, v52\n\t" \
  "v_mfma_f32_16x16x32_bf16 v[36:39], a[112:115], v[4:7], v[36:39]\n\t" \
  "v_mul_f32 v21, v17, v53\n\t" \
  "v_mul_f32 v22, v17, v54\n\t" \
  "v_mfma_f32_16x16x32_bf16 v[40:43], a[116:119], v[4:7], v[40:43]\n\t" \
  "v_mul_f32 v23, v17, v55\n\t" \
  "v_mul_f32 v24, v17, v56\n\t" \
  "v_mfma_f32_16x16x32_bf16 v[44:47], a[120:123], v[4:7], v[44:47]\n\t" \
  "v_mul_f32 v25, v17, v57\n\t" \
  "v_mul_f32 v26, v17, v58\n\t" \
  "v_mfma_f32_16x16x32_bf16 v[48:51], a[124:127], v[4:7], v[48:51]\n\t" \
  "ds_read2st64_b64 v[4:7], v1 offset0:20 offset1:21\n\t" \
  "v_mul_f32 v27, v17, v59\n\t" \
  "v_mfma_f32_16x16x32_bf16 v[36:39], a[128:131], v[8:11], v[36:39]\n\t" \
  "v_mul_f32 v28, v17, v60\n\t" \
  "v_mul_f32 v29, v17, v61\n\t" \
  "v_mfma_f32_16x16x32_bf16 v[40:43], a[132:135], v[8:11], v[40:43]\n\t" \
  "v_mul_f32 v30, v17, v62\n\t" \
  "v_mul_f32 v31, v17, v63\n\t" \
  "v_mfma_f32_16x16x32_bf16 v[44:47], a[136:139], v[8:11], v[44:47]\n\t" \
  "v_mul_f32 v32, v17, v64\n\t" \
  "v_mul_f32 v33, v17, v65\n\t" \
  "v_mfma_f32_16x16x32_bf16 v[48:51], a[140:143], v[8:11], v[48:51]\n\t" \
  "ds_read2st64_b64 v[8:11], v1 offset0:22 offset1:23\n\t" \
  "v_mul_f32 v34, v17, v66\n\t" \
  "s_waitcnt lgkmcnt(2)\n\t" \
  "v_mfma_f32_16x16x32_bf16 v[36:39], a[144:147], v[12:15], v[36:39]\n\t" \
  "v_mul_f32 v35, v17, v67\n\t" \
  "v_add_u32 v73, 81920, v72\n\t" \
  "v_mfma_f32_16x16x32_bf16 v[40:43], a[148:151], v[12:15], v[40:43]\n\t" \
  "buffer_load_dword v78, off, s[60:63], s16 offset:16 sc1\n\t" \
  "v_mfma_f32_16x16x32_bf16 v[44:47], a[152:155], v[12:15], v[44:47]\n\t" \
  "v_xor_b32 v74, 32, v73\n\t" \
  "ds_read_b64_tr_b16 v[168:169], v73 offset:0\n\t" \
  "v_mfma_f32_16x16x32_bf16 v[48:51], a[156:159], v[12:15], v[48:51]\n\t" \
  "ds_read2st64_b64 v[12:15], v1 offset0:24 offset1:25\n\t" \
  "s_waitcnt lgkmcnt(3)\n\t" \
  "v_mfma_f32_16x16x32_bf16 v[36:39], a[160:163], v[4:7], v[36:39]\n\t" \
  "ds_read_b64_tr_b16 v[170:171], v73 offset:2048\n\t" \
  "v_mfma_f32_16x16x32_bf16 v[40:43], a[164:167], v[4:7], v[40:43]\n\t" \
  "buffer_load_dwordx4 v0, s[40:43], s34 offen offset:0 sc1 lds\n\t" \
  "v_mfma_f32_16x16x32_bf16 v[44:47], a[168:171], v[4:7], v[44:47]\n\t" \
  "ds_read_b64_tr_b16 v[172:173], v74 offset:0\n\t" \
  "v_mfma_f32_16x16x32_bf16 v[48:51], a[172:175], v[4:7], v[48:51]\n\t" \
  "ds_read2st64_b64 v[4:7], v1 offset0:26 offset1:27\n\t" \
  "s_waitcnt lgkmcnt(5)\n\t" \
  "v_mfma_f32_16x16x32_bf16 v[36:39], a[176:179], v[8:11], v[36:39]\n\t" \
  "ds_read_b64_tr_b16 v[174:175], v74 offset:2048\n\t" \
  "v_add_u32 v73, 90112, v72\n\t" \
  "v_mfma_f32_16x16x32_bf16 v[40:43], a[180:183], v[8:11], v[40:43]\n\t" \
  "buffer_load_dwordx4 v0, s[40:43], s34 offen offset:1024 sc1 lds\n\t" \
  "v_mfma_f32_16x16x32_bf16 v[44:47], a[184:187], v[8:11], v[44:47]\n\t" \
  "v_xor_b32 v74, 32, v73\n\t" \
  "ds_read_b64_tr_b16 v[176:177], v73 offset:0\n\t" \
  "v_mfma_f32_16x16x32_bf16 v[48:51], a[188:191], v[8:11], v[48:51]\n\t" \
  "ds_read2st64_b64 v[8:11], v1 offset0:28 offset1:29\n\t" \
  "s_waitcnt lgkmcnt(6)\n\t" \
  "v_mfma_f32_16x16x32_bf16 v[36:39], a[192:195], v[12:15], v[36:39]\n\t" \
  "ds_read_b64_tr_b16 v[178:179], v73 offset:2048\n\t" \
  "v_mfma_f32_16x16x32_bf16 v[40:43], a[196:199], v[12:15], v[40:43]\n\t" \
  "buffer_load_dwordx4 v0, s[40:43], s34 offen offset:2048 sc1 lds\n\t" \
  "v_mfma_f32_16x16x32_bf16 v[44:47], a[200:203], v[12:15], v[44:47]\n\t" \
  "ds_read_b64_tr_b16 v[180:181], v74 offset:0\n\t" \
  "v_mfma_f32_16x16x32_bf16 v[48:51], a[204:207], v[12:15], v[48:51]\n\t" \
  "ds_read2st64_b64 v[12:15], v1 offset0:30 offset1:31\n\t" \
  "s_waitcnt lgkmcnt(6)\n\t" \
  "v_mfma_f32_16x16x32_bf16 v[36:39], a[208:211], v[4:7], v[36:39]\n\t" \
  "ds_read_b64_tr_b16 v[182:183], v74 offset:2048\n\t" \
  "v_add_u32 v73, 98304, v72\n\t" \
  "v_mfma_f32_16x16x32_bf16 v[40:43], a[212:215], v[4:7], v[40:43]\n\t" \
  "buffer_load_dwordx4 v0, s[40:43], s34 offen offset:3072 sc1 lds\n\t" \
  "v_mfma_f32_16x16x32_bf16 v[44:47], a[216:219], v[4:7], v[44:47]\n\t" \
  "s_add_u32 m0, m0, 4096\n\t" \
  "s_add_u32 s34, s34, 4096\n\t" \
  "v_xor_b32 v74, 32, v73\n\t" \
  "v_mfma_f32_16x16x32_bf16 v[48:51], a[220:223], v[4:7], v[48:51]\n\t" \
  "ds_read2st64_b64 v[4:7], v1 offset0:32 offset1:33\n\t" \
  "s_waitcnt lgkmcnt(5)\n\t" \
  "v_mfma_f32_16x16x32_bf16 v[36:39], a[224:227], v[8:11], v[36:39]\n\t" \
  "ds_read_b64_tr_b16 v[184:185], v73 offset:0\n\t" \
  "v_mfma_f32_16x16x32_bf16 v[40:43], a[228:231], v[8:11], v[40:43]\n\t" \
  "buffer_load_dwordx4 v0, s[40:43], s34 offen offset:0 sc1 lds\n\t" \
  "v_mfma_f32_16x16x32_bf16 v[44:47], a[232:235], v[8:11], v[44:47]\n\t" \
  "ds_read_b64_tr_b16 v[186:187], v73 offset:2048\n\t" \
  "v_mfma_f32_16x16x32_bf16 v[48:51], a[236:239], v[8:11], v[48:51]\n\t" \
  "ds_read2st64_b64 v[8:11], v1 offset0:34 offset1:35\n\t" \
  "s_waitcnt lgkmcnt(5)\n\t" \
  "v_mfma_f32_16x16x32_bf16 v[36:39], a[240:243], v[12:15], v[36:39]\n\t" \
  "ds_read_b64_tr_b16 v[188:189], v74 offset:0\n\t" \
  "v_mfma_f32_16x16x32_bf16 v[40:43], a[244:247], v[12:15], v[40:43]\n\t" \
  "buffer_load_dwordx4 v0, s[40:43], s34 offen offset:1024 sc1 lds\n\t" \
  "v_mfma_f32_16x16x32_bf16 v[44:47], a[248:251], v[12:15], v[44:47]\n\t" \
  "ds_read_b64_tr_b16 v[190:191], v74 offset:2048\n\t" \
  "v_xor_b32 v1, 18432, v1\n\t" \
  "v_mfma_f32_16x16x32_bf16 v[48:51], a[252:255], v[12:15], v[48:51]\n\t" \
  "s_waitcnt vmcnt(6)\n\t" \
  "v_cmp_gt_u32 vcc, s97, v78\n\t" \
  "s_waitcnt lgkmcnt(5)\n\t" \
  "v_mfma_f32_16x16x32_bf16 v[36:39], v[224:227], v[4:7], v[36:39]\n\t" \
  "v_mfma_f32_16x16x32_bf16 v[40:43], v[228:231], v[4:7], v[40:43]\n\t" \
  "buffer_load_dwordx4 v0, s[40:43], s34 offen offset:2048 sc1 lds\n\t" \
  "v_mfma_f32_16x16x32_bf16 v[44:47], v[232:235], v[4:7], v[44:47]\n\t" \
  "v_mfma_f32_16x16x32_bf16 v[48:51], v[236:239], v[4:7], v[48:51]\n\t" \
  "buffer_load_dwordx4 v0, s[40:43], s34 offen offset:3072 sc1 lds\n\t" \
  "s_add_u32 m0, m0, 4096\n\t" \
  "s_add_u32 s34, s34, 4096\n\t" \
  "buffer_load_dwordx4 v0, s[40:43], s34 offen offset:0 sc1 lds\n\t" \
  "s_waitcnt vmcnt(10) lgkmcnt(0)\n\t" \
  "s_barrier\n\t" \
  "s_cbranch_vccnz SL_r3_%=\n\t" \
  "BK_r3_%=:\n\t" \
  "ds_read2st64_b64 v[12:15], v1 offset0:0 offset1:1\n\t" \
  "ds_read2st64_b64 v[4:7], v1 offset0:2 offset1:3\n\t" \
  "v_mfma_f32_16x16x32_bf16 v[36:39], v[240:243], v[8:11], v[36:39]\n\t" \
  "s_mul_i32 s33, s77, 18432\n\t" \
  "s_lshl_b32 s18, s84, 9\n\t" \
  "s_add_u32 s18, s18, s92\n\t" \
  "v_mfma_f32_16x16x32_bf16 v[40:43], v[244:247], v[8:11], v[40:43]\n\t" \
  "s_mov_b64 exec, 2\n\t" \
  "buffer_atomic_add v0, v0, s[56:59], s18 offen\n\t" \
  "s_mov_b64 exec, -1\n\t" \
  "v_mfma_f32_16x16x32_bf16 v[44:47], v[248:251], v[8:11], v[44:47]\n\t" \
  "s_add_u32 m0, s88, 18432\n\t" \
  "s_lshl_b32 s18, s85, 9\n\t" \
  "s_add_u32 s18, s18, s92\n\t" \
  "v_mfma_f32_16x16x32_bf16 v[48:51], v[252:255], v[8:11], v[48:51]\n\t" \
  "s_add_u32 s34, s33, s88\n\t" \
  "ds_read2st64_b64 v[8:11], v1 offset0:4 offset1:5\n\t" \
  "s_waitcnt lgkmcnt(2)\n\t" \
  "v_mfma_f32_16x16x32_bf16 v[20:23], a[0:3], v[12:15], v[20:23]\n\t" \
  "s_mov_b64 exec, 2\n\t" \
  "buffer_atomic_add v0, v0, s[56:59], s18 offen\n\t" \
  "s_mov_b64 exec, -1\n\t" \
  "v_mfma_f32_16x16x32_bf16 v[24:27], a[4:7], v[12:15], v[24:27]\n\t" \
  "s_lshl_b32 s18, s72, 9\n\t" \
  "s_add_u32 s18, s18, s92\n\t" \
  "v_mfma_f32_16x16x32_bf16 v[28:31], a[8:11], v[12:15], v[28:31]\n\t" \
  "s_mov_b64 exec, 2\n\t" \
  "buffer_atomic_add v0, v0, s[56:59], s18 offen\n\t" \
  "s_mov_b64 exec, -1\n\t" \
  "v_mfma_f32_16x16x32_bf16 v[32:35], a[12:15], v[12:15], v[32:35]\n\t" \
  "ds_read2st64_b64 v[12:15], v1 offset0:6 offset1:7\n\t" \
  "v_add_u32 v73, 114688, v72\n\t" \
  "s_waitcnt lgkmcnt(2)\n\t" \
  "v_mfma_f32_16x16x32_bf16 v[20:23], a[16:19], v[4:7], v[20:23]\n\t" \
  "buffer_load_dwordx4 v0, s[36:39], s34 offen offset:0 lds\n\t" \
  "v_mfma_f32_16x16x32_bf16 v[24:27], a[20:23], v[4:7], v[24:27]\n\t" \
  "v_max_f32 v36, 0, v36\n\t" \
  "v_max_f32 v37, 0, v37\n\t" \
  "v_mfma_f32_16x16x32_bf16 v[28:31], a[24:27], v[4:7], v[28:31]\n\t" \
  "v_max_f32 v38, 0, v38\n\t" \
  "v_max_f32 v39, 0, v39\n\t" \
  "v_mfma_f32_16x16x32_bf16 v[32:35], a[28:31], v[4:7], v[32:35]\n\t" \
  "ds_read2st64_b64 v[4:7], v1 offset0:8 offset1:9\n\t" \
  "v_mul_f32 v36, v36, v40\n\t" \
  "s_waitcnt lgkmcnt(2)\n\t" \
  "v_mfma_f32_16x16x32_bf16 v[20:23], a[32:35], v[8:11], v[20:23]\n\t" \
  "v_mul_f32 v37, v37, v41\n\t" \
  "v_mul_f32 v38, v38, v42\n\t" \
  "v_mfma_f32_16x16x32_bf16 v[24:27], a[36:39], v[8:11], v[24:27]\n\t" \
  "buffer_load_dwordx4 v0, s[36:39], s34 offen offset:1024 lds\n\t" \
  "v_mfma_f32_16x16x32_bf16 v[28:31], a[40:43], v[8:11], v[28:31]\n\t" \
  "v_mul_f32 v39, v39, v43\n\t" \
  "v_max_f32 v44, 0, v44\n\t" \
  "v_mfma_f32_16x16x32_bf16 v[32:35], a[44:47], v[8:11], v[32:35]\n\t" \
  "ds_read2st64_b64 v[8:11], v1 offset0:10 offset1:11\n\t" \
  "v_max_f32 v45, 0, v45\n\t" \
  "s_waitcnt lgkmcnt(2)\n\t" \
  "v_mfma_f32_16x16x32_bf16 v[20:23], a[48:51], v[12:15], v[20:23]\n\t" \
  "v_max_f32 v46, 0, v46\n\t" \
  "v_max_f32 v47, 0, v47\n\t" \
  "v_mfma_f32_16x16x32_bf16 v[24:27], a[52:55], v[12:15], v[24:27]\n\t" \
  "v_mul_f32 v44, v44, v48\n\t" \
  "v_mul_f32 v45, v45, v49\n\t" \
  "v_mfma_f32_16x16x32_bf16 v[28:31], a[56:59], v[12:15], v[28:31]\n\t" \
  "buffer_load_dwordx4 v0, s[36:39], s34 offen offset:2048 lds\n\t" \
  "v_mfma_f32_16x16x32_bf16 v[32:35], a[60:63], v[12:15], v[32:35]\n\t" \
  "ds_read2st64_b64 v[12:15], v1 offset0:12 offset1:13\n\t" \
  "v_mul_f32 v46, v46, v50\n\t" \
  "s_waitcnt lgkmcnt(2)\n\t" \
  "v_mfma_f32_16x16x32_bf16 v[20:23], a[64:67], v[4:7], v[20:23]\n\t" \
  "v_mul_f32 v47, v47, v51\n\t" \
  "v_cvt_pk_bf16_f32 v40, v36, v37\n\t" \
  "v_mfma_f32_16x16x32_bf16 v[24:27], a[68:71], v[4:7], v[24:27]\n\t" \
  "v_cvt_pk_bf16_f32 v41, v38, v39\n\t" \
  "v_cvt_pk_bf16_f32 v42, v44, v45\n\t" \
  "v_mfma_f32_16x16x32_bf16 v[28:31], a[72:75], v[4:7], v[28:31]\n\t" \
  "v_cvt_pk_bf16_f32 v43, v46, v47\n\t" \
  "v_xor_b32 v74, 32, v73\n\t" \
  "v_mfma_f32_16x16x32_bf16 v[32:35], a[76:79], v[4:7], v[32:35]\n\t" \
  "ds_read2st64_b64 v[4:7], v1 offset0:14 offset1:15\n\t" \
  "s_waitcnt lgkmcnt(2)\n\t" \
  "v_mfma_f32_16x16x32_bf16 v[20:23], a[80:83], v[8:11], v[20:23]\n\t" \
  "buffer_load_dwordx4 v0, s[36:39], s34 offen offset:3072 lds\n\t" \
  "v_mfma_f32_16x16x32_bf16 v[24:27], a[84:87], v[8:11], v[24:27]\n\t" \
  "s_add_u32 m0, s89, 18432\n\t" \
  "s_add_u32 s34, s33, s89\n\t" \
  "ds_read_b64_tr_b16 v[192:193], v73 offset:0\n\t" \
  "v_mfma_f32_16x16x32_bf16 v[28:31], a[88:91], v[8:11], v[28:31]\n\t" \
  "ds_read_b64_tr_b16 v[194:195], v73 offset:2048\n\t" \
  "v_mfma_f32_16x16x32_bf16 v[32:35], a[92:95], v[8:11], v[32:35]\n\t" \
  "ds_read2st64_b64 v[8:11], v1 offset0:16 offset1:17\n\t" \
  "s_waitcnt lgkmcnt(4)\n\t" \
  "v_mfma_f32_16x16x32_bf16 v[20:23], a[96:99], v[12:15], v[20:23]\n\t" \
  "ds_read_b64_tr_b16 v[196:197], v74 offset:0\n\t" \
  "v_mfma_f32_16x16x32_bf16 v[24:27], a[100:103], v[12:15], v[24:27]\n\t" \
  "buffer_load_dwordx4 v0, s[36:39], s34 offen lds\n\t" \
  "v_mfma_f32_16x16x32_bf16 v[28:31], a[104:107], v[12:15], v[28:31]\n\t" \
  "s_mov_b32 s64, s26\n\t" \
  "s_mul_i32 s34, s82, 147456\n\t" \
  "s_mul_i32 s35, s79, 147456\n\t" \
  "s_add_u32 s35, s35, s91\n\t" \
  "v_mfma_f32_16x16x32_bf16 v[32:35], a[108:111], v[12:15], v[32:35]\n\t" \
  "s_mov_b32 s65, s27\n\t" \
  "s_add_u32 s34, s34, s90\n\t" \
  "ds_read2st64_b64 v[12:15], v1 offset0:18 offset1:19\n\t" \
  "s_add_u32 m0, s30, 73728\n\t" \
  "s_waitcnt lgkmcnt(5)\n\t" \
  "v_mfma_f32_16x16x32_bf16 v[20:23], a[112:115], v[4:7], v[20:23]\n\t" \
  "s_and_b32 s65, s65, 0xffff\n\t" \
  "buffer_store_dwordx4 v[40:43], v0, s[44:47], s35 offen sc1\n\t" \
  "v_mfma_f32_16x16x32_bf16 v[24:27], a[116:119], v[4:7], v[24:27]\n\t" \
  "s_mov_b32 s66, 2359296\n\t" \
  "s_lshl_b32 s16, s81, 9\n\t" \
  "s_add_u32 s16, s16, s92\n\t" \
  "ds_read_b64_tr_b16 v[198:199], v74 offset:2048\n\t" \
  "v_mfma_f32_16x16x32_bf16 v[28:31], a[120:123], v[4:7], v[28:31]\n\t" \
  "s_mov_b32 s67, 0x20000\n\t" \
  "ds_read_b32 v17, v76 offset:1152\n\t" \
  "v_add_u32 v73, 122880, v72\n\t" \
  "v_mfma_f32_16x16x32_bf16 v[32:35], a[124:127], v[4:7], v[32:35]\n\t" \
  "s_lshr_b32 s22, s93, 5\n\t" \
  "ds_read2st64_b64 v[4:7], v1 offset0:20 offset1:21\n\t" \
  "s_waitcnt lgkmcnt(1)\n\t" \
  "v_mul_f32 v36, v17, v52\n\t" \
  "v_mfma_f32_16x16x32_bf16 v[20:23], a[128:131], v[8:11], v[20:23]\n\t" \
  "s_lshr_b32 s23, s93, 1\n\t" \
  "v_mul_f32 v37, v17, v53\n\t" \
  "v_mul_f32 v38, v17, v54\n\t" \
  "v_mfma_f32_16x16x32_bf16 v[24:27], a[132:135], v[8:11], v[24:27]\n\t" \
  "s_mov_b32 s25, 0x07060503\n\t" \
  "v_mul_f32 v39, v17, v55\n\t" \
  "v_mul_f32 v40, v17, v56\n\t" \
  "v_mfma_f32_16x16x32_bf16 v[28:31], a[136:139], v[8:11], v[28:31]\n\t" \
  "s_mov_b32 s26, 0x07060502\n\t" \
  "v_mul_f32 v41, v17, v57\n\t" \
  "v_mul_f32 v42, v17, v58\n\t" \
  "v_mfma_f32_16x16x32_bf16 v[32:35], a[140:143], v[8:11], v[32:35]\n\t" \
  "s_mov_b32 s27, 0x07060501\n\t" \
  "ds_read2st64_b64 v[8:11], v1 offset0:22 offset1:23\n\t" \
  "v_mul_f32 v43, v17, v59\n\t" \
  "v_mfma_f32_16x16x32_bf16 v[20:23], a[144:147], v[12:15], v[20:23]\n\t" \
  "s_mov_b32 s28, 0x04000c0c\n\t" \
  "v_mul_f32 v44, v17, v60\n\t" \
  "v_mul_f32 v45, v17, v61\n\t" \
  "v_mfma_f32_16x16x32_bf16 v[24:27], a[148:151], v[12:15], v[24:27]\n\t" \
  "s_mov_b32 s29, 0x0706000c\n\t" \
  "v_mul_f32 v46, v17, v62\n\t" \
  "v_mul_f32 v47, v17, v63\n\t" \
  "v_mfma_f32_16x16x32_bf16 v[28:31], a[152:155], v[12:15], v[28:31]\n\t" \
  "s_mov_b32 s84, -1\n\t" \
  "v_mul_f32 v48, v17, v64\n\t" \
  "v_mul_f32 v49, v17, v65\n\t" \
  "v_mfma_f32_16x16x32_bf16 v[32:35], a[156:159], v[12:15], v[32:35]\n\t" \
  "s_mov_b32 s85, -1\n\t" \
  "ds_read2st64_b64 v[12:15], v1 offset0:24 offset1:25\n\t" \
  "v_mul_f32 v50, v17, v66\n\t" \
  "s_waitcnt lgkmcnt(2)\n\t" \
  "v_mfma_f32_16x16x32_bf16 v[20:23], a[160:163], v[4:7], v[20:23]\n\t" \
  "s_mov_b32 s72, 9\n\t" \
  "v_mul_f32 v51, v17, v67\n\t" \
  "v_xor_b32 v74, 32, v73\n\t" \
  "v_mfma_f32_16x16x32_bf16 v[24:27], a[164:167], v[4:7], v[24:27]\n\t" \
  "s_mov_b32 s75, 0\n\t" \
  "buffer_load_dword v78, off, s[60:63], s16 offset:16 sc1\n\t" \
  "v_mfma_f32_16x16x32_bf16 v[28:31], a[168:171], v[4:7], v[28:31]\n\t" \
  "s_mov_b32 s94, 0\n\t" \
  "ds_read_b64_tr_b16 v[200:201], v73 offset:0\n\t" \
  "v_mfma_f32_16x16x32_bf16 v[32:35], a[172:175], v[4:7], v[32:35]\n\t" \
  "s_mov_b32 s95, 36864\n\t" \
  "ds_read2st64_b64 v[4:7], v1 offset0:26 offset1:27\n\t" \
  "s_waitcnt lgkmcnt(3)\n\t" \
  "v_mfma_f32_16x16x32_bf16 v[20:23], a[176:179], v[8:11], v[20:23]\n\t" \
  "s_add_u32 s18, s24, 36864\n\t" \
  "ds_read_b64_tr_b16 v[202:203], v73 offset:2048\n\t" \
  "v_mfma_f32_16x16x32_bf16 v[24:27], a[180:183], v[8:11], v[24:27]\n\t" \
  "v_add_u32 v2, s18, v0\n\t" \
  "buffer_load_dwordx4 v0, s[40:43], s34 offen offset:0 sc1 lds\n\t" \
  "v_mfma_f32_16x16x32_bf16 v[28:31], a[184:187], v[8:11], v[28:31]\n\t" \
  "ds_read_b64_tr_b16 v[204:205], v74 offset:0\n\t" \
  "v_mfma_f32_16x16x32_bf16 v[32:35], a[188:191], v[8:11], v[32:35]\n\t" \
  "ds_read2st64_b64 v[8:11], v1 offset0:28 offset1:29\n\t" \
  "s_waitcnt lgkmcnt(5)\n\t" \
  "v_mfma_f32_16x16x32_bf16 v[20:23], a[192:195], v[12:15], v[20:23]\n\t" \
  "ds_read_b64_tr_b16 v[206:207], v74 offset:2048\n\t" \
  "v_add_u32 v73, 131072, v72\n\t" \
  "v_mfma_f32_16x16x32_bf16 v[24:27], a[196:199], v[12:15], v[24:27]\n\t" \
  "buffer_load_dwordx4 v0, s[40:43], s34 offen offset:1024 sc1 lds\n\t" \
  "v_mfma_f32_16x16x32_bf16 v[28:31], a[200:203], v[12:15], v[28:31]\n\t" \
  "v_xor_b32 v74, 32, v73\n\t" \
  "ds_read_b64_tr_b16 v[208:209], v73 offset:0\n\t" \
  "v_mfma_f32_16x16x32_bf16 v[32:35], a[204:207], v[12:15], v[32:35]\n\t" \
  "ds_read2st64_b64 v[12:15], v1 offset0:30 offset1:31\n\t" \
  "s_waitcnt lgkmcnt(6)\n\t" \
  "v_mfma_f32_16x16x32_bf16 v[20:23], a[208:211], v[4:7], v[20:23]\n\t" \
  "ds_read_b64_tr_b16 v[210:211], v73 offset:2048\n\t" \
  "v_mfma_f32_16x16x32_bf16 v[24:27], a[212:215], v[4:7], v[24:27]\n\t" \
  "buffer_load_dwordx4 v0, s[40:43], s34 offen offset:2048 sc1 lds\n\t" \
  "v_mfma_f32_16x16x32_bf16 v[28:31], a[216:219], v[4:7], v[28:31]\n\t" \
  "ds_read_b64_tr_b16 v[212:213], v74 offset:0\n\t" \
  "v_mfma_f32_16x16x32_bf16 v[32:35], a[220:223], v[4:7], v[32:35]\n\t" \
  "ds_read2st64_b64 v[4:7], v1 offset0:32 offset1:33\n\t" \
  "s_waitcnt lgkmcnt(6)\n\t" \
  "v_mfma_f32_16x16x32_bf16 v[20:23], a[224:227], v[8:11], v[20:23]\n\t" \
  "ds_read_b64_tr_b16 v[214:215], v74 offset:2048\n\t" \
  "v_add_u32 v73, 139264, v72\n\t" \
  "v_mfma_f32_16x16x32_bf16 v[24:27], a[228:231], v[8:11], v[24:27]\n\t" \
  "buffer_load_dwordx4 v0, s[40:43], s34 offen offset:3072 sc1 lds\n\t" \
  "v_mfma_f32_16x16x32_bf16 v[28:31], a[232:235], v[8:11], v[28:31]\n\t" \
  "s_add_u32 m0, m0, 4096\n\t" \
  "s_add_u32 s34, s34, 4096\n\t" \
  "v_xor_b32 v74, 32, v73\n\t" \
  "v_mfma_f32_16x16x32_bf16 v[32:35], a[236:239], v[8:11], v[32:35]\n\t" \
  "ds_read2st64_b64 v[8:11], v1 offset0:34 offset1:35\n\t" \
  "s_waitcnt lgkmcnt(5)\n\t" \
  "v_mfma_f32_16x16x32_bf16 v[20:23], a[240:243], v[12:15], v[20:23]\n\t" \
  "ds_read_b64_tr_b16 v[216:217], v73 offset:0\n\t" \
  "v_mfma_f32_16x16x32_bf16 v[24:27], a[244:247], v[12:15], v[24:27]\n\t" \
  "buffer_load_dwordx4 v0, s[40:43], s34 offen offset:0 sc1 lds\n\t" \
  "v_mfma_f32_16x16x32_bf16 v[28:31], a[248:251], v[12:15], v[28:31]\n\t" \
  "ds_read_b64_tr_b16 v[218:219], v73 offset:2048\n\t" \
  "ds_read_b64_tr_b16 v[220:221], v74 offset:0\n\t" \
  "v_mfma_f32_16x16x32_bf16 v[32:35], a[252:255], v[12:15], v[32:35]\n\t" \
  "ds_read_b64_tr_b16 v[222:223], v74 offset:2048\n\t" \
  "s_waitcnt vmcnt(5)\n\t" \
  "v_cmp_gt_u32 vcc, s97, v78\n\t" \
  "s_waitcnt lgkmcnt(6)\n\t" \
  "v_mfma_f32_16x16x32_bf16 v[20:23], v[224:227], v[4:7], v[20:23]\n\t" \
  "v_xor_b32 v1, 18432, v1\n\t" \
  "v_mov_b32 v72, 0\n\t" \
  "v_mfma_f32_16x16x32_bf16 v[24:27], v[228:231], v[4:7], v[24:27]\n\t" \
  "buffer_load_dwordx4 v0, s[40:43], s34 offen offset:1024 sc1 lds\n\t" \
  "v_mfma_f32_16x16x32_bf16 v[28:31], v[232:235], v[4:7], v[28:31]\n\t" \
  "v_mov_b32 v73, 0\n\t" \
  "v_mov_b32 v74, 0\n\t" \
  "v_mov_b32 v75, 0\n\t" \
  "v_add_u32 v76, 1280, v76\n\t" \
  "v_mfma_f32_16x16x32_bf16 v[32:35], v[236:239], v[4:7], v[32:35]\n\t" \
  "buffer_load_dwordx4 v0, s[40:43], s34 offen offset:2048 sc1 lds\n\t" \
  "buffer_load_dwordx4 v0, s[40:43], s34 offen offset:3072 sc1 lds\n\t" \
  "s_add_u32 m0, m0, 4096\n\t" \
  "s_add_u32 s34, s34, 4096\n\t" \
  "buffer_load_dwordx4 v0, s[40:43], s34 offen offset:0 sc1 lds\n\t" \
  "s_waitcnt vmcnt(10) lgkmcnt(0)\n\t" \
  "s_barrier\n\t" \
  "s_cbranch_vccnz SL_r4_%=\n\t" \
  "BK_r4_%=:\n\t" \
  "MIX1_%=:\n\t" \
  "s_lshl_b32 s18, s79, 9\n\t" \
  "ds_read2st64_b64 v[4:7], v1 offset0:0 offset1:1\n\t" \
  "s_add_u32 s18, s18, s92\n\t" \
  "v_mfma_f32_16x16x32_bf16 v[56:59], v[212:215], v[12:15], v[56:59]\n\t" \
  "s_mul_i32 s33, s76, 18432\n\t" \
  "ds_read_b128 v[12:15], v2 offset:0\n\t" \
  "s_add_u32 m0, s94, s88\n\t" \
  "v_mfma_f32_16x16x32_bf16 v[20:23], v[240:243], v[8:11], v[20:23]\n\t" \
  "s_mov_b64 exec, 2\n\t" \
  "buffer_atomic_add v0, v0, s[56:59], s18 offen\n\t" \
  "s_mov_b64 exec, -1\n\t" \
  "v_mfma_f32_16x16x32_bf16 v[24:27], v[244:247], v[8:11], v[24:27]\n\t" \
  "s_add_u32 s34, s33, s88\n\t" \
  "v_mfma_f32_16x16x32_bf16 v[28:31], v[248:251], v[8:11], v[28:31]\n\t" \
  "buffer_load_dwordx4 v0, s[36:39], s34 offen offset:0 lds\n\t" \
  "v_mfma_f32_16x16x32_bf16 v[32:35], v[252:255], v[8:11], v[32:35]\n\t" \
  "ds_read2st64_b64 v[8:11], v1 offset0:2 offset1:3\n\t" \
  "v_mfma_f32_16x16x32_bf16 v[52:55], v[216:219], v[16:19], v[52:55]\n\t" \
  "v_mfma_f32_16x16x32_bf16 v[56:59], v[220:223], v[16:19], v[56:59]\n\t" \
  "ds_read_b128 v[16:19], v2 offset:1024\n\t" \
  "s_waitcnt lgkmcnt(3)\n\t" \
  "v_mfma_f32_16x16x32_bf16 v[36:39], a[0:3], v[4:7], v[36:39]\n\t" \
  "v_mfma_f32_16x16x32_bf16 v[40:43], a[4:7], v[4:7], v[40:43]\n\t" \
  "buffer_load_dwordx4 v0, s[36:39], s34 offen offset:1024 lds\n\t" \
  "v_mfma_f32_16x16x32_bf16 v[44:47], a[8:11], v[4:7], v[44:47]\n\t" \
  "s_mul_i32 s35, s78, 147456\n\t" \
  "v_max_f32 v20, 0, v20\n\t" \
  "v_max_f32 v21, 0, v21\n\t" \
  "v_mfma_f32_16x16x32_bf16 v[48:51], a[12:15], v[4:7], v[48:51]\n\t" \
  "s_add_u32 s35, s35, s91\n\t" \
  "ds_read2st64_b64 v[4:7], v1 offset0:4 offset1:5\n\t" \
  "v_max_f32 v22, 0, v22\n\t" \
  "s_waitcnt lgkmcnt(3)\n\t" \
  "v_mfma_f32_16x16x32_bf16 v[60:63], v[80:83], v[12:15], 0\n\t" \
  "v_max_f32 v23, 0, v23\n\t" \
  "v_mul_f32 v20, v20, v24\n\t" \
  "v_mfma_f32_16x16x32_bf16 v[64:67], v[84:87], v[12:15], 0\n\t" \
  "ds_read_b128 v[12:15], v2 offset:2048\n\t" \
  "v_mul_f32 v21, v21, v25\n\t" \
  "s_waitcnt lgkmcnt(3)\n\t" \
  "v_mfma_f32_16x16x32_bf16 v[36:39], a[16:19], v[8:11], v[36:39]\n\t" \
  "buffer_load_dwordx4 v0, s[36:39], s34 offen offset:2048 lds\n\t" \
  "v_mfma_f32_16x16x32_bf16 v[40:43], a[20:23], v[8:11], v[40:43]\n\t" \
  "s_and_b32 s98, s86, 3\n\t" \
  "v_mul_f32 v22, v22, v26\n\t" \
  "v_mul_f32 v23, v23, v27\n\t" \
  "v_mfma_f32_16x16x32_bf16 v[44:47], a[24:27], v[8:11], v[44:47]\n\t" \
  "s_mul_i32 s99, s85, 36864\n\t" \
  "v_max_f32 v28, 0, v28\n\t" \
  "v_max_f32 v29, 0, v29\n\t" \
  "v_mfma_f32_16x16x32_bf16 v[48:51], a[28:31], v[8:11], v[48:51]\n\t" \
  "ds_read2st64_b64 v[8:11], v1 offset0:6 offset1:7\n\t" \
  "v_max_f32 v30, 0, v30\n\t" \
  "s_waitcnt lgkmcnt(3)\n\t" \
  "v_mfma_f32_16x16x32_bf16 v[60:63], v[88:91], v[16:19], v[60:63]\n\t" \
  "v_max_f32 v31, 0, v31\n\t" \
  "v_mul_f32 v28, v28, v32\n\t" \
  "v_mfma_f32_16x16x32_bf16 v[64:67], v[92:95], v[16:19], v[64:67]\n\t" \
  "ds_read_b128 v[16:19], v2 offset:3072\n\t" \
  "v_mul_f32 v29, v29, v33\n\t" \
  "s_waitcnt lgkmcnt(3)\n\t" \
  "v_mfma_f32_16x16x32_bf16 v[36:39], a[32:35], v[4:7], v[36:39]\n\t" \
  "buffer_load_dwordx4 v0, s[36:39], s34 offen offset:3072 lds\n\t" \
  "v_mfma_f32_16x16x32_bf16 v[40:43], a[36:39], v[4:7], v[40:43]\n\t" \
  "s_add_u32 m0, s94, s89\n\t" \
  "s_add_u32 s34, s33, s89\n\t" \
  "v_mul_f32 v30, v30, v34\n\t" \
  "v_mfma_f32_16x16x32_bf16 v[44:47], a[40:43], v[4:7], v[44:47]\n\t" \
  "v_mul_f32 v31, v31, v35\n\t" \
  "v_cvt_pk_bf16_f32 v24, v20, v21\n\t" \
  "v_mfma_f32_16x16x32_bf16 v[48:51], a[44:47], v[4:7], v[48:51]\n\t" \
  "ds_read2st64_b64 v[4:7], v1 offset0:8 offset1:9\n\t" \
  "v_cvt_pk_bf16_f32 v25, v22, v23\n\t" \
  "s_waitcnt lgkmcnt(3)\n\t" \
  "v_mfma_f32_16x16x32_bf16 v[60:63], v[96:99], v[12:15], v[60:63]\n\t" \
  "v_cvt_pk_bf16_f32 v26, v28, v29\n\t" \
  "v_cvt_pk_bf16_f32 v27, v30, v31\n\t" \
  "v_mfma_f32_16x16x32_bf16 v[64:67], v[100:103], v[12:15], v[64:67]\n\t" \
  "ds_read_b128 v[12:15], v2 offset:4096\n\t" \
  "v_xor_b32 v32, 6144, v79\n\t" \
  "s_waitcnt lgkmcnt(3)\n\t" \
  "v_mfma_f32_16x16x32_bf16 v[36:39], a[48:51], v[8:11], v[36:39]\n\t" \
  "buffer_load_dwordx4 v0, s[36:39], s34 offen lds\n\t" \
  "v_mfma_f32_16x16x32_bf16 v[40:43], a[52:55], v[8:11], v[40:43]\n\t" \
  "s_xor_b32 s94, s94, 18432\n\t" \
  "ds_write_b128 v79, v[56:59]\n\t" \
  "v_mfma_f32_16x16x32_bf16 v[44:47], a[56:59], v[8:11], v[44:47]\n\t" \
  "buffer_store_dwordx4 v[24:27], v0, s[44:47], s35 offen sc1\n\t" \
  "v_mfma_f32_16x16x32_bf16 v[48:51], a[60:63], v[8:11], v[48:51]\n\t" \
  "s_mul_i32 s34, s81, 147456\n\t" \
  "ds_read2st64_b64 v[8:11], v1 offset0:10 offset1:11\n\t" \
  "ds_read_b128 v[32:35], v32\n\t" \
  "s_waitcnt lgkmcnt(5)\n\t" \
  "v_mfma_f32_16x16x32_bf16 v[60:63], v[104:107], v[16:19], v[60:63]\n\t" \
  "s_cmp_lg_u32 s85, -1\n\t" \
  "s_cselect_b32 s54, 14155776, 0\n\t" \
  "s_mul_i32 s98, s98, 4718592\n\t" \
  "v_mfma_f32_16x16x32_bf16 v[64:67], v[108:111], v[16:19], v[64:67]\n\t" \
  "s_add_u32 s99, s99, s93\n\t" \
  "ds_read_b128 v[16:19], v2 offset:5120\n\t" \
  "s_add_u32 s99, s99, s98\n\t" \
  "s_waitcnt lgkmcnt(5)\n\t" \
  "v_mfma_f32_16x16x32_bf16 v[36:39], a[64:67], v[4:7], v[36:39]\n\t" \
  "s_add_u32 s34, s34, s90\n\t" \
  "s_sub_u32 s33, s95, 36864\n\t" \
  "v_mfma_f32_16x16x32_bf16 v[40:43], a[68:71], v[4:7], v[40:43]\n\t" \
  "s_waitcnt lgkmcnt(1)\n\t" \
  "v_add_f32 v32, v32, v68\n\t" \
  "v_add_f32 v33, v33, v69\n\t" \
  "v_mfma_f32_16x16x32_bf16 v[44:47], a[72:75], v[4:7], v[44:47]\n\t" \
  "v_add_f32 v34, v34, v70\n\t" \
  "v_add_f32 v35, v35, v71\n\t" \
  "v_mfma_f32_16x16x32_bf16 v[48:51], a[76:79], v[4:7], v[48:51]\n\t" \
  "ds_read2st64_b64 v[4:7], v1 offset0:12 offset1:13\n\t" \
  "v_perm_b32 v68, v32, v35, s25\n\t" \
  "v_mfma_f32_16x16x32_bf16 v[60:63], v[112:115], v[12:15], v[60:63]\n\t" \
  "v_perm_b32 v69, v33, v35, s26\n\t" \
  "v_perm_b32 v70, v34, v35, s27\n\t" \
  "v_mfma_f32_16x16x32_bf16 v[64:67], v[116:119], v[12:15], v[64:67]\n\t" \
  "ds_read_b128 v[12:15], v2 offset:6144\n\t" \
  "v_mov_b32 v71, s74\n\t" \
  "v_mfma_f32_16x16x32_bf16 v[36:39], a[80:83], v[8:11], v[36:39]\n\t" \
  "buffer_store_dwordx4 v[68:71], v0, s[52:55], s99 offen sc1\n\t" \
  "v_mfma_f32_16x16x32_bf16 v[40:43], a[84:87], v[8:11], v[40:43]\n\t" \
  "v_perm_b32 v75, v72, v73, s28\n\t" \
  "v_add_f32 v68, v52, v72\n\t" \
  "v_mfma_f32_16x16x32_bf16 v[44:47], a[88:91], v[8:11], v[44:47]\n\t" \
  "v_perm_b32 v75, v75, v74, s29\n\t" \
  "v_add_f32 v69, v53, v73\n\t" \
  "v_mfma_f32_16x16x32_bf16 v[48:51], a[92:95], v[8:11], v[48:51]\n\t" \
  "ds_read2st64_b64 v[8:11], v1 offset0:14 offset1:15\n\t" \
  "v_add_f32 v70, v54, v74\n\t" \
  "s_waitcnt lgkmcnt(3)\n\t" \
  "v_mfma_f32_16x16x32_bf16 v[60:63], v[120:123], v[16:19], v[60:63]\n\t" \
  "s_cmp_lt_u32 s33, 36864\n\t" \
  "s_cselect_b32 s33, 110592, s33\n\t" \
  "v_add_f32 v71, v55, v75\n\t" \
  "v_mfma_f32_16x16x32_bf16 v[64:67], v[124:127], v[16:19], v[64:67]\n\t" \
  "s_bfe_u32 s19, s86, 0x20004\n\t" \
  "ds_read_b128 v[16:19], v2 offset:7168\n\t" \
  "s_mul_i32 s96, s83, 36864\n\t" \
  "s_waitcnt lgkmcnt(3)\n\t" \
  "v_mfma_f32_16x16x32_bf16 v[36:39], a[96:99], v[4:7], v[36:39]\n\t" \
  "s_cmp_lg_u32 s19, 0\n\t" \
  "s_cselect_b32 s50, 14155776, 0\n\t" \
  "s_cselect_b32 s75, s74, 0\n\t" \
  "v_mfma_f32_16x16x32_bf16 v[40:43], a[100:103], v[4:7], v[40:43]\n\t" \
  "s_mul_i32 s19, s19, 4718592\n\t" \
  "s_add_u32 s96, s96, s31\n\t" \
  "s_add_u32 s96, s96, s19\n\t" \
  "v_mfma_f32_16x16x32_bf16 v[44:47], a[104:107], v[4:7], v[44:47]\n\t" \
  "buffer_load_dwordx4 v[72:75], v0, s[48:51], s96 offen sc1\n\t" \
  "v_mfma_f32_16x16x32_bf16 v[48:51], a[108:111], v[4:7], v[48:51]\n\t" \
  "s_lshl_b32 s16, s80, 9\n\t" \
  "ds_read2st64_b64 v[4:7], v1 offset0:16 offset1:17\n\t" \
  "s_add_u32 s16, s16, s92\n\t" \
  "s_waitcnt lgkmcnt(3)\n\t" \
  "v_mfma_f32_16x16x32_bf16 v[60:63], v[128:131], v[12:15], v[60:63]\n\t" \
  "buffer_load_dword v78, off, s[60:63], s16 offset:16 sc1\n\t" \
  "v_mfma_f32_16x16x32_bf16 v[64:67], v[132:135], v[12:15], v[64:67]\n\t" \
  "s_add_u32 m0, s33, s30\n\t" \
  "ds_read_b128 v[12:15], v2 offset:8192\n\t" \
  "v_xor_b32 v79, 4096, v79\n\t" \
  "s_waitcnt lgkmcnt(3)\n\t" \
  "v_mfma_f32_16x16x32_bf16 v[36:39], a[112:115], v[8:11], v[36:39]\n\t" \
  "s_mov_b32 s85, s84\n\t" \
  "ds_read_b32 v3, v76\n\t" \
  "ds_read_b128 v[20:23], v77 offset:0\n\t" \
  "v_mfma_f32_16x16x32_bf16 v[40:43], a[116:119], v[8:11], v[40:43]\n\t" \
  "s_mov_b32 s84, s83\n\t" \
  "ds_read_b128 v[24:27], v77 offset:64\n\t" \
  "ds_read_b128 v[28:31], v77 offset:128\n\t" \
  "v_mfma_f32_16x16x32_bf16 v[44:47], a[120:123], v[8:11], v[44:47]\n\t" \
  "s_mov_b32 s83, s82\n\t" \
  "ds_read_b128 v[32:35], v77 offset:192\n\t" \
  "s_mov_b32 s82, s81\n\t" \
  "v_mfma_f32_16x16x32_bf16 v[48:51], a[124:127], v[8:11], v[48:51]\n\t" \
  "s_mov_b32 s81, s80\n\t" \
  "ds_read2st64_b64 v[8:11], v1 offset0:18 offset1:19\n\t" \
  "s_add_u32 s80, s81, 1\n\t" \
  "s_waitcnt lgkmcnt(8)\n\t" \
  "v_mfma_f32_16x16x32_bf16 v[60:63], v[136:139], v[16:19], v[60:63]\n\t" \
  "buffer_load_dwordx4 v0, s[40:43], s34 offen offset:0 sc1 lds\n\t" \
  "v_mfma_f32_16x16x32_bf16 v[64:67], v[140:143], v[16:19], v[64:67]\n\t" \
  "s_cmp_eq_u32 s80, s17\n\t" \
  "s_cselect_b32 s80, s20, s80\n\t" \
  "ds_read_b128 v[16:19], v2 offset:9216\n\t" \
  "s_waitcnt lgkmcnt(8)\n\t" \
  "v_mfma_f32_16x16x32_bf16 v[36:39], a[128:131], v[4:7], v[36:39]\n\t" \
  "s_mov_b32 s79, s78\n\t" \
  "s_waitcnt lgkmcnt(5)\n\t" \
  "v_mul_f32 v20, v3, v20\n\t" \
  "v_mul_f32 v21, v3, v21\n\t" \
  "v_mfma_f32_16x16x32_bf16 v[40:43], a[132:135], v[4:7], v[40:43]\n\t" \
  "s_mov_b32 s78, s77\n\t" \
  "v_mul_f32 v22, v3, v22\n\t" \
  "v_mul_f32 v23, v3, v23\n\t" \
  "v_mfma_f32_16x16x32_bf16 v[44:47], a[136:139], v[4:7], v[44:47]\n\t" \
  "s_mov_b32 s77, s76\n\t" \
  "s_waitcnt lgkmcnt(4)\n\t" \
  "v_mul_f32 v24, v3, v24\n\t" \
  "v_mul_f32 v25, v3, v25\n\t" \
  "v_mfma_f32_16x16x32_bf16 v[48:51], a[140:143], v[4:7], v[48:51]\n\t" \
  "s_add_u32 s76, s77, 1\n\t" \
  "ds_read2st64_b64 v[4:7], v1 offset0:20 offset1:21\n\t" \
  "v_mul_f32 v26, v3, v26\n\t" \
  "v_mfma_f32_16x16x32_bf16 v[60:63], v[144:147], v[12:15], v[60:63]\n\t" \
  "buffer_load_dwordx4 v0, s[40:43], s34 offen offset:1024 sc1 lds\n\t" \
  "v_mfma_f32_16x16x32_bf16 v[64:67], v[148:151], v[12:15], v[64:67]\n\t" \
  "ds_read_b128 v[12:15], v2 offset:10240\n\t" \
  "v_mul_f32 v27, v3, v27\n\t" \
  "s_waitcnt lgkmcnt(3)\n\t" \
  "v_mfma_f32_16x16x32_bf16 v[36:39], a[144:147], v[8:11], v[36:39]\n\t" \
  "v_mul_f32 v28, v3, v28\n\t" \
  "v_mul_f32 v29, v3, v29\n\t" \
  "v_mfma_f32_16x16x32_bf16 v[40:43], a[148:151], v[8:11], v[40:43]\n\t" \
  "v_mul_f32 v30, v3, v30\n\t" \
  "v_mul_f32 v31, v3, v31\n\t" \
  "v_mfma_f32_16x16x32_bf16 v[44:47], a[152:155], v[8:11], v[44:47]\n\t" \
  "v_mul_f32 v32, v3, v32\n\t" \
  "v_mul_f32 v33, v3, v33\n\t" \
  "v_mfma_f32_16x16x32_bf16 v[48:51], a[156:159], v[8:11], v[48:51]\n\t" \
  "ds_read2st64_b64 v[8:11], v1 offset0:22 offset1:23\n\t" \
  "v_mul_f32 v34, v3, v34\n\t" \
  "s_waitcnt lgkmcnt(3)\n\t" \
  "v_mfma_f32_16x16x32_bf16 v[60:63], v[152:155], v[16:19], v[60:63]\n\t" \
  "buffer_load_dwordx4 v0, s[40:43], s34 offen offset:2048 sc1 lds\n\t" \
  "v_mfma_f32_16x16x32_bf16 v[64:67], v[156:159], v[16:19], v[64:67]\n\t" \
  "ds_read_b128 v[16:19], v2 offset:11264\n\t" \
  "v_mul_f32 v35, v3, v35\n\t" \
  "s_waitcnt lgkmcnt(3)\n\t" \
  "v_mfma_f32_16x16x32_bf16 v[36:39], a[160:163], v[4:7], v[36:39]\n\t" \
  "s_cmp_eq_u32 s76, s17\n\t" \
  "s_cselect_b32 s76, s20, s76\n\t" \
  "s_add_u32 s72, s72, 1\n\t" \
  "v_mfma_f32_16x16x32_bf16 v[40:43], a[164:167], v[4:7], v[40:43]\n\t" \
  "s_lshr_b64 s[86:87], s[86:87], 2\n\t" \
  "s_add_u32 s95, s95, 36864\n\t" \
  "v_add_u32 v76, 128, v76\n\t" \
  "v_mfma_f32_16x16x32_bf16 v[44:47], a[168:171], v[4:7], v[44:47]\n\t" \
  "s_cmp_eq_u32 s95, 147456\n\t" \
  "s_cselect_b32 s95, 36864, s95\n\t" \
  "s_add_u32 s18, s95, s24\n\t" \
  "v_mfma_f32_16x16x32_bf16 v[48:51], a[172:175], v[4:7], v[48:51]\n\t" \
  "ds_read2st64_b64 v[4:7], v1 offset0:24 offset1:25\n\t" \
  "s_waitcnt lgkmcnt(3)\n\t" \
  "v_mfma_f32_16x16x32_bf16 v[60:63], v[160:163], v[12:15], v[60:63]\n\t" \
  "buffer_load_dwordx4 v0, s[40:43], s34 offen offset:3072 sc1 lds\n\t" \
  "v_mfma_f32_16x16x32_bf16 v[64:67], v[164:167], v[12:15], v[64:67]\n\t" \
  "s_add_u32 m0, m0, 4096\n\t" \
  "ds_read_b128 v[12:15], v2 offset:12288\n\t" \
  "s_add_u32 s34, s34, 4096\n\t" \
  "s_waitcnt lgkmcnt(3)\n\t" \
  "v_mfma_f32_16x16x32_bf16 v[36:39], a[176:179], v[8:11], v[36:39]\n\t" \
  "v_mfma_f32_16x16x32_bf16 v[40:43], a[180:183], v[8:11], v[40:43]\n\t" \
  "v_mfma_f32_16x16x32_bf16 v[44:47], a[184:187], v[8:11], v[44:47]\n\t" \
  "v_mfma_f32_16x16x32_bf16 v[48:51], a[188:191], v[8:11], v[48:51]\n\t" \
  "ds_read2st64_b64 v[8:11], v1 offset0:26 offset1:27\n\t" \
  "s_waitcnt lgkmcnt(3)\n\t" \
  "v_mfma_f32_16x16x32_bf16 v[60:63], v[168:171], v[16:19], v[60:63]\n\t" \
  "buffer_load_dwordx4 v0, s[40:43], s34 offen offset:0 sc1 lds\n\t" \
  "v_mfma_f32_16x16x32_bf16 v[64:67], v[172:175], v[16:19], v[64:67]\n\t" \
  "ds_read_b128 v[16:19], v2 offset:13312\n\t" \
  "s_waitcnt lgkmcnt(3)\n\t" \
  "v_mfma_f32_16x16x32_bf16 v[36:39], a[192:195], v[4:7], v[36:39]\n\t" \
  "v_mfma_f32_16x16x32_bf16 v[40:43], a[196:199], v[4:7], v[40:43]\n\t" \
  "v_mfma_f32_16x16x32_bf16 v[44:47], a[200:203], v[4:7], v[44:47]\n\t" \
  "v_mfma_f32_16x16x32_bf16 v[48:51], a[204:207], v[4:7], v[48:51]\n\t" \
  "ds_read2st64_b64 v[4:7], v1 offset0:28 offset1:29\n\t" \
  "s_waitcnt lgkmcnt(3)\n\t" \
  "v_mfma_f32_16x16x32_bf16 v[60:63], v[176:179], v[12:15], v[60:63]\n\t" \
  "buffer_load_dwordx4 v0, s[40:43], s34 offen offset:1024 sc1 lds\n\t" \
  "v_mfma_f32_16x16x32_bf16 v[64:67], v[180:183], v[12:15], v[64:67]\n\t" \
  "ds_read_b128 v[12:15], v2 offset:14336\n\t" \
  "s_waitcnt lgkmcnt(3)\n\t" \
  "v_mfma_f32_16x16x32_bf16 v[36:39], a[208:211], v[8:11], v[36:39]\n\t" \
  "v_mfma_f32_16x16x32_bf16 v[40:43], a[212:215], v[8:11], v[40:43]\n\t" \
  "v_mfma_f32_16x16x32_bf16 v[44:47], a[216:219], v[8:11], v[44:47]\n\t" \
  "v_mfma_f32_16x16x32_bf16 v[48:51], a[220:223], v[8:11], v[48:51]\n\t" \
  "ds_read2st64_b64 v[8:11], v1 offset0:30 offset1:31\n\t" \
  "s_waitcnt lgkmcnt(3)\n\t" \
  "v_mfma_f32_16x16x32_bf16 v[60:63], v[184:187], v[16:19], v[60:63]\n\t" \
  "buffer_load_dwordx4 v0, s[40:43], s34 offen offset:2048 sc1 lds\n\t" \
  "v_mfma_f32_16x16x32_bf16 v[64:67], v[188:191], v[16:19], v[64:67]\n\t" \
  "ds_read_b128 v[16:19], v2 offset:15360\n\t" \
  "s_waitcnt lgkmcnt(3)\n\t" \
  "v_mfma_f32_16x16x32_bf16 v[36:39], a[224:227], v[4:7], v[36:39]\n\t" \
  "v_mfma_f32_16x16x32_bf16 v[40:43], a[228:231], v[4:7], v[40:43]\n\t" \
  "v_mfma_f32_16x16x32_bf16 v[44:47], a[232:235], v[4:7], v[44:47]\n\t" \
  "v_mfma_f32_16x16x32_bf16 v[48:51], a[236:239], v[4:7], v[48:51]\n\t" \
  "ds_read2st64_b64 v[4:7], v1 offset0:32 offset1:33\n\t" \
  "s_waitcnt lgkmcnt(3)\n\t" \
  "v_mfma_f32_16x16x32_bf16 v[60:63], v[192:195], v[12:15], v[60:63]\n\t" \
  "buffer_load_dwordx4 v0, s[40:43], s34 offen offset:3072 sc1 lds\n\t" \
  "v_mfma_f32_16x16x32_bf16 v[64:67], v[196:199], v[12:15], v[64:67]\n\t" \
  "s_add_u32 m0, m0, 4096\n\t" \
  "ds_read_b128 v[12:15], v2 offset:16384\n\t" \
  "s_add_u32 s34, s34, 4096\n\t" \
  "s_waitcnt lgkmcnt(3)\n\t" \
  "v_mfma_f32_16x16x32_bf16 v[36:39], a[240:243], v[8:11], v[36:39]\n\t" \
  "v_mfma_f32_16x16x32_bf16 v[40:43], a[244:247], v[8:11], v[40:43]\n\t" \
  "s_waitcnt vmcnt(8)\n\t" \
  "v_cmp_gt_u32 s[70:71], s97, v78\n\t" \
  "v_cmp_ne_u32 vcc, s75, v75\n\t" \
  "v_mfma_f32_16x16x32_bf16 v[44:47], a[248:251], v[8:11], v[44:47]\n\t" \
  "s_or_b64 vcc, vcc, s[70:71]\n\t" \
  "s_cmp_eq_u32 s72, s73\n\t" \
  "s_cselect_b64 vcc, -1, vcc\n\t" \
  "v_mfma_f32_16x16x32_bf16 v[48:51], a[252:255], v[8:11], v[48:51]\n\t" \
  "ds_read2st64_b64 v[8:11], v1 offset0:34 offset1:35\n\t" \
  "s_waitcnt lgkmcnt(3)\n\t" \
  "v_mfma_f32_16x16x32_bf16 v[60:63], v[200:203], v[16:19], v[60:63]\n\t" \
  "buffer_load_dwordx4 v0, s[40:43], s34 offen offset:0 sc1 lds\n\t" \
  "v_mfma_f32_16x16x32_bf16 v[64:67], v[204:207], v[16:19], v[64:67]\n\t" \
  "ds_read_b128 v[16:19], v2 offset:17408\n\t" \
  "v_xor_b32 v1, 18432, v1\n\t" \
  "s_waitcnt lgkmcnt(3)\n\t" \
  "v_mfma_f32_16x16x32_bf16 v[36:39], v[224:227], v[4:7], v[36:39]\n\t" \
  "v_add_u32 v2, s18, v0\n\t" \
  "v_mfma_f32_16x16x32_bf16 v[40:43], v[228:231], v[4:7], v[40:43]\n\t" \
  "v_mfma_f32_16x16x32_bf16 v[44:47], v[232:235], v[4:7], v[44:47]\n\t" \
  "v_mfma_f32_16x16x32_bf16 v[48:51], v[236:239], v[4:7], v[48:51]\n\t" \
  "s_waitcnt lgkmcnt(2)\n\t" \
  "v_mfma_f32_16x16x32_bf16 v[60:63], v[208:211], v[12:15], v[60:63]\n\t" \
  "s_waitcnt vmcnt(12) lgkmcnt(0)\n\t" \
  "s_barrier\n\t" \
  "s_cbranch_vccnz X1_%=\n\t" \
  "MIX0_%=:\n\t" \
  "s_lshl_b32 s18, s79, 9\n\t" \
  "ds_read2st64_b64 v[4:7], v1 offset0:0 offset1:1\n\t" \
  "s_add_u32 s18, s18, s92\n\t" \
  "v_mfma_f32_16x16x32_bf16 v[64:67], v[212:215], v[12:15], v[64:67]\n\t" \
  "s_mul_i32 s33, s76, 18432\n\t" \
  "ds_read_b128 v[12:15], v2 offset:0\n\t" \
  "s_add_u32 m0, s94, s88\n\t" \
  "v_mfma_f32_16x16x32_bf16 v[36:39], v[240:243], v[8:11], v[36:39]\n\t" \
  "s_mov_b64 exec, 2\n\t" \
  "buffer_atomic_add v0, v0, s[56:59], s18 offen\n\t" \
  "s_mov_b64 exec, -1\n\t" \
  "v_mfma_f32_16x16x32_bf16 v[40:43], v[244:247], v[8:11], v[40:43]\n\t" \
  "s_add_u32 s34, s33, s88\n\t" \
  "v_mfma_f32_16x16x32_bf16 v[44:47], v[248:251], v[8:11], v[44:47]\n\t" \
  "buffer_load_dwordx4 v0, s[36:39], s34 offen offset:0 lds\n\t" \
  "v_mfma_f32_16x16x32_bf16 v[48:51], v[252:255], v[8:11], v[48:51]\n\t" \
  "ds_read2st64_b64 v[8:11], v1 offset0:2 offset1:3\n\t" \
  "v_mfma_f32_16x16x32_bf16 v[60:63], v[216:219], v[16:19], v[60:63]\n\t" \
  "v_mfma_f32_16x16x32_bf16 v[64:67], v[220:223], v[16:19], v[64:67]\n\t" \
  "ds_read_b128 v[16:19], v2 offset:1024\n\t" \
  "s_waitcnt lgkmcnt(3)\n\t" \
  "v_mfma_f32_16x16x32_bf16 v[20:23], a[0:3], v[4:7], v[20:23]\n\t" \
  "v_mfma_f32_16x16x32_bf16 v[24:27], a[4:7], v[4:7], v[24:27]\n\t" \
  "buffer_load_dwordx4 v0, s[36:39], s34 offen offset:1024 lds\n\t" \
  "v_mfma_f32_16x16x32_bf16 v[28:31], a[8:11], v[4:7], v[28:31]\n\t" \
  "s_mul_i32 s35, s78, 147456\n\t" \
  "v_max_f32 v36, 0, v36\n\t" \
  "v_max_f32 v37, 0, v37\n\t" \
  "v_mfma_f32_16x16x32_bf16 v[32:35], a[12:15], v[4:7], v[32:35]\n\t" \
  "s_add_u32 s35, s35, s91\n\t" \
  "ds_read2st64_b64 v[4:7], v1 offset0:4 offset1:5\n\t" \
  "v_max_f32 v38, 0, v38\n\t" \
  "s_waitcnt lgkmcnt(3)\n\t" \
  "v_mfma_f32_16x16x32_bf16 v[52:55], v[80:83], v[12:15], 0\n\t" \
  "v_max_f32 v39, 0, v39\n\t" \
  "v_mul_f32 v36, v36, v40\n\t" \
  "v_mfma_f32_16x16x32_bf16 v[56:59], v[84:87], v[12:15], 0\n\t" \
  "ds_read_b128 v[12:15], v2 offset:2048\n\t" \
  "v_mul_f32 v37, v37, v41\n\t" \
  "s_waitcnt lgkmcnt(3)\n\t" \
  "v_mfma_f32_16x16x32_bf16 v[20:23], a[16:19], v[8:11], v[20:23]\n\t" \
  "buffer_load_dwordx4 v0, s[36:39], s34 offen offset:2048 lds\n\t" \
  "v_mfma_f32_16x16x32_bf16 v[24:27], a[20:23], v[8:11], v[24:27]\n\t" \
  "s_and_b32 s98, s86, 3\n\t" \
  "v_mul_f32 v38, v38, v42\n\t" \
  "v_mul_f32 v39, v39, v43\n\t" \
  "v_mfma_f32_16x16x32_bf16 v[28:31], a[24:27], v[8:11], v[28:31]\n\t" \
  "s_mul_i32 s99, s85, 36864\n\t" \
  "v_max_f32 v44, 0, v44\n\t" \
  "v_max_f32 v45, 0, v45\n\t" \
  "v_mfma_f32_16x16x32_bf16 v[32:35], a[28:31], v[8:11], v[32:35]\n\t" \
  "ds_read2st64_b64 v[8:11], v1 offset0:6 offset1:7\n\t" \
  "v_max_f32 v46, 0, v46\n\t" \
  "s_waitcnt lgkmcnt(3)\n\t" \
  "v_mfma_f32_16x16x32_bf16 v[52:55], v[88:91], v[16:19], v[52:55]\n\t" \
  "v_max_f32 v47, 0, v47\n\t" \
  "v_mul_f32 v44, v44, v48\n\t" \
  "v_mfma_f32_16x16x32_bf16 v[56:59], v[92:95], v[16:19], v[56:59]\n\t" \
  "ds_read_b128 v[16:19], v2 offset:3072\n\t" \
  "v_mul_f32 v45, v45, v49\n\t" \
  "s_waitcnt lgkmcnt(3)\n\t" \
  "v_mfma_f32_16x16x32_bf16 v[20:23], a[32:35], v[4:7], v[20:23]\n\t" \
  "buffer_load_dwordx4 v0, s[36:39], s34 offen offset:3072 lds\n\t" \
  "v_mfma_f32_16x16x32_bf16 v[24:27], a[36:39], v[4:7], v[24:27]\n\t" \
  "s_add_u32 m0, s94, s89\n\t" \
  "s_add_u32 s34, s33, s89\n\t" \
  "v_mul_f32 v46, v46, v50\n\t" \
  "v_mfma_f32_16x16x32_bf16 v[28:31], a[40:43], v[4:7], v[28:31]\n\t" \
  "v_mul_f32 v47, v47, v51\n\t" \
  "v_cvt_pk_bf16_f32 v40, v36, v37\n\t" \
  "v_mfma_f32_16x16x32_bf16 v[32:35], a[44:47], v[4:7], v[32:35]\n\t" \
  "ds_read2st64_b64 v[4:7], v1 offset0:8 offset1:9\n\t" \
  "v_cvt_pk_bf16_f32 v41, v38, v39\n\t" \
  "s_waitcnt lgkmcnt(3)\n\t" \
  "v_mfma_f32_16x16x32_bf16 v[52:55], v[96:99], v[12:15], v[52:55]\n\t" \
  "v_cvt_pk_bf16_f32 v42, v44, v45\n\t" \
  "v_cvt_pk_bf16_f32 v43, v46, v47\n\t" \
  "v_mfma_f32_16x16x32_bf16 v[56:59], v[100:103], v[12:15], v[56:59]\n\t" \
  "ds_read_b128 v[12:15], v2 offset:4096\n\t" \
  "v_xor_b32 v48, 6144, v79\n\t" \
  "s_waitcnt lgkmcnt(3)\n\t" \
  "v_mfma_f32_16x16x32_bf16 v[20:23], a[48:51], v[8:11], v[20:23]\n\t" \
  "buffer_load_dwordx4 v0, s[36:39], s34 offen lds\n\t" \
  "v_mfma_f32_16x16x32_bf16 v[24:27], a[52:55], v[8:11], v[24:27]\n\t" \
  "s_xor_b32 s94, s94, 18432\n\t" \
  "ds_write_b128 v79, v[64:67]\n\t" \
  "v_mfma_f32_16x16x32_bf16 v[28:31], a[56:59], v[8:11], v[28:31]\n\t" \
  "buffer_store_dwordx4 v[40:43], v0, s[44:47], s35 offen sc1\n\t" \
  "v_mfma_f32_16x16x32_bf16 v[32:35], a[60:63], v[8:11], v[32:35]\n\t" \
  "s_mul_i32 s34, s81, 147456\n\t" \
  "ds_read2st64_b64 v[8:11], v1 offset0:10 offset1:11\n\t" \
  "ds_read_b128 v[48:51], v48\n\t" \
  "s_waitcnt lgkmcnt(5)\n\t" \
  "v_mfma_f32_16x16x32_bf16 v[52:55], v[104:107], v[16:19], v[52:55]\n\t" \
  "s_cmp_lg_u32 s85, -1\n\t" \
  "s_cselect_b32 s54, 14155776, 0\n\t" \
  "s_mul_i32 s98, s98, 4718592\n\t" \
  "v_mfma_f32_16x16x32_bf16 v[56:59], v[108:111], v[16:19], v[56:59]\n\t" \
  "s_add_u32 s99, s99, s93\n\t" \
  "ds_read_b128 v[16:19], v2 offset:5120\n\t" \
  "s_add_u32 s99, s99, s98\n\t" \
  "s_waitcnt lgkmcnt(5)\n\t" \
  "v_mfma_f32_16x16x32_bf16 v[20:23], a[64:67], v[4:7], v[20:23]\n\t" \
  "s_add_u32 s34, s34, s90\n\t" \
  "s_sub_u32 s33, s95, 36864\n\t" \
  "v_mfma_f32_16x16x32_bf16 v[24:27], a[68:71], v[4:7], v[24:27]\n\t" \
  "s_waitcnt lgkmcnt(1)\n\t" \
  "v_add_f32 v48, v48, v68\n\t" \
  "v_add_f32 v49, v49, v69\n\t" \
  "v_mfma_f32_16x16x32_bf16 v[28:31], a[72:75], v[4:7], v[28:31]\n\t" \
  "v_add_f32 v50, v50, v70\n\t" \
  "v_add_f32 v51, v51, v71\n\t" \
  "v_mfma_f32_16x16x32_bf16 v[32:35], a[76:79], v[4:7], v[32:35]\n\t" \
  "ds_read2st64_b64 v[4:7], v1 offset0:12 offset1:13\n\t" \
  "v_perm_b32 v68, v48, v51, s25\n\t" \
  "v_mfma_f32_16x16x32_bf16 v[52:55], v[112:115], v[12:15], v[52:55]\n\t" \
  "v_perm_b32 v69, v49, v51, s26\n\t" \
  "v_perm_b32 v70, v50, v51, s27\n\t" \
  "v_mfma_f32_16x16x32_bf16 v[56:59], v[116:119], v[12:15], v[56:59]\n\t" \
  "ds_read_b128 v[12:15], v2 offset:6144\n\t" \
  "v_mov_b32 v71, s74\n\t" \
  "v_mfma_f32_16x16x32_bf16 v[20:23], a[80:83], v[8:11], v[20:23]\n\t" \
  "buffer_store_dwordx4 v[68:71], v0, s[52:55], s99 offen sc1\n\t" \
  "v_mfma_f32_16x16x32_bf16 v[24:27], a[84:87], v[8:11], v[24:27]\n\t" \
  "v_perm_b32 v75, v72, v73, s28\n\t" \
  "v_add_f32 v68, v60, v72\n\t" \
  "v_mfma_f32_16x16x32_bf16 v[28:31], a[88:91], v[8:11], v[28:31]\n\t" \
  "v_perm_b32 v75, v75, v74, s29\n\t" \
  "v_add_f32 v69, v61, v73\n\t" \
  "v_mfma_f32_16x16x32_bf16 v[32:35], a[92:95], v[8:11], v[32:35]\n\t" \
  "ds_read2st64_b64 v[8:11], v1 offset0:14 offset1:15\n\t" \
  "v_add_f32 v70, v62, v74\n\t" \
  "s_waitcnt lgkmcnt(3)\n\t" \
  "v_mfma_f32_16x16x32_bf16 v[52:55], v[120:123], v[16:19], v[52:55]\n\t" \
  "s_cmp_lt_u32 s33, 36864\n\t" \
  "s_cselect_b32 s33, 110592, s33\n\t" \
  "v_add_f32 v71, v63, v75\n\t" \
  "v_mfma_f32_16x16x32_bf16 v[56:59], v[124:127], v[16:19], v[56:59]\n\t" \
  "s_bfe_u32 s19, s86, 0x20004\n\t" \
  "ds_read_b128 v[16:19], v2 offset:7168\n\t" \
  "s_mul_i32 s96, s83, 36864\n\t" \
  "s_waitcnt lgkmcnt(3)\n\t" \
  "v_mfma_f32_16x16x32_bf16 v[20:23], a[96:99], v[4:7], v[20:23]\n\t" \
  "s_cmp_lg_u32 s19, 0\n\t" \
  "s_cselect_b32 s50, 14155776, 0\n\t" \
  "s_cselect_b32 s75, s74, 0\n\t" \
  "v_mfma_f32_16x16x32_bf16 v[24:27], a[100:103], v[4:7], v[24:27]\n\t" \
  "s_mul_i32 s19, s19, 4718592\n\t" \
  "s_add_u32 s96, s96, s31\n\t" \
  "s_add_u32 s96, s96, s19\n\t" \
  "v_mfma_f32_16x16x32_bf16 v[28:31], a[104:107], v[4:7], v[28:31]\n\t" \
  "buffer_load_dwordx4 v[72:75], v0, s[48:51], s96 offen sc1\n\t" \
  "v_mfma_f32_16x16x32_bf16 v[32:35], a[108:111], v[4:7], v[32:35]\n\t" \
  "s_lshl_b32 s16, s80, 9\n\t" \
  "ds_read2st64_b64 v[4:7], v1 offset0:16 offset1:17\n\t" \
  "s_add_u32 s16, s16, s92\n\t" \
  "s_waitcnt lgkmcnt(3)\n\t" \
  "v_mfma_f32_16x16x32_bf16 v[52:55], v[128:131], v[12:15], v[52:55]\n\t" \
  "buffer_load_dword v78, off, s[60:63], s16 offset:16 sc1\n\t" \
  "v_mfma_f32_16x16x32_bf16 v[56:59], v[132:135], v[12:15], v[56:59]\n\t" \
  "s_add_u32 m0, s33, s30\n\t" \
  "ds_read_b128 v[12:15], v2 offset:8192\n\t" \
  "v_xor_b32 v79, 4096, v79\n\t" \
  "s_waitcnt lgkmcnt(3)\n\t" \
  "v_mfma_f32_16x16x32_bf16 v[20:23], a[112:115], v[8:11], v[20:23]\n\t" \
  "s_mov_b32 s85, s84\n\t" \
  "ds_read_b32 v3, v76\n\t" \
  "ds_read_b128 v[36:39], v77 offset:0\n\t" \
  "v_mfma_f32_16x16x32_bf16 v[24:27], a[116:119], v[8:11], v[24:27]\n\t" \
  "s_mov_b32 s84, s83\n\t" \
  "ds_read_b128 v[40:43], v77 offset:64\n\t" \
  "ds_read_b128 v[44:47], v77 offset:128\n\t" \
  "v_mfma_f32_16x16x32_bf16 v[28:31], a[120:123], v[8:11], v[28:31]\n\t" \
  "s_mov_b32 s83, s82\n\t" \
  "ds_read_b128 v[48:51], v77 offset:192\n\t" \
  "s_mov_b32 s82, s81\n\t" \
  "v_mfma_f32_16x16x32_bf16 v[32:35], a[124:127], v[8:11], v[32:35]\n\t" \
  "s_mov_b32 s81, s80\n\t" \
  "ds_read2st64_b64 v[8:11], v1 offset0:18 offset1:19\n\t" \
  "s_add_u32 s80, s81, 1\n\t" \
  "s_waitcnt lgkmcnt(8)\n\t" \
  "v_mfma_f32_16x16x32_bf16 v[52:55], v[136:139], v[16:19], v[52:55]\n\t" \
  "buffer_load_dwordx4 v0, s[40:43], s34 offen offset:0 sc1 lds\n\t" \
  "v_mfma_f32_16x16x32_bf16 v[56:59], v[140:143], v[16:19], v[56:59]\n\t" \
  "s_cmp_eq_u32 s80, s17\n\t" \
  "s_cselect_b32 s80, s20, s80\n\t" \
  "ds_read_b128 v[16:19], v2 offset:9216\n\t" \
  "s_waitcnt lgkmcnt(8)\n\t" \
  "v_mfma_f32_16x16x32_bf16 v[20:23], a[128:131], v[4:7], v[20:23]\n\t" \
  "s_mov_b32 s79, s78\n\t" \
  "s_waitcnt lgkmcnt(5)\n\t" \
  "v_mul_f32 v36, v3, v36\n\t" \
  "v_mul_f32 v37, v3, v37\n\t" \
  "v_mfma_f32_16x16x32_bf16 v[24:27], a[132:135], v[4:7], v[24:27]\n\t" \
  "s_mov_b32 s78, s77\n\t" \
  "v_mul_f32 v38, v3, v38\n\t" \
  "v_mul_f32 v39, v3, v39\n\t" \
  "v_mfma_f32_16x16x32_bf16 v[28:31], a[136:139], v[4:7], v[28:31]\n\t" \
  "s_mov_b32 s77, s76\n\t" \
  "s_waitcnt lgkmcnt(4)\n\t" \
  "v_mul_f32 v40, v3, v40\n\t" \
  "v_mul_f32 v41, v3, v41\n\t" \
  "v_mfma_f32_16x16x32_bf16 v[32:35], a[140:143], v[4:7], v[32:35]\n\t" \
  "s_add_u32 s76, s77, 1\n\t" \
  "ds_read2st64_b64 v[4:7], v1 offset0:20 offset1:21\n\t" \
  "v_mul_f32 v42, v3, v42\n\t" \
  "v_mfma_f32_16x16x32_bf16 v[52:55], v[144:147], v[12:15], v[52:55]\n\t" \
  "buffer_load_dwordx4 v0, s[40:43], s34 offen offset:1024 sc1 lds\n\t" \
  "v_mfma_f32_16x16x32_bf16 v[56:59], v[148:151], v[12:15], v[56:59]\n\t" \
  "ds_read_b128 v[12:15], v2 offset:10240\n\t" \
  "v_mul_f32 v43, v3, v43\n\t" \
  "s_waitcnt lgkmcnt(3)\n\t" \
  "v_mfma_f32_16x16x32_bf16 v[20:23], a[144:147], v[8:11], v[20:23]\n\t" \
  "v_mul_f32 v44, v3, v44\n\t" \
  "v_mul_f32 v45, v3, v45\n\t" \
  "v_mfma_f32_16x16x32_bf16 v[24:27], a[148:151], v[8:11], v[24:27]\n\t" \
  "v_mul_f32 v46, v3, v46\n\t" \
  "v_mul_f32 v47, v3, v47\n\t" \
  "v_mfma_f32_16x16x32_bf16 v[28:31], a[152:155], v[8:11], v[28:31]\n\t" \
  "v_mul_f32 v48, v3, v48\n\t" \
  "v_mul_f32 v49, v3, v49\n\t" \
  "v_mfma_f32_16x16x32_bf16 v[32:35], a[156:159], v[8:11], v[32:35]\n\t" \
  "ds_read2st64_b64 v[8:11], v1 offset0:22 offset1:23\n\t" \
  "v_mul_f32 v50, v3, v50\n\t" \
  "s_waitcnt lgkmcnt(3)\n\t" \
  "v_mfma_f32_16x16x32_bf16 v[52:55], v[152:155], v[16:19], v[52:55]\n\t" \
  "buffer_load_dwordx4 v0, s[40:43], s34 offen offset:2048 sc1 lds\n\t" \
  "v_mfma_f32_16x16x32_bf16 v[56:59], v[156:159], v[16:19], v[56:59]\n\t" \
  "ds_read_b128 v[16:19], v2 offset:11264\n\t" \
  "v_mul_f32 v51, v3, v51\n\t" \
  "s_waitcnt lgkmcnt(3)\n\t" \
  "v_mfma_f32_16x16x32_bf16 v[20:23], a[160:163], v[4:7], v[20:23]\n\t" \
  "s_cmp_eq_u32 s76, s17\n\t" \
  "s_cselect_b32 s76, s20, s76\n\t" \
  "s_add_u32 s72, s72, 1\n\t" \
  "v_mfma_f32_16x16x32_bf16 v[24:27], a[164:167], v[4:7], v[24:27]\n\t" \
  "s_lshr_b64 s[86:87], s[86:87], 2\n\t" \
  "s_add_u32 s95, s95, 36864\n\t" \
  "v_add_u32 v76, 128, v76\n\t" \
  "v_mfma_f32_16x16x32_bf16 v[28:31], a[168:171], v[4:7], v[28:31]\n\t" \
  "s_cmp_eq_u32 s95, 147456\n\t" \
  "s_cselect_b32 s95, 36864, s95\n\t" \
  "s_add_u32 s18, s95, s24\n\t" \
  "v_mfma_f32_16x16x32_bf16 v[32:35], a[172:175], v[4:7], v[32:35]\n\t" \
  "ds_read2st64_b64 v[4:7], v1 offset0:24 offset1:25\n\t" \
  "s_waitcnt lgkmcnt(3)\n\t" \
  "v_mfma_f32_16x16x32_bf16 v[52:55], v[160:163], v[12:15], v[52:55]\n\t" \
  "buffer_load_dwordx4 v0, s[40:43], s34 offen offset:3072 sc1 lds\n\t" \
  "v_mfma_f32_16x16x32_bf16 v[56:59], v[164:167], v[12:15], v[56:59]\n\t" \
  "s_add_u32 m0, m0, 4096\n\t" \
  "ds_read_b128 v[12:15], v2 offset:12288\n\t" \
  "s_add_u32 s34, s34, 4096\n\t" \
  "s_waitcnt lgkmcnt(3)\n\t" \
  "v_mfma_f32_16x16x32_bf16 v[20:23], a[176:179], v[8:11], v[20:23]\n\t" \
  "v_mfma_f32_16x16x32_bf16 v[24:27], a[180:183], v[8:11], v[24:27]\n\t" \
  "v_mfma_f32_16x16x32_bf16 v[28:31], a[184:187], v[8:11], v[28:31]\n\t" \
  "v_mfma_f32_16x16x32_bf16 v[32:35], a[188:191], v[8:11], v[32:35]\n\t" \
  "ds_read2st64_b64 v[8:11], v1 offset0:26 offset1:27\n\t" \
  "s_waitcnt lgkmcnt(3)\n\t" \
  "v_mfma_f32_16x16x32_bf16 v[52:55], v[168:171], v[16:19], v[52:55]\n\t" \
  "buffer_load_dwordx4 v0, s[40:43], s34 offen offset:0 sc1 lds\n\t" \
  "v_mfma_f32_16x16x32_bf16 v[56:59], v[172:175], v[16:19], v[56:59]\n\t" \
  "ds_read_b128 v[16:19], v2 offset:13312\n\t" \
  "s_waitcnt lgkmcnt(3)\n\t" \
  "v_mfma_f32_16x16x32_bf16 v[20:23], a[192:195], v[4:7], v[20:23]\n\t" \
  "v_mfma_f32_16x16x32_bf16 v[24:27], a[196:199], v[4:7], v[24:27]\n\t" \
  "v_mfma_f32_16x16x32_bf16 v[28:31], a[200:203], v[4:7], v[28:31]\n\t" \
  "v_mfma_f32_16x16x32_bf16 v[32:35], a[204:207], v[4:7], v[32:35]\n\t" \
  "ds_read2st64_b64 v[4:7], v1 offset0:28 offset1:29\n\t" \
  "s_waitcnt lgkmcnt(3)\n\t" \
  "v_mfma_f32_16x16x32_bf16 v[52:55], v[176:179], v[12:15], v[52:55]\n\t" \
  "buffer_load_dwordx4 v0, s[40:43], s34 offen offset:1024 sc1 lds\n\t" \
  "v_mfma_f32_16x16x32_bf16 v[56:59], v[180:183], v[12:15], v[56:59]\n\t" \
  "ds_read_b128 v[12:15], v2 offset:14336\n\t" \
  "s_waitcnt lgkmcnt(3)\n\t" \
  "v_mfma_f32_16x16x32_bf16 v[20:23], a[208:211], v[8:11], v[20:23]\n\t" \
  "v_mfma_f32_16x16x32_bf16 v[24:27], a[212:215], v[8:11], v[24:27]\n\t" \
  "v_mfma_f32_16x16x32_bf16 v[28:31], a[216:219], v[8:11], v[28:31]\n\t" \
  "v_mfma_f32_16x16x32_bf16 v[32:35], a[220:223], v[8:11], v[32:35]\n\t" \
  "ds_read2st64_b64 v[8:11], v1 offset0:30 offset1:31\n\t" \
  "s_waitcnt lgkmcnt(3)\n\t" \
  "v_mfma_f32_16x16x32_bf16 v[52:55], v[184:187], v[16:19], v[52:55]\n\t" \
  "buffer_load_dwordx4 v0, s[40:43], s34 offen offset:2048 sc1 lds\n\t" \
  "v_mfma_f32_16x16x32_bf16 v[56:59], v[188:191], v[16:19], v[56:59]\n\t" \
  "ds_read_b128 v[16:19], v2 offset:15360\n\t" \
  "s_waitcnt lgkmcnt(3)\n\t" \
  "v_mfma_f32_16x16x32_bf16 v[20:23], a[224:227], v[4:7], v[20:23]\n\t" \
  "v_mfma_f32_16x16x32_bf16 v[24:27], a[228:231], v[4:7], v[24:27]\n\t" \
  "v_mfma_f32_16x16x32_bf16 v[28:31], a[232:235], v[4:7], v[28:31]\n\t" \
  "v_mfma_f32_16x16x32_bf16 v[32:35], a[236:239], v[4:7], v[32:35]\n\t" \
  "ds_read2st64_b64 v[4:7], v1 offset0:32 offset1:33\n\t" \
  "s_waitcnt lgkmcnt(3)\n\t" \
  "v_mfma_f32_16x16x32_bf16 v[52:55], v[192:195], v[12:15], v[52:55]\n\t" \
  "buffer_load_dwordx4 v0, s[40:43], s34 offen offset:3072 sc1 lds\n\t" \
  "v_mfma_f32_16x16x32_bf16 v[56:59], v[196:199], v[12:15], v[56:59]\n\t" \
  "s_add_u32 m0, m0, 4096\n\t" \
  "ds_read_b128 v[12:15], v2 offset:16384\n\t" \
  "s_add_u32 s34, s34, 4096\n\t" \
  "s_waitcnt lgkmcnt(3)\n\t" \
  "v_mfma_f32_16x16x32_bf16 v[20:23], a[240:243], v[8:11], v[20:23]\n\t" \
  "v_mfma_f32_16x16x32_bf16 v[24:27], a[244:247], v[8:11], v[24:27]\n\t" \
  "s_waitcnt vmcnt(8)\n\t" \
  "v_cmp_gt_u32 s[70:71], s97, v78\n\t" \
  "v_cmp_ne_u32 vcc, s75, v75\n\t" \
  "v_mfma_f32_16x16x32_bf16 v[28:31], a[248:251], v[8:11], v[28:31]\n\t" \
  "s_or_b64 vcc, vcc, s[70:71]\n\t" \
  "s_cmp_eq_u32 s72, s73\n\t" \
  "s_cselect_b64 vcc, -1, vcc\n\t" \
  "v_mfma_f32_16x16x32_bf16 v[32:35], a[252:255], v[8:11], v[32:35]\n\t" \
  "ds_read2st64_b64 v[8:11], v1 offset0:34 offset1:35\n\t" \
  "s_waitcnt lgkmcnt(3)\n\t" \
  "v_mfma_f32_16x16x32_bf16 v[52:55], v[200:203], v[16:19], v[52:55]\n\t" \
  "buffer_load_dwordx4 v0, s[40:43], s34 offen offset:0 sc1 lds\n\t" \
  "v_mfma_f32_16x16x32_bf16 v[56:59], v[204:207], v[16:19], v[56:59]\n\t" \
  "ds_read_b128 v[16:19], v2 offset:17408\n\t" \
  "v_xor_b32 v1, 18432, v1\n\t" \
  "s_waitcnt lgkmcnt(3)\n\t" \
  "v_mfma_f32_16x16x32_bf16 v[20:23], v[224:227], v[4:7], v[20:23]\n\t" \
  "v_add_u32 v2, s18, v0\n\t" \
  "v_mfma_f32_16x16x32_bf16 v[24:27], v[228:231], v[4:7], v[24:27]\n\t" \
  "v_mfma_f32_16x16x32_bf16 v[28:31], v[232:235], v[4:7], v[28:31]\n\t" \
  "v_mfma_f32_16x16x32_bf16 v[32:35], v[236:239], v[4:7], v[32:35]\n\t" \
  "s_waitcnt lgkmcnt(2)\n\t" \
  "v_mfma_f32_16x16x32_bf16 v[52:55], v[208:211], v[12:15], v[52:55]\n\t" \
  "s_waitcnt vmcnt(12) lgkmcnt(0)\n\t" \
  "s_barrier\n\t" \
  "s_cbranch_vccz MIX1_%=\n\t" \
  "s_call_b64 s[68:69], WAIT_FQ_%=\n\t" \
  "s_cmp_lg_u32 s72, s73\n\t" \
  "s_cbranch_scc1 MIX1_%=\n\t" \
  "TAIL_%=:\n\t" \
  "s_mov_b32 s54, 14155776\n\t" \
  "s_mov_b32 s50, 14155776\n\t" \
  "s_mov_b32 s75, s74\n\t" \
  "s_mul_i32 s34, s81, 147456\n\t" \
  "s_lshl_b32 s18, s79, 9\n\t" \
  "s_add_u32 s18, s18, s92\n\t" \
  "v_mfma_f32_16x16x32_bf16 v[56:59], v[212:215], v[12:15], v[56:59]\n\t" \
  "s_add_u32 s34, s34, s90\n\t" \
  "ds_read_b128 v[12:15], v2 offset:0\n\t" \
  "ds_read_b128 v[4:7], v2 offset:2048\n\t" \
  "v_mfma_f32_16x16x32_bf16 v[20:23], v[240:243], v[8:11], v[20:23]\n\t" \
  "s_sub_u32 s33, s95, 36864\n\t" \
  "ds_read_b128 v[224:227], v2 offset:4096\n\t" \
  "v_subrev_u32 v76, 1536, v76\n\t" \
  "v_mfma_f32_16x16x32_bf16 v[24:27], v[244:247], v[8:11], v[24:27]\n\t" \
  "s_mul_i32 s35, s78, 147456\n\t" \
  "ds_read_b128 v[228:231], v2 offset:5120\n\t" \
  "s_add_u32 s35, s35, s91\n\t" \
  "v_mfma_f32_16x16x32_bf16 v[28:31], v[248:251], v[8:11], v[28:31]\n\t" \
  "s_mov_b64 exec, 2\n\t" \
  "buffer_atomic_add v0, v0, s[56:59], s18 offen\n\t" \
  "s_mov_b64 exec, -1\n\t" \
  "v_mfma_f32_16x16x32_bf16 v[32:35], v[252:255], v[8:11], v[32:35]\n\t" \
  "s_and_b32 s98, s86, 3\n\t" \
  "ds_read_b128 v[8:11], v2 offset:3072\n\t" \
  "v_and_b32 v1, 0x1ff, v1\n\t" \
  "v_mfma_f32_16x16x32_bf16 v[52:55], v[216:219], v[16:19], v[52:55]\n\t" \
  "s_lshl_b32 s16, s80, 9\n\t" \
  "s_add_u32 s16, s16, s92\n\t" \
  "v_bfe_u32 v239, v0, 4, 4\n\t" \
  "v_mfma_f32_16x16x32_bf16 v[56:59], v[220:223], v[16:19], v[56:59]\n\t" \
  "s_mul_i32 s99, s85, 36864\n\t" \
  "ds_read_b128 v[16:19], v2 offset:1024\n\t" \
  "v_mul_u32_u24 v239, 1152, v239\n\t" \
  "s_waitcnt lgkmcnt(5)\n\t" \
  "v_mfma_f32_16x16x32_bf16 v[60:63], v[80:83], v[12:15], 0\n\t" \
  "buffer_load_dword v78, off, s[60:63], s16 offset:16 sc1\n\t" \
  "v_mfma_f32_16x16x32_bf16 v[64:67], v[84:87], v[12:15], 0\n\t" \
  "s_bfe_u32 s19, s86, 0x20004\n\t" \
  "ds_read_b128 v[12:15], v2 offset:6144\n\t" \
  "s_mul_i32 s94, s83, 36864\n\t" \
  "s_waitcnt lgkmcnt(1)\n\t" \
  "v_mfma_f32_16x16x32_bf16 v[60:63], v[88:91], v[16:19], v[60:63]\n\t" \
  "s_mul_i32 s19, s19, 4718592\n\t" \
  "s_add_u32 s94, s94, s31\n\t" \
  "s_add_u32 s94, s94, s19\n\t" \
  "v_mfma_f32_16x16x32_bf16 v[64:67], v[92:95], v[16:19], v[64:67]\n\t" \
  "s_mul_i32 s98, s98, 4718592\n\t" \
  "ds_read_b128 v[16:19], v2 offset:7168\n\t" \
  "v_max_f32 v20, 0, v20\n\t" \
  "v_mfma_f32_16x16x32_bf16 v[60:63], v[96:99], v[4:7], v[60:63]\n\t" \
  "buffer_load_dwordx4 v[248:251], v0, s[48:51], s94 offen sc1\n\t" \
  "v_mfma_f32_16x16x32_bf16 v[64:67], v[100:103], v[4:7], v[64:67]\n\t" \
  "s_add_u32 s99, s99, s93\n\t" \
  "ds_read_b128 v[4:7], v2 offset:8192\n\t" \
  "v_max_f32 v21, 0, v21\n\t" \
  "ds_write_b128 v79, v[56:59]\n\t" \
  "v_mfma_f32_16x16x32_bf16 v[60:63], v[104:107], v[8:11], v[60:63]\n\t" \
  "s_add_u32 s99, s99, s98\n\t" \
  "v_max_f32 v22, 0, v22\n\t" \
  "v_max_f32 v23, 0, v23\n\t" \
  "v_mfma_f32_16x16x32_bf16 v[64:67], v[108:111], v[8:11], v[64:67]\n\t" \
  "ds_read_b128 v[8:11], v2 offset:9216\n\t" \
  "v_mul_f32 v20, v20, v24\n\t" \
  "v_mfma_f32_16x16x32_bf16 v[60:63], v[112:115], v[224:227], v[60:63]\n\t" \
  "v_mul_f32 v21, v21, v25\n\t" \
  "v_mul_f32 v22, v22, v26\n\t" \
  "v_mfma_f32_16x16x32_bf16 v[64:67], v[116:119], v[224:227], v[64:67]\n\t" \
  "ds_read_b128 v[224:227], v2 offset:10240\n\t" \
  "v_mul_f32 v23, v23, v27\n\t" \
  "v_mfma_f32_16x16x32_bf16 v[60:63], v[120:123], v[228:231], v[60:63]\n\t" \
  "v_max_f32 v28, 0, v28\n\t" \
  "v_max_f32 v29, 0, v29\n\t" \
  "v_mfma_f32_16x16x32_bf16 v[64:67], v[124:127], v[228:231], v[64:67]\n\t" \
  "ds_read_b128 v[228:231], v2 offset:11264\n\t" \
  "v_max_f32 v30, 0, v30\n\t" \
  "s_waitcnt lgkmcnt(6)\n\t" \
  "v_mfma_f32_16x16x32_bf16 v[60:63], v[128:131], v[12:15], v[60:63]\n\t" \
  "v_max_f32 v31, 0, v31\n\t" \
  "v_mul_f32 v28, v28, v32\n\t" \
  "v_mfma_f32_16x16x32_bf16 v[64:67], v[132:135], v[12:15], v[64:67]\n\t" \
  "ds_read_b128 v[12:15], v2 offset:12288\n\t" \
  "v_mul_f32 v29, v29, v33\n\t" \
  "s_waitcnt lgkmcnt(6)\n\t" \
  "v_mfma_f32_16x16x32_bf16 v[60:63], v[136:139], v[16:19], v[60:63]\n\t" \
  "v_mul_f32 v30, v30, v34\n\t" \
  "v_mul_f32 v31, v31, v35\n\t" \
  "v_mfma_f32_16x16x32_bf16 v[64:67], v[140:143], v[16:19], v[64:67]\n\t" \
  "ds_read_b128 v[16:19], v2 offset:13312\n\t" \
  "v_cvt_pk_bf16_f32 v24, v20, v21\n\t" \
  "s_waitcnt lgkmcnt(6)\n\t" \
  "v_mfma_f32_16x16x32_bf16 v[60:63], v[144:147], v[4:7], v[60:63]\n\t" \
  "v_cvt_pk_bf16_f32 v25, v22, v23\n\t" \
  "v_cvt_pk_bf16_f32 v26, v28, v29\n\t" \
  "v_mfma_f32_16x16x32_bf16 v[64:67], v[148:151], v[4:7], v[64:67]\n\t" \
  "ds_read_b128 v[4:7], v2 offset:14336\n\t" \
  "v_cvt_pk_bf16_f32 v27, v30, v31\n\t" \
  "s_waitcnt lgkmcnt(5)\n\t" \
  "v_mfma_f32_16x16x32_bf16 v[60:63], v[152:155], v[8:11], v[60:63]\n\t" \
  "buffer_store_dwordx4 v[24:27], v0, s[44:47], s35 offen sc1\n\t" \
  "v_mfma_f32_16x16x32_bf16 v[64:67], v[156:159], v[8:11], v[64:67]\n\t" \
  "ds_read_b128 v[8:11], v2 offset:15360\n\t" \
  "v_lshrrev_b32 v238, 8, v0\n\t" \
  "s_waitcnt lgkmcnt(5)\n\t" \
  "v_mfma_f32_16x16x32_bf16 v[60:63], v[160:163], v[224:227], v[60:63]\n\t" \
  "s_cmp_lt_u32 s33, 36864\n\t" \
  "s_cselect_b32 s33, 110592, s33\n\t" \
  "v_lshl_add_u32 v239, v238, 3, v239\n\t" \
  "v_mfma_f32_16x16x32_bf16 v[64:67], v[164:167], v[224:227], v[64:67]\n\t" \
  "s_add_u32 m0, s33, s30\n\t" \
  "ds_read_b128 v[224:227], v2 offset:16384\n\t" \
  "v_xor_b32 v232, 6144, v79\n\t" \
  "s_waitcnt lgkmcnt(5)\n\t" \
  "v_mfma_f32_16x16x32_bf16 v[60:63], v[168:171], v[228:231], v[60:63]\n\t" \
  "buffer_load_dwordx4 v0, s[40:43], s34 offen offset:0 sc1 lds\n\t" \
  "v_mfma_f32_16x16x32_bf16 v[64:67], v[172:175], v[228:231], v[64:67]\n\t" \
  "ds_read_b128 v[228:231], v2 offset:17408\n\t" \
  "ds_read_b128 v[232:235], v232\n\t" \
  "s_waitcnt lgkmcnt(6)\n\t" \
  "v_mfma_f32_16x16x32_bf16 v[60:63], v[176:179], v[12:15], v[60:63]\n\t" \
  "buffer_load_dwordx4 v0, s[40:43], s34 offen offset:1024 sc1 lds\n\t" \
  "v_mfma_f32_16x16x32_bf16 v[64:67], v[180:183], v[12:15], v[64:67]\n\t" \
  "buffer_load_dwordx4 v0, s[40:43], s34 offen offset:2048 sc1 lds\n\t" \
  "s_waitcnt lgkmcnt(5)\n\t" \
  "v_mfma_f32_16x16x32_bf16 v[60:63], v[184:187], v[16:19], v[60:63]\n\t" \
  "buffer_load_dwordx4 v0, s[40:43], s34 offen offset:3072 sc1 lds\n\t" \
  "v_mfma_f32_16x16x32_bf16 v[64:67], v[188:191], v[16:19], v[64:67]\n\t" \
  "s_add_u32 m0, m0, 4096\n\t" \
  "s_add_u32 s34, s34, 4096\n\t" \
  "s_waitcnt lgkmcnt(0)\n\t" \
  "v_add_f32 v232, v232, v68\n\t" \
  "v_add_f32 v233, v233, v69\n\t" \
  "v_mfma_f32_16x16x32_bf16 v[60:63], v[192:195], v[4:7], v[60:63]\n\t" \
  "buffer_load_dwordx4 v0, s[40:43], s34 offen offset:0 sc1 lds\n\t" \
  "v_add_f32 v234, v234, v70\n\t" \
  "v_add_f32 v235, v235, v71\n\t" \
  "v_perm_b32 v68, v232, v235, s25\n\t" \
  "v_perm_b32 v69, v233, v235, s26\n\t" \
  "v_perm_b32 v70, v234, v235, s27\n\t" \
  "v_mov_b32 v71, s74\n\t" \
  "v_mfma_f32_16x16x32_bf16 v[64:67], v[196:199], v[4:7], v[64:67]\n\t" \
  "buffer_load_dwordx4 v0, s[40:43], s34 offen offset:1024 sc1 lds\n\t" \
  "buffer_load_dwordx4 v0, s[40:43], s34 offen offset:2048 sc1 lds\n\t" \
  "buffer_load_dwordx4 v0, s[40:43], s34 offen offset:3072 sc1 lds\n\t" \
  "s_add_u32 m0, m0, 4096\n\t" \
  "s_add_u32 s34, s34, 4096\n\t" \
  "buffer_load_dwordx4 v0, s[40:43], s34 offen offset:0 sc1 lds\n\t" \
  "buffer_store_dwordx4 v[68:71], v0, s[52:55], s99 offen sc1\n\t" \
  "s_nop 0\n\t" \
  "v_perm_b32 v75, v72, v73, s28\n\t" \
  "v_add_f32 v68, v52, v72\n\t" \
  "v_perm_b32 v75, v75, v74, s29\n\t" \
  "v_add_f32 v69, v53, v73\n\t" \
  "v_add_f32 v70, v54, v74\n\t" \
  "v_add_f32 v71, v55, v75\n\t" \
  "s_mov_b32 s85, s84\n\t" \
  "s_mov_b32 s84, s83\n\t" \
  "s_mov_b32 s83, s82\n\t" \
  "s_mov_b32 s82, s81\n\t" \
  "s_mov_b32 s81, s80\n\t" \
  "s_add_u32 s80, s81, 1\n\t" \
  "s_cmp_eq_u32 s80, s17\n\t" \
  "s_cselect_b32 s80, s20, s80\n\t" \
  "s_lshr_b64 s[86:87], s[86:87], 2\n\t" \
  "s_add_u32 s95, s95, 36864\n\t" \
  "s_cmp_eq_u32 s95, 147456\n\t" \
  "s_cselect_b32 s95, 36864, s95\n\t" \
  "s_add_u32 s18, s95, s24\n\t" \
  "v_add_u32 v76, 128, v76\n\t" \
  "v_add_u32 v2, s18, v0\n\t" \
  "v_xor_b32 v79, 4096, v79\n\t" \
  "s_waitcnt vmcnt(12)\n\t" \
  "v_cmp_gt_u32 vcc, s97, v78\n\t" \
  "s_waitcnt vmcnt(10) lgkmcnt(0)\n\t" \
  "s_barrier\n\t" \
  "s_cbranch_vccnz SL_t0_%=\n\t" \
  "BK_t0_%=:\n\t" \
  "s_lshl_b32 s18, s78, 9\n\t" \
  "ds_read_b128 v[12:15], v2 offset:0\n\t" \
  "ds_read_b128 v[16:19], v2 offset:1024\n\t" \
  "v_mfma_f32_16x16x32_bf16 v[60:63], v[200:203], v[8:11], v[60:63]\n\t" \
  "s_mul_i32 s34, s81, 147456\n\t" \
  "s_add_u32 s18, s18, s92\n\t" \
  "ds_read_b128 v[4:7], v2 offset:2048\n\t" \
  "v_mfma_f32_16x16x32_bf16 v[64:67], v[204:207], v[8:11], v[64:67]\n\t" \
  "s_add_u32 s34, s34, s90\n\t" \
  "ds_read_b128 v[8:11], v2 offset:3072\n\t" \
  "s_sub_u32 s33, s95, 36864\n\t" \
  "v_mfma_f32_16x16x32_bf16 v[60:63], v[208:211], v[224:227], v[60:63]\n\t" \
  "s_mov_b64 exec, 2\n\t" \
  "buffer_atomic_add v0, v0, s[56:59], s18 offen\n\t" \
  "s_mov_b64 exec, -1\n\t" \
  "v_mfma_f32_16x16x32_bf16 v[64:67], v[212:215], v[224:227], v[64:67]\n\t" \
  "s_lshl_b32 s16, s80, 9\n\t" \
  "ds_read_b128 v[224:227], v2 offset:4096\n\t" \
  "s_add_u32 s16, s16, s92\n\t" \
  "v_mfma_f32_16x16x32_bf16 v[60:63], v[216:219], v[228:231], v[60:63]\n\t" \
  "buffer_load_dword v78, off, s[60:63], s16 offset:16 sc1\n\t" \
  "v_mfma_f32_16x16x32_bf16 v[64:67], v[220:223], v[228:231], v[64:67]\n\t" \
  "s_bfe_u32 s19, s86, 0x20004\n\t" \
  "ds_read_b128 v[228:231], v2 offset:5120\n\t" \
  "s_mul_i32 s96, s83, 36864\n\t" \
  "s_waitcnt lgkmcnt(5)\n\t" \
  "v_mfma_f32_16x16x32_bf16 v[52:55], v[80:83], v[12:15], 0\n\t" \
  "s_mul_i32 s19, s19, 4718592\n\t" \
  "s_add_u32 s96, s96, s31\n\t" \
  "s_add_u32 s96, s96, s19\n\t" \
  "v_mfma_f32_16x16x32_bf16 v[56:59], v[84:87], v[12:15], 0\n\t" \
  "s_cmp_lt_u32 s33, 36864\n\t" \
  "s_cselect_b32 s33, 110592, s33\n\t" \
  "ds_read_b128 v[12:15], v2 offset:6144\n\t" \
  "s_waitcnt lgkmcnt(5)\n\t" \
  "v_mfma_f32_16x16x32_bf16 v[52:55], v[88:91], v[16:19], v[52:55]\n\t" \
  "buffer_load_dwordx4 v[72:75], v0, s[48:51], s96 offen sc1\n\t" \
  "v_mfma_f32_16x16x32_bf16 v[56:59], v[92:95], v[16:19], v[56:59]\n\t" \
  "s_add_u32 m0, s33, s30\n\t" \
  "ds_read_b128 v[16:19], v2 offset:7168\n\t" \
  "v_xor_b32 v232, 6144, v79\n\t" \
  "s_waitcnt lgkmcnt(5)\n\t" \
  "v_mfma_f32_16x16x32_bf16 v[52:55], v[96:99], v[4:7], v[52:55]\n\t" \
  "s_and_b32 s98, s86, 3\n\t" \
  "ds_read_b128 v[232:235], v232\n\t" \
  "s_mul_i32 s99, s85, 36864\n\t" \
  "v_mfma_f32_16x16x32_bf16 v[56:59], v[100:103], v[4:7], v[56:59]\n\t" \
  "s_mul_i32 s98, s98, 4718592\n\t" \
  "ds_read_b128 v[4:7], v2 offset:8192\n\t" \
  "s_add_u32 s99, s99, s93\n\t" \
  "s_waitcnt lgkmcnt(6)\n\t" \
  "v_mfma_f32_16x16x32_bf16 v[52:55], v[104:107], v[8:11], v[52:55]\n\t" \
  "buffer_load_dwordx4 v0, s[40:43], s34 offen offset:0 sc1 lds\n\t" \
  "v_mfma_f32_16x16x32_bf16 v[56:59], v[108:111], v[8:11], v[56:59]\n\t" \
  "s_add_u32 s99, s99, s98\n\t" \
  "ds_read_b128 v[8:11], v2 offset:9216\n\t" \
  "s_waitcnt lgkmcnt(2)\n\t" \
  "v_add_f32 v232, v232, v68\n\t" \
  "v_mfma_f32_16x16x32_bf16 v[52:55], v[112:115], v[224:227], v[52:55]\n\t" \
  "v_add_f32 v233, v233, v69\n\t" \
  "v_add_f32 v234, v234, v70\n\t" \
  "v_mfma_f32_16x16x32_bf16 v[56:59], v[116:119], v[224:227], v[56:59]\n\t" \
  "ds_read_b128 v[224:227], v2 offset:10240\n\t" \
  "v_add_f32 v235, v235, v71\n\t" \
  "v_mfma_f32_16x16x32_bf16 v[52:55], v[120:123], v[228:231], v[52:55]\n\t" \
  "buffer_load_dwordx4 v0, s[40:43], s34 offen offset:1024 sc1 lds\n\t" \
  "v_mfma_f32_16x16x32_bf16 v[56:59], v[124:127], v[228:231], v[56:59]\n\t" \
  "ds_read_b128 v[228:231], v2 offset:11264\n\t" \
  "v_perm_b32 v68, v232, v235, s25\n\t" \
  "v_mfma_f32_16x16x32_bf16 v[52:55], v[128:131], v[12:15], v[52:55]\n\t" \
  "v_perm_b32 v69, v233, v235, s26\n\t" \
  "v_perm_b32 v70, v234, v235, s27\n\t" \
  "v_mfma_f32_16x16x32_bf16 v[56:59], v[132:135], v[12:15], v[56:59]\n\t" \
  "ds_read_b128 v[12:15], v2 offset:12288\n\t" \
  "v_mov_b32 v71, s74\n\t" \
  "v_mfma_f32_16x16x32_bf16 v[52:55], v[136:139], v[16:19], v[52:55]\n\t" \
  "buffer_load_dwordx4 v0, s[40:43], s34 offen offset:2048 sc1 lds\n\t" \
  "v_mfma_f32_16x16x32_bf16 v[56:59], v[140:143], v[16:19], v[56:59]\n\t" \
  "ds_read_b128 v[16:19], v2 offset:13312\n\t" \
  "s_waitcnt lgkmcnt(5)\n\t" \
  "v_mfma_f32_16x16x32_bf16 v[52:55], v[144:147], v[4:7], v[52:55]\n\t" \
  "buffer_store_dwordx4 v[68:71], v0, s[52:55], s99 offen sc1\n\t" \
  "v_mfma_f32_16x16x32_bf16 v[56:59], v[148:151], v[4:7], v[56:59]\n\t" \
  "ds_read_b128 v[4:7], v2 offset:14336\n\t" \
  "s_waitcnt vmcnt(18)\n\t" \
  "v_cmp_ne_u32 vcc, s75, v251\n\t" \
  "s_waitcnt lgkmcnt(5)\n\t" \
  "v_mfma_f32_16x16x32_bf16 v[52:55], v[152:155], v[8:11], v[52:55]\n\t" \
  "buffer_load_dwordx4 v0, s[40:43], s34 offen offset:3072 sc1 lds\n\t" \
  "v_mfma_f32_16x16x32_bf16 v[56:59], v[156:159], v[8:11], v[56:59]\n\t" \
  "s_add_u32 m0, m0, 4096\n\t" \
  "ds_read_b128 v[8:11], v2 offset:15360\n\t" \
  "s_add_u32 s34, s34, 4096\n\t" \
  "s_waitcnt lgkmcnt(5)\n\t" \
  "v_mfma_f32_16x16x32_bf16 v[52:55], v[160:163], v[224:227], v[52:55]\n\t" \
  "s_cbranch_vccnz SL_q1_%=\n\t" \
  "BK_q1_%=:\n\t" \
  "ds_write_b128 v79, v[64:67]\n\t" \
  "v_mfma_f32_16x16x32_bf16 v[56:59], v[164:167], v[224:227], v[56:59]\n\t" \
  "s_nop 0\n\t" \
  "ds_read_b128 v[224:227], v2 offset:16384\n\t" \
  "v_perm_b32 v251, v248, v249, s28\n\t" \
  "s_waitcnt lgkmcnt(6)\n\t" \
  "v_mfma_f32_16x16x32_bf16 v[52:55], v[168:171], v[228:231], v[52:55]\n\t" \
  "buffer_load_dwordx4 v0, s[40:43], s34 offen offset:0 sc1 lds\n\t" \
  "v_mfma_f32_16x16x32_bf16 v[56:59], v[172:175], v[228:231], v[56:59]\n\t" \
  "ds_read_b128 v[228:231], v2 offset:17408\n\t" \
  "v_add_f32 v68, v60, v248\n\t" \
  "s_waitcnt lgkmcnt(6)\n\t" \
  "v_mfma_f32_16x16x32_bf16 v[52:55], v[176:179], v[12:15], v[52:55]\n\t" \
  "v_perm_b32 v251, v251, v250, s29\n\t" \
  "v_add_f32 v69, v61, v249\n\t" \
  "v_mfma_f32_16x16x32_bf16 v[56:59], v[180:183], v[12:15], v[56:59]\n\t" \
  "buffer_load_dwordx4 v0, s[40:43], s34 offen offset:1024 sc1 lds\n\t" \
  "s_waitcnt lgkmcnt(5)\n\t" \
  "v_mfma_f32_16x16x32_bf16 v[52:55], v[184:187], v[16:19], v[52:55]\n\t" \
  "v_add_f32 v70, v62, v250\n\t" \
  "v_add_f32 v71, v63, v251\n\t" \
  "v_mfma_f32_16x16x32_bf16 v[56:59], v[188:191], v[16:19], v[56:59]\n\t" \
  "s_waitcnt lgkmcnt(4)\n\t" \
  "v_mfma_f32_16x16x32_bf16 v[52:55], v[192:195], v[4:7], v[52:55]\n\t" \
  "buffer_load_dwordx4 v0, s[40:43], s34 offen offset:2048 sc1 lds\n\t" \
  "v_mfma_f32_16x16x32_bf16 v[56:59], v[196:199], v[4:7], v[56:59]\n\t" \
  "buffer_load_dwordx4 v0, s[40:43], s34 offen offset:3072 sc1 lds\n\t" \
  "s_add_u32 m0, m0, 4096\n\t" \
  "s_add_u32 s34, s34, 4096\n\t" \
  "buffer_load_dwordx4 v0, s[40:43], s34 offen offset:0 sc1 lds\n\t" \
  "s_mov_b32 s85, s84\n\t" \
  "s_mov_b32 s84, s83\n\t" \
  "s_mov_b32 s83, s82\n\t" \
  "s_mov_b32 s82, s81\n\t" \
  "s_mov_b32 s81, s80\n\t" \
  "s_add_u32 s80, s81, 1\n\t" \
  "s_cmp_eq_u32 s80, s17\n\t" \
  "s_cselect_b32 s80, s20, s80\n\t" \
  "s_lshr_b64 s[86:87], s[86:87], 2\n\t" \
  "s_add_u32 s95, s95, 36864\n\t" \
  "s_cmp_eq_u32 s95, 147456\n\t" \
  "s_cselect_b32 s95, 36864, s95\n\t" \
  "s_add_u32 s18, s95, s24\n\t" \
  "v_add_u32 v76, 128, v76\n\t" \
  "v_add_u32 v2, s18, v0\n\t" \
  "v_xor_b32 v79, 4096, v79\n\t" \
  "s_waitcnt vmcnt(11)\n\t" \
  "v_cmp_gt_u32 vcc, s97, v78\n\t" \
  "s_waitcnt vmcnt(14) lgkmcnt(0)\n\t" \
  "s_barrier\n\t" \
  "s_cbranch_vccnz SL_t1_%=\n\t" \
  "BK_t1_%=:\n\t" \
  "s_mul_i32 s34, s81, 147456\n\t" \
  "ds_read_b128 v[12:15], v2 offset:0\n\t" \
  "ds_read_b128 v[16:19], v2 offset:1024\n\t" \
  "v_mfma_f32_16x16x32_bf16 v[52:55], v[200:203], v[8:11], v[52:55]\n\t" \
  "s_lshl_b32 s16, s80, 9\n\t" \
  "ds_read_b128 v[4:7], v2 offset:2048\n\t" \
  "s_add_u32 s16, s16, s92\n\t" \
  "v_mfma_f32_16x16x32_bf16 v[56:59], v[204:207], v[8:11], v[56:59]\n\t" \
  "s_add_u32 s34, s34, s90\n\t" \
  "ds_read_b128 v[8:11], v2 offset:3072\n\t" \
  "s_sub_u32 s33, s95, 36864\n\t" \
  "v_mfma_f32_16x16x32_bf16 v[52:55], v[208:211], v[224:227], v[52:55]\n\t" \
  "buffer_load_dword v78, off, s[60:63], s16 offset:16 sc1\n\t" \
  "v_mfma_f32_16x16x32_bf16 v[56:59], v[212:215], v[224:227], v[56:59]\n\t" \
  "s_bfe_u32 s19, s86, 0x20004\n\t" \
  "ds_read_b128 v[224:227], v2 offset:4096\n\t" \
  "s_mul_i32 s94, s83, 36864\n\t" \
  "v_mfma_f32_16x16x32_bf16 v[52:55], v[216:219], v[228:231], v[52:55]\n\t" \
  "s_mul_i32 s19, s19, 4718592\n\t" \
  "s_add_u32 s94, s94, s31\n\t" \
  "s_add_u32 s94, s94, s19\n\t" \
  "v_mfma_f32_16x16x32_bf16 v[56:59], v[220:223], v[228:231], v[56:59]\n\t" \
  "s_cmp_lt_u32 s33, 36864\n\t" \
  "s_cselect_b32 s33, 110592, s33\n\t" \
  "ds_read_b128 v[228:231], v2 offset:5120\n\t" \
  "s_waitcnt lgkmcnt(5)\n\t" \
  "v_mfma_f32_16x16x32_bf16 v[60:63], v[80:83], v[12:15], 0\n\t" \
  "buffer_load_dwordx4 v[248:251], v0, s[48:51], s94 offen sc1\n\t" \
  "v_mfma_f32_16x16x32_bf16 v[64:67], v[84:87], v[12:15], 0\n\t" \
  "s_add_u32 m0, s33, s30\n\t" \
  "ds_read_b128 v[12:15], v2 offset:6144\n\t" \
  "v_xor_b32 v232, 6144, v79\n\t" \
  "s_waitcnt lgkmcnt(5)\n\t" \
  "v_mfma_f32_16x16x32_bf16 v[60:63], v[88:91], v[16:19], v[60:63]\n\t" \
  "s_and_b32 s98, s86, 3\n\t" \
  "ds_read_b128 v[232:235], v232\n\t" \
  "s_mul_i32 s99, s85, 36864\n\t" \
  "v_mfma_f32_16x16x32_bf16 v[64:67], v[92:95], v[16:19], v[64:67]\n\t" \
  "s_mul_i32 s98, s98, 4718592\n\t" \
  "ds_read_b128 v[16:19], v2 offset:7168\n\t" \
  "s_add_u32 s99, s99, s93\n\t" \
  "s_waitcnt lgkmcnt(6)\n\t" \
  "v_mfma_f32_16x16x32_bf16 v[60:63], v[96:99], v[4:7], v[60:63]\n\t" \
  "buffer_load_dwordx4 v0, s[40:43], s34 offen offset:0 sc1 lds\n\t" \
  "v_mfma_f32_16x16x32_bf16 v[64:67], v[100:103], v[4:7], v[64:67]\n\t" \
  "s_add_u32 s99, s99, s98\n\t" \
  "ds_read_b128 v[4:7], v2 offset:8192\n\t" \
  "s_waitcnt lgkmcnt(2)\n\t" \
  "v_add_f32 v232, v232, v68\n\t" \
  "v_mfma_f32_16x16x32_bf16 v[60:63], v[104:107], v[8:11], v[60:63]\n\t" \
  "v_add_f32 v233, v233, v69\n\t" \
  "v_add_f32 v234, v234, v70\n\t" \
  "v_mfma_f32_16x16x32_bf16 v[64:67], v[108:111], v[8:11], v[64:67]\n\t" \
  "ds_read_b128 v[8:11], v2 offset:9216\n\t" \
  "v_add_f32 v235, v235, v71\n\t" \
  "v_mfma_f32_16x16x32_bf16 v[60:63], v[112:115], v[224:227], v[60:63]\n\t" \
  "buffer_load_dwordx4 v0, s[40:43], s34 offen offset:1024 sc1 lds\n\t" \
  "v_mfma_f32_16x16x32_bf16 v[64:67], v[116:119], v[224:227], v[64:67]\n\t" \
  "ds_read_b128 v[224:227], v2 offset:10240\n\t" \
  "v_perm_b32 v68, v232, v235, s25\n\t" \
  "v_mfma_f32_16x16x32_bf16 v[60:63], v[120:123], v[228:231], v[60:63]\n\t" \
  "v_perm_b32 v69, v233, v235, s26\n\t" \
  "v_perm_b32 v70, v234, v235, s27\n\t" \
  "v_mfma_f32_16x16x32_bf16 v[64:67], v[124:127], v[228:231], v[64:67]\n\t" \
  "ds_read_b128 v[228:231], v2 offset:11264\n\t" \
  "v_mov_b32 v71, s74\n\t" \
  "v_mfma_f32_16x16x32_bf16 v[60:63], v[128:131], v[12:15], v[60:63]\n\t" \
  "buffer_load_dwordx4 v0, s[40:43], s34 offen offset:2048 sc1 lds\n\t" \
  "v_mfma_f32_16x16x32_bf16 v[64:67], v[132:135], v[12:15], v[64:67]\n\t" \
  "ds_read_b128 v[12:15], v2 offset:12288\n\t" \
  "s_waitcnt lgkmcnt(5)\n\t" \
  "v_mfma_f32_16x16x32_bf16 v[60:63], v[136:139], v[16:19], v[60:63]\n\t" \
  "buffer_store_dwordx4 v[68:71], v0, s[52:55], s99 offen sc1\n\t" \
  "v_mfma_f32_16x16x32_bf16 v[64:67], v[140:143], v[16:19], v[64:67]\n\t" \
  "ds_read_b128 v[16:19], v2 offset:13312\n\t" \
  "s_waitcnt vmcnt(16)\n\t" \
  "v_cmp_ne_u32 vcc, s75, v75\n\t" \
  "s_waitcnt lgkmcnt(5)\n\t" \
  "v_mfma_f32_16x16x32_bf16 v[60:63], v[144:147], v[4:7], v[60:63]\n\t" \
  "buffer_load_dwordx4 v0, s[40:43], s34 offen offset:3072 sc1 lds\n\t" \
  "v_mfma_f32_16x16x32_bf16 v[64:67], v[148:151], v[4:7], v[64:67]\n\t" \
  "s_add_u32 m0, m0, 4096\n\t" \
  "ds_read_b128 v[4:7], v2 offset:14336\n\t" \
  "s_add_u32 s34, s34, 4096\n\t" \
  "s_waitcnt lgkmcnt(5)\n\t" \
  "v_mfma_f32_16x16x32_bf16 v[60:63], v[152:155], v[8:11], v[60:63]\n\t" \
  "s_cbranch_vccnz SL_q2_%=\n\t" \
  "BK_q2_%=:\n\t" \
  "ds_write_b128 v79, v[56:59]\n\t" \
  "v_mfma_f32_16x16x32_bf16 v[64:67], v[156:159], v[8:11], v[64:67]\n\t" \
  "s_nop 0\n\t" \
  "ds_read_b128 v[8:11], v2 offset:15360\n\t" \
  "v_perm_b32 v75, v72, v73, s28\n\t" \
  "s_waitcnt lgkmcnt(6)\n\t" \
  "v_mfma_f32_16x16x32_bf16 v[60:63], v[160:163], v[224:227], v[60:63]\n\t" \
  "buffer_load_dwordx4 v0, s[40:43], s34 offen offset:0 sc1 lds\n\t" \
  "v_mfma_f32_16x16x32_bf16 v[64:67], v[164:167], v[224:227], v[64:67]\n\t" \
  "ds_read_b128 v[224:227], v2 offset:16384\n\t" \
  "v_add_f32 v68, v52, v72\n\t" \
  "s_waitcnt lgkmcnt(6)\n\t" \
  "v_mfma_f32_16x16x32_bf16 v[60:63], v[168:171], v[228:231], v[60:63]\n\t" \
  "v_perm_b32 v75, v75, v74, s29\n\t" \
  "v_add_f32 v69, v53, v73\n\t" \
  "v_mfma_f32_16x16x32_bf16 v[64:67], v[172:175], v[228:231], v[64:67]\n\t" \
  "ds_read_b128 v[228:231], v2 offset:17408\n\t" \
  "v_add_f32 v70, v54, v74\n\t" \
  "s_waitcnt lgkmcnt(6)\n\t" \
  "v_mfma_f32_16x16x32_bf16 v[60:63], v[176:179], v[12:15], v[60:63]\n\t" \
  "buffer_load_dwordx4 v0, s[40:43], s34 offen offset:1024 sc1 lds\n\t" \
  "v_mfma_f32_16x16x32_bf16 v[64:67], v[180:183], v[12:15], v[64:67]\n\t" \
  "v_add_f32 v71, v55, v75\n\t" \
  "s_waitcnt lgkmcnt(5)\n\t" \
  "v_mfma_f32_16x16x32_bf16 v[60:63], v[184:187], v[16:19], v[60:63]\n\t" \
  "v_mfma_f32_16x16x32_bf16 v[64:67], v[188:191], v[16:19], v[64:67]\n\t" \
  "buffer_load_dwordx4 v0, s[40:43], s34 offen offset:2048 sc1 lds\n\t" \
  "s_waitcnt lgkmcnt(4)\n\t" \
  "v_mfma_f32_16x16x32_bf16 v[60:63], v[192:195], v[4:7], v[60:63]\n\t" \
  "v_mfma_f32_16x16x32_bf16 v[64:67], v[196:199], v[4:7], v[64:67]\n\t" \
  "buffer_load_dwordx4 v0, s[40:43], s34 offen offset:3072 sc1 lds\n\t" \
  "s_add_u32 m0, m0, 4096\n\t" \
  "s_add_u32 s34, s34, 4096\n\t" \
  "buffer_load_dwordx4 v0, s[40:43], s34 offen offset:0 sc1 lds\n\t" \
  "s_mov_b32 s85, s84\n\t" \
  "s_mov_b32 s84, s83\n\t" \
  "s_mov_b32 s83, s82\n\t" \
  "s_mov_b32 s82, s81\n\t" \
  "s_mov_b32 s81, s80\n\t" \
  "s_add_u32 s80, s81, 1\n\t" \
  "s_cmp_eq_u32 s80, s17\n\t" \
  "s_cselect_b32 s80, s20, s80\n\t" \
  "s_lshr_b64 s[86:87], s[86:87], 2\n\t" \
  "s_add_u32 s95, s95, 36864\n\t" \
  "s_cmp_eq_u32 s95, 147456\n\t" \
  "s_cselect_b32 s95, 36864, s95\n\t" \
  "s_add_u32 s18, s95, s24\n\t" \
  "v_add_u32 v76, 128, v76\n\t" \
  "v_add_u32 v2, s18, v0\n\t" \
  "v_xor_b32 v79, 4096, v79\n\t" \
  "s_waitcnt vmcnt(11)\n\t" \
  "v_cmp_gt_u32 vcc, s97, v78\n\t" \
  "s_waitcnt vmcnt(12) lgkmcnt(0)\n\t" \
  "s_barrier\n\t" \
  "s_cbranch_vccnz SL_t2_%=\n\t" \
  "BK_t2_%=:\n\t" \
  "s_mul_i32 s34, s81, 147456\n\t" \
  "ds_read_b128 v[12:15], v2 offset:0\n\t" \
  "ds_read_b128 v[16:19], v2 offset:1024\n\t" \
  "v_mfma_f32_16x16x32_bf16 v[60:63], v[200:203], v[8:11], v[60:63]\n\t" \
  "s_lshl_b32 s16, s80, 9\n\t" \
  "ds_read_b128 v[4:7], v2 offset:2048\n\t" \
  "s_add_u32 s16, s16, s92\n\t" \
  "v_mfma_f32_16x16x32_bf16 v[64:67], v[204:207], v[8:11], v[64:67]\n\t" \
  "s_add_u32 s34, s34, s90\n\t" \
  "ds_read_b128 v[8:11], v2 offset:3072\n\t" \
  "s_sub_u32 s33, s95, 36864\n\t" \
  "v_mfma_f32_16x16x32_bf16 v[60:63], v[208:211], v[224:227], v[60:63]\n\t" \
  "buffer_load_dword v78, off, s[60:63], s16 offset:16 sc1\n\t" \
  "v_mfma_f32_16x16x32_bf16 v[64:67], v[212:215], v[224:227], v[64:67]\n\t" \
  "s_bfe_u32 s19, s86, 0x20004\n\t" \
  "ds_read_b128 v[224:227], v2 offset:4096\n\t" \
  "s_mul_i32 s96, s83, 36864\n\t" \
  "v_mfma_f32_16x16x32_bf16 v[60:63], v[216:219], v[228:231], v[60:63]\n\t" \
  "s_mul_i32 s19, s19, 4718592\n\t" \
  "s_add_u32 s96, s96, s31\n\t" \
  "s_add_u32 s96, s96, s19\n\t" \
  "v_mfma_f32_16x16x32_bf16 v[64:67], v[220:223], v[228:231], v[64:67]\n\t" \
  "s_cmp_lt_u32 s33, 36864\n\t" \
  "s_cselect_b32 s33, 110592, s33\n\t" \
  "ds_read_b128 v[228:231], v2 offset:5120\n\t" \
  "s_waitcnt lgkmcnt(5)\n\t" \
  "v_mfma_f32_16x16x32_bf16 v[52:55], v[80:83], v[12:15], 0\n\t" \
  "buffer_load_dwordx4 v[72:75], v0, s[48:51], s96 offen sc1\n\t" \
  "v_mfma_f32_16x16x32_bf16 v[56:59], v[84:87], v[12:15], 0\n\t" \
  "s_add_u32 m0, s33, s30\n\t" \
  "ds_read_b128 v[12:15], v2 offset:6144\n\t" \
  "v_xor_b32 v232, 6144, v79\n\t" \
  "s_waitcnt lgkmcnt(5)\n\t" \
  "v_mfma_f32_16x16x32_bf16 v[52:55], v[88:91], v[16:19], v[52:55]\n\t" \
  "s_and_b32 s98, s86, 3\n\t" \
  "ds_read_b128 v[232:235], v232\n\t" \
  "s_mul_i32 s99, s85, 36864\n\t" \
  "v_mfma_f32_16x16x32_bf16 v[56:59], v[92:95], v[16:19], v[56:59]\n\t" \
  "s_mul_i32 s98, s98, 4718592\n\t" \
  "ds_read_b128 v[16:19], v2 offset:7168\n\t" \
  "s_add_u32 s99, s99, s93\n\t" \
  "s_waitcnt lgkmcnt(6)\n\t" \
  "v_mfma_f32_16x16x32_bf16 v[52:55], v[96:99], v[4:7], v[52:55]\n\t" \
  "buffer_load_dwordx4 v0, s[40:43], s34 offen offset:0 sc1 lds\n\t" \
  "v_mfma_f32_16x16x32_bf16 v[56:59], v[100:103], v[4:7], v[56:59]\n\t" \
  "s_add_u32 s99, s99, s98\n\t" \
  "ds_read_b128 v[4:7], v2 offset:8192\n\t" \
  "s_waitcnt lgkmcnt(2)\n\t" \
  "v_add_f32 v232, v232, v68\n\t" \
  "v_mfma_f32_16x16x32_bf16 v[52:55], v[104:107], v[8:11], v[52:55]\n\t" \
  "v_add_f32 v233, v233, v69\n\t" \
  "v_add_f32 v234, v234, v70\n\t" \
  "v_mfma_f32_16x16x32_bf16 v[56:59], v[108:111], v[8:11], v[56:59]\n\t" \
  "ds_read_b128 v[8:11], v2 offset:9216\n\t" \
  "v_add_f32 v235, v235, v71\n\t" \
  "v_mfma_f32_16x16x32_bf16 v[52:55], v[112:115], v[224:227], v[52:55]\n\t" \
  "buffer_load_dwordx4 v0, s[40:43], s34 offen offset:1024 sc1 lds\n\t" \
  "v_mfma_f32_16x16x32_bf16 v[56:59], v[116:119], v[224:227], v[56:59]\n\t" \
  "ds_read_b128 v[224:227], v2 offset:10240\n\t" \
  "v_perm_b32 v68, v232, v235, s25\n\t" \
  "v_mfma_f32_16x16x32_bf16 v[52:55], v[120:123], v[228:231], v[52:55]\n\t" \
  "v_perm_b32 v69, v233, v235, s26\n\t" \
  "v_perm_b32 v70, v234, v235, s27\n\t" \
  "v_mfma_f32_16x16x32_bf16 v[56:59], v[124:127], v[228:231], v[56:59]\n\t" \
  "ds_read_b128 v[228:231], v2 offset:11264\n\t" \
  "v_mov_b32 v71, s74\n\t" \
  "v_mfma_f32_16x16x32_bf16 v[52:55], v[128:131], v[12:15], v[52:55]\n\t" \
  "buffer_load_dwordx4 v0, s[40:43], s34 offen offset:2048 sc1 lds\n\t" \
  "v_mfma_f32_16x16x32_bf16 v[56:59], v[132:135], v[12:15], v[56:59]\n\t" \
  "ds_read_b128 v[12:15], v2 offset:12288\n\t" \
  "s_waitcnt lgkmcnt(5)\n\t" \
  "v_mfma_f32_16x16x32_bf16 v[52:55], v[136:139], v[16:19], v[52:55]\n\t" \
  "buffer_store_dwordx4 v[68:71], v0, s[52:55], s99 offen sc1\n\t" \
  "v_mfma_f32_16x16x32_bf16 v[56:59], v[140:143], v[16:19], v[56:59]\n\t" \
  "ds_read_b128 v[16:19], v2 offset:13312\n\t" \
  "s_waitcnt vmcnt(16)\n\t" \
  "v_cmp_ne_u32 vcc, s75, v251\n\t" \
  "s_waitcnt lgkmcnt(5)\n\t" \
  "v_mfma_f32_16x16x32_bf16 v[52:55], v[144:147], v[4:7], v[52:55]\n\t" \
  "buffer_load_dwordx4 v0, s[40:43], s34 offen offset:3072 sc1 lds\n\t" \
  "v_mfma_f32_16x16x32_bf16 v[56:59], v[148:151], v[4:7], v[56:59]\n\t" \
  "s_add_u32 m0, m0, 4096\n\t" \
  "ds_read_b128 v[4:7], v2 offset:14336\n\t" \
  "s_add_u32 s34, s34, 4096\n\t" \
  "s_waitcnt lgkmcnt(5)\n\t" \
  "v_mfma_f32_16x16x32_bf16 v[52:55], v[152:155], v[8:11], v[52:55]\n\t" \
  "s_cbranch_vccnz SL_q3_%=\n\t" \
  "BK_q3_%=:\n\t" \
  "ds_write_b128 v79, v[64:67]\n\t" \
  "v_mfma_f32_16x16x32_bf16 v[56:59], v[156:159], v[8:11], v[56:59]\n\t" \
  "s_nop 0\n\t" \
  "ds_read_b128 v[8:11], v2 offset:15360\n\t" \
  "v_perm_b32 v251, v248, v249, s28\n\t" \
  "s_waitcnt lgkmcnt(6)\n\t" \
  "v_mfma_f32_16x16x32_bf16 v[52:55], v[160:163], v[224:227], v[52:55]\n\t" \
  "buffer_load_dwordx4 v0, s[40:43], s34 offen offset:0 sc1 lds\n\t" \
  "v_mfma_f32_16x16x32_bf16 v[56:59], v[164:167], v[224:227], v[56:59]\n\t" \
  "ds_read_b128 v[224:227], v2 offset:16384\n\t" \
  "v_add_f32 v68, v60, v248\n\t" \
  "s_waitcnt lgkmcnt(6)\n\t" \
  "v_mfma_f32_16x16x32_bf16 v[52:55], v[168:171], v[228:231], v[52:55]\n\t" \
  "v_perm_b32 v251, v251, v250, s29\n\t" \
  "v_add_f32 v69, v61, v249\n\t" \
  "v_mfma_f32_16x16x32_bf16 v[56:59], v[172:175], v[228:231], v[56:59]\n\t" \
  "ds_read_b128 v[228:231], v2 offset:17408\n\t" \
  "v_add_f32 v70, v62, v250\n\t" \
  "s_waitcnt lgkmcnt(6)\n\t" \
  "v_mfma_f32_16x16x32_bf16 v[52:55], v[176:179], v[12:15], v[52:55]\n\t" \
  "buffer_load_dwordx4 v0, s[40:43], s34 offen offset:1024 sc1 lds\n\t" \
  "v_mfma_f32_16x16x32_bf16 v[56:59], v[180:183], v[12:15], v[56:59]\n\t" \
  "v_add_f32 v71, v63, v251\n\t" \
  "s_waitcnt lgkmcnt(5)\n\t" \
  "v_mfma_f32_16x16x32_bf16 v[52:55], v[184:187], v[16:19], v[52:55]\n\t" \
  "v_mfma_f32_16x16x32_bf16 v[56:59], v[188:191], v[16:19], v[56:59]\n\t" \
  "buffer_load_dwordx4 v0, s[40:43], s34 offen offset:2048 sc1 lds\n\t" \
  "s_waitcnt lgkmcnt(4)\n\t" \
  "v_mfma_f32_16x16x32_bf16 v[52:55], v[192:195], v[4:7], v[52:55]\n\t" \
  "v_mfma_f32_16x16x32_bf16 v[56:59], v[196:199], v[4:7], v[56:59]\n\t" \
  "buffer_load_dwordx4 v0, s[40:43], s34 offen offset:3072 sc1 lds\n\t" \
  "s_add_u32 m0, m0, 4096\n\t" \
  "s_add_u32 s34, s34, 4096\n\t" \
  "buffer_load_dwordx4 v0, s[40:43], s34 offen offset:0 sc1 lds\n\t" \
  "s_mov_b32 s85, s84\n\t" \
  "s_mov_b32 s84, s83\n\t" \
  "s_mov_b32 s83, s82\n\t" \
  "s_mov_b32 s82, s81\n\t" \
  "s_mov_b32 s81, s80\n\t" \
  "s_add_u32 s80, s81, 1\n\t" \
  "s_cmp_eq_u32 s80, s17\n\t" \
  "s_cselect_b32 s80, s20, s80\n\t" \
  "s_lshr_b64 s[86:87], s[86:87], 2\n\t" \
  "s_add_u32 s95, s95, 36864\n\t" \
  "s_cmp_eq_u32 s95, 147456\n\t" \
  "s_cselect_b32 s95, 36864, s95\n\t" \
  "s_add_u32 s18, s95, s24\n\t" \
  "v_add_u32 v76, 128, v76\n\t" \
  "v_add_u32 v2, s18, v0\n\t" \
  "v_xor_b32 v79, 4096, v79\n\t" \
  "s_waitcnt vmcnt(11)\n\t" \
  "v_cmp_gt_u32 vcc, s97, v78\n\t" \
  "s_waitcnt vmcnt(12) lgkmcnt(0)\n\t" \
  "s_barrier\n\t" \
  "s_cbranch_vccnz SL_t3_%=\n\t" \
  "BK_t3_%=:\n\t" \
  "s_mul_i32 s34, s81, 147456\n\t" \
  "ds_read_b128 v[12:15], v2 offset:0\n\t" \
  "ds_read_b128 v[16:19], v2 offset:1024\n\t" \
  "v_mfma_f32_16x16x32_bf16 v[52:55], v[200:203], v[8:11], v[52:55]\n\t" \
  "s_lshl_b32 s16, s80, 9\n\t" \
  "ds_read_b128 v[4:7], v2 offset:2048\n\t" \
  "s_add_u32 s16, s16, s92\n\t" \
  "v_mfma_f32_16x16x32_bf16 v[56:59], v[204:207], v[8:11], v[56:59]\n\t" \
  "s_add_u32 s34, s34, s90\n\t" \
  "ds_read_b128 v[8:11], v2 offset:3072\n\t" \
  "s_sub_u32 s33, s95, 36864\n\t" \
  "v_mfma_f32_16x16x32_bf16 v[52:55], v[208:211], v[224:227], v[52:55]\n\t" \
  "buffer_load_dword v78, off, s[60:63], s16 offset:16 sc1\n\t" \
  "v_mfma_f32_16x16x32_bf16 v[56:59], v[212:215], v[224:227], v[56:59]\n\t" \
  "s_bfe_u32 s19, s86, 0x20004\n\t" \
  "ds_read_b128 v[224:227], v2 offset:4096\n\t" \
  "s_mul_i32 s94, s83, 36864\n\t" \
  "v_mfma_f32_16x16x32_bf16 v[52:55], v[216:219], v[228:231], v[52:55]\n\t" \
  "s_mul_i32 s19, s19, 4718592\n\t" \
  "s_add_u32 s94, s94, s31\n\t" \
  "s_add_u32 s94, s94, s19\n\t" \
  "v_mfma_f32_16x16x32_bf16 v[56:59], v[220:223], v[228:231], v[56:59]\n\t" \
  "s_cmp_lt_u32 s33, 36864\n\t" \
  "s_cselect_b32 s33, 110592, s33\n\t" \
  "ds_read_b128 v[228:231], v2 offset:5120\n\t" \
  "s_waitcnt lgkmcnt(5)\n\t" \
  "v_mfma_f32_16x16x32_bf16 v[60:63], v[80:83], v[12:15], 0\n\t" \
  "buffer_load_dwordx4 v[248:251], v0, s[48:51], s94 offen sc1\n\t" \
  "v_mfma_f32_16x16x32_bf16 v[64:67], v[84:87], v[12:15], 0\n\t" \
  "s_add_u32 m0, s33, s30\n\t" \
  "ds_read_b128 v[12:15], v2 offset:6144\n\t" \
  "v_xor_b32 v232, 6144, v79\n\t" \
  "s_waitcnt lgkmcnt(5)\n\t" \
  "v_mfma_f32_16x16x32_bf16 v[60:63], v[88:91], v[16:19], v[60:63]\n\t" \
  "s_and_b32 s98, s86, 3\n\t" \
  "ds_read_b128 v[232:235], v232\n\t" \
  "s_mul_i32 s99, s85, 36864\n\t" \
  "v_mfma_f32_16x16x32_bf16 v[64:67], v[92:95], v[16:19], v[64:67]\n\t" \
  "s_mul_i32 s98, s98, 4718592\n\t" \
  "ds_read_b128 v[16:19], v2 offset:7168\n\t" \
  "s_add_u32 s99, s99, s93\n\t" \
  "s_waitcnt lgkmcnt(6)\n\t" \
  "v_mfma_f32_16x16x32_bf16 v[60:63], v[96:99], v[4:7], v[60:63]\n\t" \
  "buffer_load_dwordx4 v0, s[40:43], s34 offen offset:0 sc1 lds\n\t" \
  "v_mfma_f32_16x16x32_bf16 v[64:67], v[100:103], v[4:7], v[64:67]\n\t" \
  "s_add_u32 s99, s99, s98\n\t" \
  "ds_read_b128 v[4:7], v2 offset:8192\n\t" \
  "s_waitcnt lgkmcnt(2)\n\t" \
  "v_add_f32 v232, v232, v68\n\t" \
  "v_mfma_f32_16x16x32_bf16 v[60:63], v[104:107], v[8:11], v[60:63]\n\t" \
  "v_add_f32 v233, v233, v69\n\t" \
  "v_add_f32 v234, v234, v70\n\t" \
  "v_mfma_f32_16x16x32_bf16 v[64:67], v[108:111], v[8:11], v[64:67]\n\t" \
  "ds_read_b128 v[8:11], v2 offset:9216\n\t" \
  "v_add_f32 v235, v235, v71\n\t" \
  "v_mfma_f32_16x16x32_bf16 v[60:63], v[112:115], v[224:227], v[60:63]\n\t" \
  "buffer_load_dwordx4 v0, s[40:43], s34 offen offset:1024 sc1 lds\n\t" \
  "v_mfma_f32_16x16x32_bf16 v[64:67], v[116:119], v[224:227], v[64:67]\n\t" \
  "ds_read_b128 v[224:227], v2 offset:10240\n\t" \
  "v_perm_b32 v68, v232, v235, s25\n\t" \
  "v_mfma_f32_16x16x32_bf16 v[60:63], v[120:123], v[228:231], v[60:63]\n\t" \
  "v_perm_b32 v69, v233, v235, s26\n\t" \
  "v_perm_b32 v70, v234, v235, s27\n\t" \
  "v_mfma_f32_16x16x32_bf16 v[64:67], v[124:127], v[228:231], v[64:67]\n\t" \
  "ds_read_b128 v[228:231], v2 offset:11264\n\t" \
  "v_mov_b32 v71, s74\n\t" \
  "v_mfma_f32_16x16x32_bf16 v[60:63], v[128:131], v[12:15], v[60:63]\n\t" \
  "buffer_load_dwordx4 v0, s[40:43], s34 offen offset:2048 sc1 lds\n\t" \
  "v_mfma_f32_16x16x32_bf16 v[64:67], v[132:135], v[12:15], v[64:67]\n\t" \
  "ds_read_b128 v[12:15], v2 offset:12288\n\t" \
  "s_waitcnt lgkmcnt(5)\n\t" \
  "v_mfma_f32_16x16x32_bf16 v[60:63], v[136:139], v[16:19], v[60:63]\n\t" \
  "buffer_store_dwordx4 v[68:71], v0, s[52:55], s99 offen sc1\n\t" \
  "v_mfma_f32_16x16x32_bf16 v[64:67], v[140:143], v[16:19], v[64:67]\n\t" \
  "ds_read_b128 v[16:19], v2 offset:13312\n\t" \
  "s_waitcnt vmcnt(16)\n\t" \
  "v_cmp_ne_u32 vcc, s75, v75\n\t" \
  "s_waitcnt lgkmcnt(5)\n\t" \
  "v_mfma_f32_16x16x32_bf16 v[60:63], v[144:147], v[4:7], v[60:63]\n\t" \
  "buffer_load_dwordx4 v0, s[40:43], s34 offen offset:3072 sc1 lds\n\t" \
  "v_mfma_f32_16x16x32_bf16 v[64:67], v[148:151], v[4:7], v[64:67]\n\t" \
  "s_add_u32 m0, m0, 4096\n\t" \
  "ds_read_b128 v[4:7], v2 offset:14336\n\t" \
  "s_add_u32 s34, s34, 4096\n\t" \
  "s_waitcnt lgkmcnt(5)\n\t" \
  "v_mfma_f32_16x16x32_bf16 v[60:63], v[152:155], v[8:11], v[60:63]\n\t" \
  "s_cbranch_vccnz SL_q4_%=\n\t" \
  "BK_q4_%=:\n\t" \
  "ds_write_b128 v79, v[56:59]\n\t" \
  "v_mfma_f32_16x16x32_bf16 v[64:67], v[156:159], v[8:11], v[64:67]\n\t" \
  "s_nop 0\n\t" \
  "ds_read_b128 v[8:11], v2 offset:15360\n\t" \
  "v_perm_b32 v75, v72, v73, s28\n\t" \
  "s_waitcnt lgkmcnt(6)\n\t" \
  "v_mfma_f32_16x16x32_bf16 v[60:63], v[160:163], v[224:227], v[60:63]\n\t" \
  "buffer_load_dwordx4 v0, s[40:43], s34 offen offset:0 sc1 lds\n\t" \
  "v_mfma_f32_16x16x32_bf16 v[64:67], v[164:167], v[224:227], v[64:67]\n\t" \
  "ds_read_b128 v[224:227], v2 offset:16384\n\t" \
  "v_add_f32 v68, v52, v72\n\t" \
  "s_waitcnt lgkmcnt(6)\n\t" \
  "v_mfma_f32_16x16x32_bf16 v[60:63], v[168:171], v[228:231], v[60:63]\n\t" \
  "v_perm_b32 v75, v75, v74, s29\n\t" \
  "v_add_f32 v69, v53, v73\n\t" \
  "v_mfma_f32_16x16x32_bf16 v[64:67], v[172:175], v[228:231], v[64:67]\n\t" \
  "ds_read_b128 v[228:231], v2 offset:17408\n\t" \
  "v_add_f32 v70, v54, v74\n\t" \
  "s_waitcnt lgkmcnt(6)\n\t" \
  "v_mfma_f32_16x16x32_bf16 v[60:63], v[176:179], v[12:15], v[60:63]\n\t" \
  "buffer_load_dwordx4 v0, s[40:43], s34 offen offset:1024 sc1 lds\n\t" \
  "v_mfma_f32_16x16x32_bf16 v[64:67], v[180:183], v[12:15], v[64:67]\n\t" \
  "v_add_f32 v71, v55, v75\n\t" \
  "s_waitcnt lgkmcnt(5)\n\t" \
  "v_mfma_f32_16x16x32_bf16 v[60:63], v[184:187], v[16:19], v[60:63]\n\t" \
  "v_mfma_f32_16x16x32_bf16 v[64:67], v[188:191], v[16:19], v[64:67]\n\t" \
  "buffer_load_dwordx4 v0, s[40:43], s34 offen offset:2048 sc1 lds\n\t" \
  "s_waitcnt lgkmcnt(4)\n\t" \
  "v_mfma_f32_16x16x32_bf16 v[60:63], v[192:195], v[4:7], v[60:63]\n\t" \
  "v_mfma_f32_16x16x32_bf16 v[64:67], v[196:199], v[4:7], v[64:67]\n\t" \
  "buffer_load_dwordx4 v0, s[40:43], s34 offen offset:3072 sc1 lds\n\t" \
  "s_add_u32 m0, m0, 4096\n\t" \
  "s_add_u32 s34, s34, 4096\n\t" \
  "buffer_load_dwordx4 v0, s[40:43], s34 offen offset:0 sc1 lds\n\t" \
  "s_mov_b32 s85, s84\n\t" \
  "s_mov_b32 s84, s83\n\t" \
  "s_mov_b32 s83, s82\n\t" \
  "s_mov_b32 s82, s81\n\t" \
  "s_mov_b32 s81, s80\n\t" \
  "s_add_u32 s80, s81, 1\n\t" \
  "s_cmp_eq_u32 s80, s17\n\t" \
  "s_cselect_b32 s80, s20, s80\n\t" \
  "s_lshr_b64 s[86:87], s[86:87], 2\n\t" \
  "s_add_u32 s95, s95, 36864\n\t" \
  "s_cmp_eq_u32 s95, 147456\n\t" \
  "s_cselect_b32 s95, 36864, s95\n\t" \
  "s_add_u32 s18, s95, s24\n\t" \
  "v_add_u32 v76, 128, v76\n\t" \
  "v_add_u32 v2, s18, v0\n\t" \
  "v_xor_b32 v79, 4096, v79\n\t" \
  "s_waitcnt vmcnt(11)\n\t" \
  "v_cmp_gt_u32 vcc, s97, v78\n\t" \
  "s_waitcnt vmcnt(12) lgkmcnt(0)\n\t" \
  "s_barrier\n\t" \
  "s_cbranch_vccnz SL_t4_%=\n\t" \
  "BK_t4_%=:\n\t" \
  "s_mul_i32 s34, s81, 147456\n\t" \
  "ds_read_b128 v[12:15], v2 offset:0\n\t" \
  "ds_read_b128 v[16:19], v2 offset:1024\n\t" \
  "v_mfma_f32_16x16x32_bf16 v[60:63], v[200:203], v[8:11], v[60:63]\n\t" \
  "s_lshl_b32 s16, s80, 9\n\t" \
  "ds_read_b128 v[4:7], v2 offset:2048\n\t" \
  "s_add_u32 s16, s16, s92\n\t" \
  "v_mfma_f32_16x16x32_bf16 v[64:67], v[204:207], v[8:11], v[64:67]\n\t" \
  "s_add_u32 s34, s34, s90\n\t" \
  "ds_read_b128 v[8:11], v2 offset:3072\n\t" \
  "s_sub_u32 s33, s95, 36864\n\t" \
  "v_mfma_f32_16x16x32_bf16 v[60:63], v[208:211], v[224:227], v[60:63]\n\t" \
  "buffer_load_dword v78, off, s[60:63], s16 offset:16 sc1\n\t" \
  "v_mfma_f32_16x16x32_bf16 v[64:67], v[212:215], v[224:227], v[64:67]\n\t" \
  "s_bfe_u32 s19, s86, 0x20004\n\t" \
  "ds_read_b128 v[224:227], v2 offset:4096\n\t" \
  "s_mul_i32 s96, s83, 36864\n\t" \
  "v_mfma_f32_16x16x32_bf16 v[60:63], v[216:219], v[228:231], v[60:63]\n\t" \
  "s_mul_i32 s19, s19, 4718592\n\t" \
  "s_add_u32 s96, s96, s31\n\t" \
  "s_add_u32 s96, s96, s19\n\t" \
  "v_mfma_f32_16x16x32_bf16 v[64:67], v[220:223], v[228:231], v[64:67]\n\t" \
  "s_cmp_lt_u32 s33, 36864\n\t" \
  "s_cselect_b32 s33, 110592, s33\n\t" \
  "ds_read_b128 v[228:231], v2 offset:5120\n\t" \
  "s_waitcnt lgkmcnt(5)\n\t" \
  "v_mfma_f32_16x16x32_bf16 v[52:55], v[80:83], v[12:15], 0\n\t" \
  "buffer_load_dwordx4 v[72:75], v0, s[48:51], s96 offen sc1\n\t" \
  "v_mfma_f32_16x16x32_bf16 v[56:59], v[84:87], v[12:15], 0\n\t" \
  "s_mul_i32 s19, s84, 18432\n\t" \
  "ds_read_b128 v[12:15], v2 offset:6144\n\t" \
  "s_add_u32 s19, s19, s23\n\t" \
  "s_waitcnt lgkmcnt(5)\n\t" \
  "v_mfma_f32_16x16x32_bf16 v[52:55], v[88:91], v[16:19], v[52:55]\n\t" \
  "buffer_load_dwordx2 v[236:237], v1, s[36:39], s19 offen\n\t" \
  "v_mfma_f32_16x16x32_bf16 v[56:59], v[92:95], v[16:19], v[56:59]\n\t" \
  "s_add_u32 m0, s33, s30\n\t" \
  "ds_read_b128 v[16:19], v2 offset:7168\n\t" \
  "v_xor_b32 v232, 6144, v79\n\t" \
  "s_waitcnt lgkmcnt(5)\n\t" \
  "v_mfma_f32_16x16x32_bf16 v[52:55], v[96:99], v[4:7], v[52:55]\n\t" \
  "s_and_b32 s98, s86, 3\n\t" \
  "ds_read_b128 v[232:235], v232\n\t" \
  "s_mul_i32 s99, s85, 36864\n\t" \
  "v_mfma_f32_16x16x32_bf16 v[56:59], v[100:103], v[4:7], v[56:59]\n\t" \
  "s_mul_i32 s98, s98, 4718592\n\t" \
  "ds_read_b128 v[4:7], v2 offset:8192\n\t" \
  "s_add_u32 s99, s99, s93\n\t" \
  "s_waitcnt lgkmcnt(6)\n\t" \
  "v_mfma_f32_16x16x32_bf16 v[52:55], v[104:107], v[8:11], v[52:55]\n\t" \
  "buffer_load_dwordx4 v0, s[40:43], s34 offen offset:0 sc1 lds\n\t" \
  "v_mfma_f32_16x16x32_bf16 v[56:59], v[108:111], v[8:11], v[56:59]\n\t" \
  "s_add_u32 s99, s99, s98\n\t" \
  "ds_read_b128 v[8:11], v2 offset:9216\n\t" \
  "s_waitcnt lgkmcnt(2)\n\t" \
  "v_add_f32 v232, v232, v68\n\t" \
  "v_mfma_f32_16x16x32_bf16 v[52:55], v[112:115], v[224:227], v[52:55]\n\t" \
  "v_add_f32 v233, v233, v69\n\t" \
  "v_add_f32 v234, v234, v70\n\t" \
  "v_mfma_f32_16x16x32_bf16 v[56:59], v[116:119], v[224:227], v[56:59]\n\t" \
  "ds_read_b128 v[224:227], v2 offset:10240\n\t" \
  "v_add_f32 v235, v235, v71\n\t" \
  "v_mfma_f32_16x16x32_bf16 v[52:55], v[120:123], v[228:231], v[52:55]\n\t" \
  "buffer_load_dwordx4 v0, s[40:43], s34 offen offset:1024 sc1 lds\n\t" \
  "v_mfma_f32_16x16x32_bf16 v[56:59], v[124:127], v[228:231], v[56:59]\n\t" \
  "ds_read_b128 v[228:231], v2 offset:11264\n\t" \
  "v_perm_b32 v68, v232, v235, s25\n\t" \
  "v_mfma_f32_16x16x32_bf16 v[52:55], v[128:131], v[12:15], v[52:55]\n\t" \
  "v_perm_b32 v69, v233, v235, s26\n\t" \
  "v_perm_b32 v70, v234, v235, s27\n\t" \
  "v_mfma_f32_16x16x32_bf16 v[56:59], v[132:135], v[12:15], v[56:59]\n\t" \
  "ds_read_b128 v[12:15], v2 offset:12288\n\t" \
  "v_mov_b32 v71, s74\n\t" \
  "v_mfma_f32_16x16x32_bf16 v[52:55], v[136:139], v[16:19], v[52:55]\n\t" \
  "buffer_load_dwordx4 v0, s[40:43], s34 offen offset:2048 sc1 lds\n\t" \
  "v_mfma_f32_16x16x32_bf16 v[56:59], v[140:143], v[16:19], v[56:59]\n\t" \
  "ds_read_b128 v[16:19], v2 offset:13312\n\t" \
  "s_waitcnt lgkmcnt(5)\n\t" \
  "v_mfma_f32_16x16x32_bf16 v[52:55], v[144:147], v[4:7], v[52:55]\n\t" \
  "buffer_store_dwordx4 v[68:71], v0, s[52:55], s99 offen sc1\n\t" \
  "v_mfma_f32_16x16x32_bf16 v[56:59], v[148:151], v[4:7], v[56:59]\n\t" \
  "ds_read_b128 v[4:7], v2 offset:14336\n\t" \
  "s_waitcnt vmcnt(17)\n\t" \
  "v_cmp_ne_u32 vcc, s75, v251\n\t" \
  "s_waitcnt lgkmcnt(5)\n\t" \
  "v_mfma_f32_16x16x32_bf16 v[52:55], v[152:155], v[8:11], v[52:55]\n\t" \
  "buffer_load_dwordx4 v0, s[40:43], s34 offen offset:3072 sc1 lds\n\t" \
  "v_mfma_f32_16x16x32_bf16 v[56:59], v[156:159], v[8:11], v[56:59]\n\t" \
  "s_add_u32 m0, m0, 4096\n\t" \
  "ds_read_b128 v[8:11], v2 offset:15360\n\t" \
  "s_add_u32 s34, s34, 4096\n\t" \
  "s_waitcnt lgkmcnt(5)\n\t" \
  "v_mfma_f32_16x16x32_bf16 v[52:55], v[160:163], v[224:227], v[52:55]\n\t" \
  "s_cbranch_vccnz SL_q5_%=\n\t" \
  "BK_q5_%=:\n\t" \
  "ds_write_b128 v79, v[64:67]\n\t" \
  "v_mfma_f32_16x16x32_bf16 v[56:59], v[164:167], v[224:227], v[56:59]\n\t" \
  "s_nop 0\n\t" \
  "ds_read_b128 v[224:227], v2 offset:16384\n\t" \
  "v_perm_b32 v251, v248, v249, s28\n\t" \
  "s_waitcnt lgkmcnt(6)\n\t" \
  "v_mfma_f32_16x16x32_bf16 v[52:55], v[168:171], v[228:231], v[52:55]\n\t" \
  "buffer_load_dwordx4 v0, s[40:43], s34 offen offset:0 sc1 lds\n\t" \
  "v_mfma_f32_16x16x32_bf16 v[56:59], v[172:175], v[228:231], v[56:59]\n\t" \
  "ds_read_b128 v[228:231], v2 offset:17408\n\t" \
  "v_add_f32 v68, v60, v248\n\t" \
  "s_waitcnt lgkmcnt(6)\n\t" \
  "v_mfma_f32_16x16x32_bf16 v[52:55], v[176:179], v[12:15], v[52:55]\n\t" \
  "v_perm_b32 v251, v251, v250, s29\n\t" \
  "v_add_f32 v69, v61, v249\n\t" \
  "v_mfma_f32_16x16x32_bf16 v[56:59], v[180:183], v[12:15], v[56:59]\n\t" \
  "buffer_load_dwordx4 v0, s[40:43], s34 offen offset:1024 sc1 lds\n\t" \
  "s_waitcnt lgkmcnt(5)\n\t" \
  "v_mfma_f32_16x16x32_bf16 v[52:55], v[184:187], v[16:19], v[52:55]\n\t" \
  "v_add_f32 v70, v62, v250\n\t" \
  "v_add_f32 v71, v63, v251\n\t" \
  "v_mfma_f32_16x16x32_bf16 v[56:59], v[188:191], v[16:19], v[56:59]\n\t" \
  "s_waitcnt lgkmcnt(4)\n\t" \
  "v_mfma_f32_16x16x32_bf16 v[52:55], v[192:195], v[4:7], v[52:55]\n\t" \
  "buffer_load_dwordx4 v0, s[40:43], s34 offen offset:2048 sc1 lds\n\t" \
  "v_mfma_f32_16x16x32_bf16 v[56:59], v[196:199], v[4:7], v[56:59]\n\t" \
  "buffer_load_dwordx4 v0, s[40:43], s34 offen offset:3072 sc1 lds\n\t" \
  "s_add_u32 m0, m0, 4096\n\t" \
  "s_add_u32 s34, s34, 4096\n\t" \
  "buffer_load_dwordx4 v0, s[40:43], s34 offen offset:0 sc1 lds\n\t" \
  "s_mov_b32 s85, s84\n\t" \
  "s_mov_b32 s84, s83\n\t" \
  "s_mov_b32 s83, s82\n\t" \
  "s_mov_b32 s82, s81\n\t" \
  "s_mov_b32 s81, s80\n\t" \
  "s_add_u32 s80, s81, 1\n\t" \
  "s_cmp_eq_u32 s80, s17\n\t" \
  "s_cselect_b32 s80, s20, s80\n\t" \
  "s_lshr_b64 s[86:87], s[86:87], 2\n\t" \
  "s_add_u32 s95, s95, 36864\n\t" \
  "s_cmp_eq_u32 s95, 147456\n\t" \
  "s_cselect_b32 s95, 36864, s95\n\t" \
  "s_add_u32 s18, s95, s24\n\t" \
  "v_add_u32 v76, 128, v76\n\t" \
  "v_add_u32 v2, s18, v0\n\t" \
  "v_xor_b32 v79, 4096, v79\n\t" \
  "s_waitcnt vmcnt(12)\n\t" \
  "v_cmp_gt_u32 vcc, s97, v78\n\t" \
  "s_waitcnt vmcnt(13) lgkmcnt(0)\n\t" \
  "s_barrier\n\t" \
  "s_cbranch_vccnz SL_t5_%=\n\t" \
  "BK_t5_%=:\n\t" \
  "s_mul_i32 s34, s81, 147456\n\t" \
  "ds_read_b128 v[12:15], v2 offset:0\n\t" \
  "ds_read_b128 v[16:19], v2 offset:1024\n\t" \
  "v_mfma_f32_16x16x32_bf16 v[52:55], v[200:203], v[8:11], v[52:55]\n\t" \
  "s_bfe_u32 s19, s86, 0x20004\n\t" \
  "ds_read_b128 v[4:7], v2 offset:2048\n\t" \
  "s_mul_i32 s94, s83, 36864\n\t" \
  "s_waitcnt vmcnt(10)\n\t" \
  "v_mfma_f32_16x16x32_bf16 v[56:59], v[204:207], v[8:11], v[56:59]\n\t" \
  "s_mul_i32 s19, s19, 4718592\n\t" \
  "ds_read_b128 v[8:11], v2 offset:3072\n\t" \
  "s_add_u32 s94, s94, s31\n\t" \
  "v_mfma_f32_16x16x32_bf16 v[52:55], v[208:211], v[224:227], v[52:55]\n\t" \
  "s_add_u32 s34, s34, s90\n\t" \
  "s_add_u32 s94, s94, s19\n\t" \
  "s_sub_u32 s33, s95, 36864\n\t" \
  "v_mfma_f32_16x16x32_bf16 v[56:59], v[212:215], v[224:227], v[56:59]\n\t" \
  "s_cmp_lt_u32 s33, 36864\n\t" \
  "s_cselect_b32 s33, 110592, s33\n\t" \
  "ds_read_b128 v[224:227], v2 offset:4096\n\t" \
  "v_mfma_f32_16x16x32_bf16 v[52:55], v[216:219], v[228:231], v[52:55]\n\t" \
  "buffer_load_dwordx4 v[248:251], v0, s[48:51], s94 offen sc1\n\t" \
  "v_mfma_f32_16x16x32_bf16 v[56:59], v[220:223], v[228:231], v[56:59]\n\t" \
  "s_mul_i32 s19, s84, 18432\n\t" \
  "ds_read_b128 v[228:231], v2 offset:5120\n\t" \
  "s_add_u32 s19, s19, s23\n\t" \
  "s_waitcnt lgkmcnt(5)\n\t" \
  "v_mfma_f32_16x16x32_bf16 v[60:63], v[80:83], v[12:15], 0\n\t" \
  "buffer_load_dwordx2 v[246:247], v1, s[36:39], s19 offen\n\t" \
  "v_mfma_f32_16x16x32_bf16 v[64:67], v[84:87], v[12:15], 0\n\t" \
  "s_add_u32 m0, s33, s30\n\t" \
  "ds_read_b128 v[12:15], v2 offset:6144\n\t" \
  "v_xor_b32 v232, 6144, v79\n\t" \
  "s_waitcnt lgkmcnt(5)\n\t" \
  "v_mfma_f32_16x16x32_bf16 v[60:63], v[88:91], v[16:19], v[60:63]\n\t" \
  "s_and_b32 s98, s86, 3\n\t" \
  "ds_read_b128 v[232:235], v232\n\t" \
  "ds_read_b32 v238, v76 offset:4\n\t" \
  "v_mfma_f32_16x16x32_bf16 v[64:67], v[92:95], v[16:19], v[64:67]\n\t" \
  "s_mul_i32 s35, s85, 18432\n\t" \
  "s_mul_i32 s99, s85, 36864\n\t" \
  "ds_read_b128 v[16:19], v2 offset:7168\n\t" \
  "s_waitcnt lgkmcnt(7)\n\t" \
  "v_mfma_f32_16x16x32_bf16 v[60:63], v[96:99], v[4:7], v[60:63]\n\t" \
  "buffer_load_dwordx4 v0, s[40:43], s34 offen offset:0 sc1 lds\n\t" \
  "v_mfma_f32_16x16x32_bf16 v[64:67], v[100:103], v[4:7], v[64:67]\n\t" \
  "s_add_u32 s35, s35, s22\n\t" \
  "ds_read_b128 v[4:7], v2 offset:8192\n\t" \
  "v_lshlrev_b32 v240, 16, v236\n\t" \
  "ds_write_b128 v79, v[56:59]\n\t" \
  "s_waitcnt lgkmcnt(8)\n\t" \
  "v_mfma_f32_16x16x32_bf16 v[60:63], v[104:107], v[8:11], v[60:63]\n\t" \
  "s_cmp_eq_u32 s98, 3\n\t" \
  "s_cselect_b32 s54, 0, 14155776\n\t" \
  "s_cselect_b32 s66, 2359296, 0\n\t" \
  "v_mfma_f32_16x16x32_bf16 v[64:67], v[108:111], v[8:11], v[64:67]\n\t" \
  "s_mul_i32 s98, s98, 4718592\n\t" \
  "ds_read_b128 v[8:11], v2 offset:9216\n\t" \
  "s_add_u32 s99, s99, s93\n\t" \
  "s_waitcnt lgkmcnt(8)\n\t" \
  "v_mfma_f32_16x16x32_bf16 v[60:63], v[112:115], v[224:227], v[60:63]\n\t" \
  "buffer_load_dwordx4 v0, s[40:43], s34 offen offset:1024 sc1 lds\n\t" \
  "v_mfma_f32_16x16x32_bf16 v[64:67], v[116:119], v[224:227], v[64:67]\n\t" \
  "s_add_u32 s99, s99, s98\n\t" \
  "ds_read_b128 v[224:227], v2 offset:10240\n\t" \
  "v_and_b32 v241, 0xffff0000, v236\n\t" \
  "s_waitcnt lgkmcnt(8)\n\t" \
  "v_mfma_f32_16x16x32_bf16 v[60:63], v[120:123], v[228:231], v[60:63]\n\t" \
  "v_lshlrev_b32 v242, 16, v237\n\t" \
  "v_and_b32 v243, 0xffff0000, v237\n\t" \
  "v_mfma_f32_16x16x32_bf16 v[64:67], v[124:127], v[228:231], v[64:67]\n\t" \
  "ds_read_b128 v[228:231], v2 offset:11264\n\t" \
  "s_waitcnt lgkmcnt(7)\n\t" \
  "v_add_f32 v232, v232, v68\n\t" \
  "v_mfma_f32_16x16x32_bf16 v[60:63], v[128:131], v[12:15], v[60:63]\n\t" \
  "buffer_load_dwordx4 v0, s[40:43], s34 offen offset:2048 sc1 lds\n\t" \
  "v_mfma_f32_16x16x32_bf16 v[64:67], v[132:135], v[12:15], v[64:67]\n\t" \
  "ds_read_b128 v[12:15], v2 offset:12288\n\t" \
  "v_add_f32 v233, v233, v69\n\t" \
  "s_waitcnt lgkmcnt(6)\n\t" \
  "v_mfma_f32_16x16x32_bf16 v[60:63], v[136:139], v[16:19], v[60:63]\n\t" \
  "v_add_f32 v234, v234, v70\n\t" \
  "v_add_f32 v235, v235, v71\n\t" \
  "v_mfma_f32_16x16x32_bf16 v[64:67], v[140:143], v[16:19], v[64:67]\n\t" \
  "ds_read_b128 v[16:19], v2 offset:13312\n\t" \
  "v_perm_b32 v68, v232, v235, s25\n\t" \
  "s_waitcnt lgkmcnt(6)\n\t" \
  "v_mfma_f32_16x16x32_bf16 v[60:63], v[144:147], v[4:7], v[60:63]\n\t" \
  "buffer_load_dwordx4 v0, s[40:43], s34 offen offset:3072 sc1 lds\n\t" \
  "v_mfma_f32_16x16x32_bf16 v[64:67], v[148:151], v[4:7], v[64:67]\n\t" \
  "s_add_u32 m0, m0, 4096\n\t" \
  "ds_read_b128 v[4:7], v2 offset:14336\n\t" \
  "s_add_u32 s34, s34, 4096\n\t" \
  "s_waitcnt lgkmcnt(5)\n\t" \
  "v_mfma_f32_16x16x32_bf16 v[60:63], v[152:155], v[8:11], v[60:63]\n\t" \
  "v_perm_b32 v69, v233, v235, s26\n\t" \
  "v_perm_b32 v70, v234, v235, s27\n\t" \
  "v_mfma_f32_16x16x32_bf16 v[64:67], v[156:159], v[8:11], v[64:67]\n\t" \
  "ds_read_b128 v[8:11], v2 offset:15360\n\t" \
  "v_mov_b32 v71, s74\n\t" \
  "s_waitcnt lgkmcnt(5)\n\t" \
  "v_mfma_f32_16x16x32_bf16 v[60:63], v[160:163], v[224:227], v[60:63]\n\t" \
  "buffer_load_dwordx4 v0, s[40:43], s34 offen offset:0 sc1 lds\n\t" \
  "v_mfma_f32_16x16x32_bf16 v[64:67], v[164:167], v[224:227], v[64:67]\n\t" \
  "ds_read_b128 v[224:227], v2 offset:16384\n\t" \
  "v_fmac_f32 v240, v238, v232\n\t" \
  "s_waitcnt lgkmcnt(5)\n\t" \
  "v_mfma_f32_16x16x32_bf16 v[60:63], v[168:171], v[228:231], v[60:63]\n\t" \
  "v_fmac_f32 v241, v238, v233\n\t" \
  "v_fmac_f32 v242, v238, v234\n\t" \
  "v_mfma_f32_16x16x32_bf16 v[64:67], v[172:175], v[228:231], v[64:67]\n\t" \
  "ds_read_b128 v[228:231], v2 offset:17408\n\t" \
  "v_fmac_f32 v243, v238, v235\n\t" \
  "s_waitcnt lgkmcnt(5)\n\t" \
  "v_mfma_f32_16x16x32_bf16 v[60:63], v[176:179], v[12:15], v[60:63]\n\t" \
  "buffer_load_dwordx4 v0, s[40:43], s34 offen offset:1024 sc1 lds\n\t" \
  "v_mfma_f32_16x16x32_bf16 v[64:67], v[180:183], v[12:15], v[64:67]\n\t" \
  "v_cvt_pk_bf16_f32 v244, v240, v241\n\t" \
  "v_cvt_pk_bf16_f32 v245, v242, v243\n\t" \
  "s_waitcnt lgkmcnt(4)\n\t" \
  "v_mfma_f32_16x16x32_bf16 v[60:63], v[184:187], v[16:19], v[60:63]\n\t" \
  "buffer_store_dwordx2 v[244:245], v239, s[64:67], s35 offen\n\t" \
  "v_mfma_f32_16x16x32_bf16 v[64:67], v[188:191], v[16:19], v[64:67]\n\t" \
  "buffer_load_dwordx4 v0, s[40:43], s34 offen offset:2048 sc1 lds\n\t" \
  "s_waitcnt lgkmcnt(3)\n\t" \
  "v_mfma_f32_16x16x32_bf16 v[60:63], v[192:195], v[4:7], v[60:63]\n\t" \
  "buffer_store_dwordx4 v[68:71], v0, s[52:55], s99 offen sc1\n\t" \
  "v_mfma_f32_16x16x32_bf16 v[64:67], v[196:199], v[4:7], v[64:67]\n\t" \
  "buffer_load_dwordx4 v0, s[40:43], s34 offen offset:3072 sc1 lds\n\t" \
  "s_add_u32 m0, m0, 4096\n\t" \
  "s_add_u32 s34, s34, 4096\n\t" \
  "buffer_load_dwordx4 v0, s[40:43], s34 offen offset:0 sc1 lds\n\t" \
  "s_waitcnt vmcnt(24)\n\t" \
  "v_cmp_ne_u32 vcc, s75, v75\n\t" \
  "s_cbranch_vccnz SL_q6_%=\n\t" \
  "BK_q6_%=:\n\t" \
  "s_nop 0\n\t" \
  "v_perm_b32 v75, v72, v73, s28\n\t" \
  "v_add_f32 v68, v52, v72\n\t" \
  "v_perm_b32 v75, v75, v74, s29\n\t" \
  "v_add_f32 v69, v53, v73\n\t" \
  "v_add_f32 v70, v54, v74\n\t" \
  "v_add_f32 v71, v55, v75\n\t" \
  "s_mov_b32 s85, s84\n\t" \
  "s_mov_b32 s84, s83\n\t" \
  "s_mov_b32 s83, s82\n\t" \
  "s_mov_b32 s82, s81\n\t" \
  "s_mov_b32 s81, s80\n\t" \
  "s_add_u32 s80, s81, 1\n\t" \
  "s_cmp_eq_u32 s80, s17\n\t" \
  "s_cselect_b32 s80, s20, s80\n\t" \
  "s_lshr_b64 s[86:87], s[86:87], 2\n\t" \
  "s_add_u32 s95, s95, 36864\n\t" \
  "s_cmp_eq_u32 s95, 147456\n\t" \
  "s_cselect_b32 s95, 36864, s95\n\t" \
  "s_add_u32 s18, s95, s24\n\t" \
  "v_add_u32 v76, 128, v76\n\t" \
  "v_add_u32 v2, s18, v0\n\t" \
  "v_xor_b32 v79, 4096, v79\n\t" \
  "s_waitcnt vmcnt(13) lgkmcnt(0)\n\t" \
  "s_barrier\n\t" \
  "s_mov_b32 s66, 2359296\n\t" \
  "ds_read_b128 v[12:15], v2 offset:0\n\t" \
  "ds_read_b128 v[16:19], v2 offset:1024\n\t" \
  "v_mfma_f32_16x16x32_bf16 v[60:63], v[200:203], v[8:11], v[60:63]\n\t" \
  "s_bfe_u32 s19, s86, 0x20004\n\t" \
  "ds_read_b128 v[4:7], v2 offset:2048\n\t" \
  "s_mul_i32 s96, s83, 36864\n\t" \
  "s_waitcnt vmcnt(11)\n\t" \
  "v_mfma_f32_16x16x32_bf16 v[64:67], v[204:207], v[8:11], v[64:67]\n\t" \
  "s_mul_i32 s19, s19, 4718592\n\t" \
  "ds_read_b128 v[8:11], v2 offset:3072\n\t" \
  "s_add_u32 s96, s96, s31\n\t" \
  "v_mfma_f32_16x16x32_bf16 v[60:63], v[208:211], v[224:227], v[60:63]\n\t" \
  "s_add_u32 s96, s96, s19\n\t" \
  "v_xor_b32 v232, 6144, v79\n\t" \
  "ds_read_b128 v[232:235], v232\n\t" \
  "v_mfma_f32_16x16x32_bf16 v[64:67], v[212:215], v[224:227], v[64:67]\n\t" \
  "s_mul_i32 s35, s85, 18432\n\t" \
  "ds_read_b128 v[224:227], v2 offset:4096\n\t" \
  "ds_read_b32 v238, v76 offset:4\n\t" \
  "v_mfma_f32_16x16x32_bf16 v[60:63], v[216:219], v[228:231], v[60:63]\n\t" \
  "buffer_load_dwordx4 v[72:75], v0, s[48:51], s96 offen sc1\n\t" \
  "v_mfma_f32_16x16x32_bf16 v[64:67], v[220:223], v[228:231], v[64:67]\n\t" \
  "s_mul_i32 s19, s84, 18432\n\t" \
  "ds_read_b128 v[228:231], v2 offset:5120\n\t" \
  "s_add_u32 s19, s19, s23\n\t" \
  "s_waitcnt lgkmcnt(7)\n\t" \
  "v_mfma_f32_16x16x32_bf16 v[52:55], v[80:83], v[12:15], 0\n\t" \
  "buffer_load_dwordx2 v[236:237], v1, s[36:39], s19 offen\n\t" \
  "v_mfma_f32_16x16x32_bf16 v[56:59], v[84:87], v[12:15], 0\n\t" \
  "s_add_u32 s35, s35, s22\n\t" \
  "ds_read_b128 v[12:15], v2 offset:6144\n\t" \
  "v_lshlrev_b32 v240, 16, v246\n\t" \
  "s_waitcnt lgkmcnt(7)\n\t" \
  "v_mfma_f32_16x16x32_bf16 v[52:55], v[88:91], v[16:19], v[52:55]\n\t" \
  "v_and_b32 v241, 0xffff0000, v246\n\t" \
  "v_lshlrev_b32 v242, 16, v247\n\t" \
  "v_mfma_f32_16x16x32_bf16 v[56:59], v[92:95], v[16:19], v[56:59]\n\t" \
  "ds_read_b128 v[16:19], v2 offset:7168\n\t" \
  "v_and_b32 v243, 0xffff0000, v247\n\t" \
  "s_waitcnt lgkmcnt(7)\n\t" \
  "v_mfma_f32_16x16x32_bf16 v[52:55], v[96:99], v[4:7], v[52:55]\n\t" \
  "s_waitcnt lgkmcnt(5)\n\t" \
  "v_add_f32 v232, v232, v68\n\t" \
  "v_add_f32 v233, v233, v69\n\t" \
  "v_mfma_f32_16x16x32_bf16 v[56:59], v[100:103], v[4:7], v[56:59]\n\t" \
  "ds_read_b128 v[4:7], v2 offset:8192\n\t" \
  "v_add_f32 v234, v234, v70\n\t" \
  "v_mfma_f32_16x16x32_bf16 v[52:55], v[104:107], v[8:11], v[52:55]\n\t" \
  "v_add_f32 v235, v235, v71\n\t" \
  "v_mfma_f32_16x16x32_bf16 v[56:59], v[108:111], v[8:11], v[56:59]\n\t" \
  "ds_read_b128 v[8:11], v2 offset:9216\n\t" \
  "s_waitcnt lgkmcnt(5)\n\t" \
  "v_fmac_f32 v240, v238, v232\n\t" \
  "v_mfma_f32_16x16x32_bf16 v[52:55], v[112:115], v[224:227], v[52:55]\n\t" \
  "v_fmac_f32 v241, v238, v233\n\t" \
  "v_fmac_f32 v242, v238, v234\n\t" \
  "v_mfma_f32_16x16x32_bf16 v[56:59], v[116:119], v[224:227], v[56:59]\n\t" \
  "ds_read_b128 v[224:227], v2 offset:10240\n\t" \
  "v_fmac_f32 v243, v238, v235\n\t" \
  "s_waitcnt lgkmcnt(5)\n\t" \
  "v_mfma_f32_16x16x32_bf16 v[52:55], v[120:123], v[228:231], v[52:55]\n\t" \
  "v_cvt_pk_bf16_f32 v244, v240, v241\n\t" \
  "v_cvt_pk_bf16_f32 v245, v242, v243\n\t" \
  "v_mfma_f32_16x16x32_bf16 v[56:59], v[124:127], v[228:231], v[56:59]\n\t" \
  "ds_read_b128 v[228:231], v2 offset:11264\n\t" \
  "s_waitcnt lgkmcnt(5)\n\t" \
  "v_mfma_f32_16x16x32_bf16 v[52:55], v[128:131], v[12:15], v[52:55]\n\t" \
  "buffer_store_dwordx2 v[244:245], v239, s[64:67], s35 offen\n\t" \
  "v_mfma_f32_16x16x32_bf16 v[56:59], v[132:135], v[12:15], v[56:59]\n\t" \
  "ds_read_b128 v[12:15], v2 offset:12288\n\t" \
  "s_waitcnt vmcnt(15)\n\t" \
  "v_cmp_ne_u32 vcc, s75, v251\n\t" \
  "s_waitcnt lgkmcnt(5)\n\t" \
  "v_mfma_f32_16x16x32_bf16 v[52:55], v[136:139], v[16:19], v[52:55]\n\t" \
  "s_cbranch_vccnz SL_q7_%=\n\t" \
  "BK_q7_%=:\n\t" \
  "ds_write_b128 v79, v[64:67]\n\t" \
  "v_mfma_f32_16x16x32_bf16 v[56:59], v[140:143], v[16:19], v[56:59]\n\t" \
  "s_nop 0\n\t" \
  "ds_read_b128 v[16:19], v2 offset:13312\n\t" \
  "v_perm_b32 v251, v248, v249, s28\n\t" \
  "s_waitcnt lgkmcnt(6)\n\t" \
  "v_mfma_f32_16x16x32_bf16 v[52:55], v[144:147], v[4:7], v[52:55]\n\t" \
  "v_add_f32 v68, v60, v248\n\t" \
  "v_perm_b32 v251, v251, v250, s29\n\t" \
  "v_mfma_f32_16x16x32_bf16 v[56:59], v[148:151], v[4:7], v[56:59]\n\t" \
  "ds_read_b128 v[4:7], v2 offset:14336\n\t" \
  "v_add_f32 v69, v61, v249\n\t" \
  "s_waitcnt lgkmcnt(6)\n\t" \
  "v_mfma_f32_16x16x32_bf16 v[52:55], v[152:155], v[8:11], v[52:55]\n\t" \
  "v_add_f32 v70, v62, v250\n\t" \
  "v_add_f32 v71, v63, v251\n\t" \
  "v_mfma_f32_16x16x32_bf16 v[56:59], v[156:159], v[8:11], v[56:59]\n\t" \
  "ds_read_b128 v[8:11], v2 offset:15360\n\t" \
  "s_waitcnt lgkmcnt(6)\n\t" \
  "v_mfma_f32_16x16x32_bf16 v[52:55], v[160:163], v[224:227], v[52:55]\n\t" \
  "v_mfma_f32_16x16x32_bf16 v[56:59], v[164:167], v[224:227], v[56:59]\n\t" \
  "ds_read_b128 v[224:227], v2 offset:16384\n\t" \
  "s_waitcnt lgkmcnt(6)\n\t" \
  "v_mfma_f32_16x16x32_bf16 v[52:55], v[168:171], v[228:231], v[52:55]\n\t" \
  "v_mfma_f32_16x16x32_bf16 v[56:59], v[172:175], v[228:231], v[56:59]\n\t" \
  "ds_read_b128 v[228:231], v2 offset:17408\n\t" \
  "s_waitcnt lgkmcnt(6)\n\t" \
  "v_mfma_f32_16x16x32_bf16 v[52:55], v[176:179], v[12:15], v[52:55]\n\t" \
  "s_mov_b32 s85, s84\n\t" \
  "s_mov_b32 s84, s83\n\t" \
  "s_mov_b32 s83, s82\n\t" \
  "v_mfma_f32_16x16x32_bf16 v[56:59], v[180:183], v[12:15], v[56:59]\n\t" \
  "s_mov_b32 s82, s81\n\t" \
  "s_mov_b32 s81, s80\n\t" \
  "s_add_u32 s80, s81, 1\n\t" \
  "s_waitcnt lgkmcnt(4)\n\t" \
  "v_mfma_f32_16x16x32_bf16 v[52:55], v[184:187], v[16:19], v[52:55]\n\t" \
  "s_cmp_eq_u32 s80, s17\n\t" \
  "s_cselect_b32 s80, s20, s80\n\t" \
  "s_lshr_b64 s[86:87], s[86:87], 2\n\t" \
  "v_mfma_f32_16x16x32_bf16 v[56:59], v[188:191], v[16:19], v[56:59]\n\t" \
  "s_add_u32 s95, s95, 36864\n\t" \
  "s_cmp_eq_u32 s95, 147456\n\t" \
  "s_cselect_b32 s95, 36864, s95\n\t" \
  "s_waitcnt lgkmcnt(3)\n\t" \
  "v_mfma_f32_16x16x32_bf16 v[52:55], v[192:195], v[4:7], v[52:55]\n\t" \
  "s_add_u32 s18, s95, s24\n\t" \
  "v_add_u32 v76, 128, v76\n\t" \
  "v_add_u32 v2, s18, v0\n\t" \
  "v_mfma_f32_16x16x32_bf16 v[56:59], v[196:199], v[4:7], v[56:59]\n\t" \
  "v_xor_b32 v79, 4096, v79\n\t" \
  "s_waitcnt vmcnt(3) lgkmcnt(0)\n\t" \
  "s_barrier\n\t" \
  "ds_read_b128 v[12:15], v2 offset:0\n\t" \
  "ds_read_b128 v[16:19], v2 offset:1024\n\t" \
  "v_mfma_f32_16x16x32_bf16 v[52:55], v[200:203], v[8:11], v[52:55]\n\t" \
  "s_bfe_u32 s19, s86, 0x20004\n\t" \
  "ds_read_b128 v[4:7], v2 offset:2048\n\t" \
  "s_mul_i32 s94, s83, 36864\n\t" \
  "s_waitcnt vmcnt(1)\n\t" \
  "v_mfma_f32_16x16x32_bf16 v[56:59], v[204:207], v[8:11], v[56:59]\n\t" \
  "s_mul_i32 s19, s19, 4718592\n\t" \
  "ds_read_b128 v[8:11], v2 offset:3072\n\t" \
  "s_add_u32 s94, s94, s31\n\t" \
  "v_mfma_f32_16x16x32_bf16 v[52:55], v[208:211], v[224:227], v[52:55]\n\t" \
  "s_add_u32 s94, s94, s19\n\t" \
  "v_xor_b32 v232, 6144, v79\n\t" \
  "ds_read_b128 v[232:235], v232\n\t" \
  "v_mfma_f32_16x16x32_bf16 v[56:59], v[212:215], v[224:227], v[56:59]\n\t" \
  "s_mul_i32 s35, s85, 18432\n\t" \
  "ds_read_b128 v[224:227], v2 offset:4096\n\t" \
  "ds_read_b32 v238, v76 offset:4\n\t" \
  "v_mfma_f32_16x16x32_bf16 v[52:55], v[216:219], v[228:231], v[52:55]\n\t" \
  "buffer_load_dwordx4 v[248:251], v0, s[48:51], s94 offen sc1\n\t" \
  "v_mfma_f32_16x16x32_bf16 v[56:59], v[220:223], v[228:231], v[56:59]\n\t" \
  "s_mul_i32 s19, s84, 18432\n\t" \
  "ds_read_b128 v[228:231], v2 offset:5120\n\t" \
  "s_add_u32 s19, s19, s23\n\t" \
  "s_waitcnt lgkmcnt(7)\n\t" \
  "v_mfma_f32_16x16x32_bf16 v[60:63], v[80:83], v[12:15], 0\n\t" \
  "buffer_load_dwordx2 v[246:247], v1, s[36:39], s19 offen\n\t" \
  "v_mfma_f32_16x16x32_bf16 v[64:67], v[84:87], v[12:15], 0\n\t" \
  "s_add_u32 s35, s35, s22\n\t" \
  "ds_read_b128 v[12:15], v2 offset:6144\n\t" \
  "v_lshlrev_b32 v240, 16, v236\n\t" \
  "s_waitcnt lgkmcnt(7)\n\t" \
  "v_mfma_f32_16x16x32_bf16 v[60:63], v[88:91], v[16:19], v[60:63]\n\t" \
  "v_and_b32 v241, 0xffff0000, v236\n\t" \
  "v_lshlrev_b32 v242, 16, v237\n\t" \
  "v_mfma_f32_16x16x32_bf16 v[64:67], v[92:95], v[16:19], v[64:67]\n\t" \
  "ds_read_b128 v[16:19], v2 offset:7168\n\t" \
  "v_and_b32 v243, 0xffff0000, v237\n\t" \
  "s_waitcnt lgkmcnt(7)\n\t" \
  "v_mfma_f32_16x16x32_bf16 v[60:63], v[96:99], v[4:7], v[60:63]\n\t" \
  "s_waitcnt lgkmcnt(5)\n\t" \
  "v_add_f32 v232, v232, v68\n\t" \
  "v_add_f32 v233, v233, v69\n\t" \
  "v_mfma_f32_16x16x32_bf16 v[64:67], v[100:103], v[4:7], v[64:67]\n\t" \
  "ds_read_b128 v[4:7], v2 offset:8192\n\t" \
  "v_add_f32 v234, v234, v70\n\t" \
  "v_mfma_f32_16x16x32_bf16 v[60:63], v[104:107], v[8:11], v[60:63]\n\t" \
  "v_add_f32 v235, v235, v71\n\t" \
  "v_mfma_f32_16x16x32_bf16 v[64:67], v[108:111], v[8:11], v[64:67]\n\t" \
  "ds_read_b128 v[8:11], v2 offset:9216\n\t" \
  "s_waitcnt lgkmcnt(5)\n\t" \
  "v_fmac_f32 v240, v238, v232\n\t" \
  "v_mfma_f32_16x16x32_bf16 v[60:63], v[112:115], v[224:227], v[60:63]\n\t" \
  "v_fmac_f32 v241, v238, v233\n\t" \
  "v_fmac_f32 v242, v238, v234\n\t" \
  "v_mfma_f32_16x16x32_bf16 v[64:67], v[116:119], v[224:227], v[64:67]\n\t" \
  "ds_read_b128 v[224:227], v2 offset:10240\n\t" \
  "v_fmac_f32 v243, v238, v235\n\t" \
  "s_waitcnt lgkmcnt(5)\n\t" \
  "v_mfma_f32_16x16x32_bf16 v[60:63], v[120:123], v[228:231], v[60:63]\n\t" \
  "v_cvt_pk_bf16_f32 v244, v240, v241\n\t" \
  "v_cvt_pk_bf16_f32 v245, v242, v243\n\t" \
  "v_mfma_f32_16x16x32_bf16 v[64:67], v[124:127], v[228:231], v[64:67]\n\t" \
  "ds_read_b128 v[228:231], v2 offset:11264\n\t" \
  "s_waitcnt lgkmcnt(5)\n\t" \
  "v_mfma_f32_16x16x32_bf16 v[60:63], v[128:131], v[12:15], v[60:63]\n\t" \
  "buffer_store_dwordx2 v[244:245], v239, s[64:67], s35 offen\n\t" \
  "v_mfma_f32_16x16x32_bf16 v[64:67], v[132:135], v[12:15], v[64:67]\n\t" \
  "ds_read_b128 v[12:15], v2 offset:12288\n\t" \
  "s_waitcnt vmcnt(5)\n\t" \
  "v_cmp_ne_u32 vcc, s75, v75\n\t" \
  "s_waitcnt lgkmcnt(5)\n\t" \
  "v_mfma_f32_16x16x32_bf16 v[60:63], v[136:139], v[16:19], v[60:63]\n\t" \
  "s_cbranch_vccnz SL_q8_%=\n\t" \
  "BK_q8_%=:\n\t" \
  "ds_write_b128 v79, v[56:59]\n\t" \
  "v_mfma_f32_16x16x32_bf16 v[64:67], v[140:143], v[16:19], v[64:67]\n\t" \
  "s_nop 0\n\t" \
  "ds_read_b128 v[16:19], v2 offset:13312\n\t" \
  "v_perm_b32 v75, v72, v73, s28\n\t" \
  "s_waitcnt lgkmcnt(6)\n\t" \
  "v_mfma_f32_16x16x32_bf16 v[60:63], v[144:147], v[4:7], v[60:63]\n\t" \
  "v_add_f32 v68, v52, v72\n\t" \
  "v_perm_b32 v75, v75, v74, s29\n\t" \
  "v_mfma_f32_16x16x32_bf16 v[64:67], v[148:151], v[4:7], v[64:67]\n\t" \
  "ds_read_b128 v[4:7], v2 offset:14336\n\t" \
  "v_add_f32 v69, v53, v73\n\t" \
  "s_waitcnt lgkmcnt(6)\n\t" \
  "v_mfma_f32_16x16x32_bf16 v[60:63], v[152:155], v[8:11], v[60:63]\n\t" \
  "v_add_f32 v70, v54, v74\n\t" \
  "v_add_f32 v71, v55, v75\n\t" \
  "v_mfma_f32_16x16x32_bf16 v[64:67], v[156:159], v[8:11], v[64:67]\n\t" \
  "ds_read_b128 v[8:11], v2 offset:15360\n\t" \
  "s_waitcnt lgkmcnt(6)\n\t" \
  "v_mfma_f32_16x16x32_bf16 v[60:63], v[160:163], v[224:227], v[60:63]\n\t" \
  "v_mfma_f32_16x16x32_bf16 v[64:67], v[164:167], v[224:227], v[64:67]\n\t" \
  "ds_read_b128 v[224:227], v2 offset:16384\n\t" \
  "s_waitcnt lgkmcnt(6)\n\t" \
  "v_mfma_f32_16x16x32_bf16 v[60:63], v[168:171], v[228:231], v[60:63]\n\t" \
  "v_mfma_f32_16x16x32_bf16 v[64:67], v[172:175], v[228:231], v[64:67]\n\t" \
  "ds_read_b128 v[228:231], v2 offset:17408\n\t" \
  "s_waitcnt lgkmcnt(6)\n\t" \
  "v_mfma_f32_16x16x32_bf16 v[60:63], v[176:179], v[12:15], v[60:63]\n\t" \
  "s_mov_b32 s85, s84\n\t" \
  "s_mov_b32 s84, s83\n\t" \
  "s_mov_b32 s83, s82\n\t" \
  "v_mfma_f32_16x16x32_bf16 v[64:67], v[180:183], v[12:15], v[64:67]\n\t" \
  "s_mov_b32 s82, s81\n\t" \
  "s_mov_b32 s81, s80\n\t" \
  "s_add_u32 s80, s81, 1\n\t" \
  "s_waitcnt lgkmcnt(4)\n\t" \
  "v_mfma_f32_16x16x32_bf16 v[60:63], v[184:187], v[16:19], v[60:63]\n\t" \
  "s_cmp_eq_u32 s80, s17\n\t" \
  "s_cselect_b32 s80, s20, s80\n\t" \
  "s_lshr_b64 s[86:87], s[86:87], 2\n\t" \
  "v_mfma_f32_16x16x32_bf16 v[64:67], v[188:191], v[16:19], v[64:67]\n\t" \
  "s_add_u32 s95, s95, 36864\n\t" \
  "s_cmp_eq_u32 s95, 147456\n\t" \
  "s_cselect_b32 s95, 36864, s95\n\t" \
  "s_waitcnt lgkmcnt(3)\n\t" \
  "v_mfma_f32_16x16x32_bf16 v[60:63], v[192:195], v[4:7], v[60:63]\n\t" \
  "s_add_u32 s18, s95, s24\n\t" \
  "v_add_u32 v76, 128, v76\n\t" \
  "v_add_u32 v2, s18, v0\n\t" \
  "v_mfma_f32_16x16x32_bf16 v[64:67], v[196:199], v[4:7], v[64:67]\n\t" \
  "v_xor_b32 v79, 4096, v79\n\t" \
  "s_waitcnt lgkmcnt(0)\n\t" \
  "s_barrier\n\t" \
  "v_mfma_f32_16x16x32_bf16 v[60:63], v[200:203], v[8:11], v[60:63]\n\t" \
  "s_mul_i32 s19, s84, 18432\n\t" \
  "s_add_u32 s19, s19, s23\n\t" \
  "v_xor_b32 v232, 6144, v79\n\t" \
  "s_waitcnt vmcnt(1)\n\t" \
  "v_mfma_f32_16x16x32_bf16 v[64:67], v[204:207], v[8:11], v[64:67]\n\t" \
  "buffer_load_dwordx2 v[236:237], v1, s[36:39], s19 offen\n\t" \
  "v_mfma_f32_16x16x32_bf16 v[60:63], v[208:211], v[224:227], v[60:63]\n\t" \
  "s_mul_i32 s35, s85, 18432\n\t" \
  "ds_read_b128 v[232:235], v232\n\t" \
  "ds_read_b32 v238, v76 offset:4\n\t" \
  "v_mfma_f32_16x16x32_bf16 v[64:67], v[212:215], v[224:227], v[64:67]\n\t" \
  "s_add_u32 s35, s35, s22\n\t" \
  "v_lshlrev_b32 v240, 16, v246\n\t" \
  "v_and_b32 v241, 0xffff0000, v246\n\t" \
  "v_mfma_f32_16x16x32_bf16 v[60:63], v[216:219], v[228:231], v[60:63]\n\t" \
  "v_lshlrev_b32 v242, 16, v247\n\t" \
  "v_and_b32 v243, 0xffff0000, v247\n\t" \
  "v_mfma_f32_16x16x32_bf16 v[64:67], v[220:223], v[228:231], v[64:67]\n\t" \
  "s_waitcnt lgkmcnt(1)\n\t" \
  "v_add_f32 v232, v232, v68\n\t" \
  "v_add_f32 v233, v233, v69\n\t" \
  "v_add_f32 v234, v234, v70\n\t" \
  "v_add_f32 v235, v235, v71\n\t" \
  "s_waitcnt lgkmcnt(0)\n\t" \
  "v_fmac_f32 v240, v238, v232\n\t" \
  "v_fmac_f32 v241, v238, v233\n\t" \
  "v_fmac_f32 v242, v238, v234\n\t" \
  "v_fmac_f32 v243, v238, v235\n\t" \
  "v_cvt_pk_bf16_f32 v244, v240, v241\n\t" \
  "v_cvt_pk_bf16_f32 v245, v242, v243\n\t" \
  "buffer_store_dwordx2 v[244:245], v239, s[64:67], s35 offen\n\t" \
  "s_nop 7\n\t" \
  "ds_write_b128 v79, v[64:67]\n\t" \
  "s_waitcnt vmcnt(4)\n\t" \
  "v_cmp_ne_u32 vcc, s75, v251\n\t" \
  "s_cbranch_vccnz SL_q9_%=\n\t" \
  "BK_q9_%=:\n\t" \
  "v_perm_b32 v251, v248, v249, s28\n\t" \
  "v_add_f32 v68, v60, v248\n\t" \
  "v_perm_b32 v251, v251, v250, s29\n\t" \
  "v_add_f32 v69, v61, v249\n\t" \
  "v_add_f32 v70, v62, v250\n\t" \
  "v_add_f32 v71, v63, v251\n\t" \
  "s_mov_b32 s85, s84\n\t" \
  "s_mov_b32 s84, s83\n\t" \
  "s_mov_b32 s83, s82\n\t" \
  "s_mov_b32 s82, s81\n\t" \
  "s_mov_b32 s81, s80\n\t" \
  "v_add_u32 v76, 128, v76\n\t" \
  "v_xor_b32 v79, 4096, v79\n\t" \
  "s_waitcnt lgkmcnt(0)\n\t" \
  "s_barrier\n\t" \
  "v_xor_b32 v232, 6144, v79\n\t" \
  "ds_read_b128 v[232:235], v232\n\t" \
  "ds_read_b32 v238, v76 offset:4\n\t" \
  "s_mul_i32 s35, s85, 18432\n\t" \
  "s_add_u32 s35, s35, s22\n\t" \
  "s_waitcnt vmcnt(1)\n\t" \
  "v_lshlrev_b32 v240, 16, v236\n\t" \
  "v_and_b32 v241, 0xffff0000, v236\n\t" \
  "v_lshlrev_b32 v242, 16, v237\n\t" \
  "v_and_b32 v243, 0xffff0000, v237\n\t" \
  "s_waitcnt lgkmcnt(1)\n\t" \
  "v_add_f32 v232, v232, v68\n\t" \
  "v_add_f32 v233, v233, v69\n\t" \
  "v_add_f32 v234, v234, v70\n\t" \
  "v_add_f32 v235, v235, v71\n\t" \
  "s_waitcnt lgkmcnt(0)\n\t" \
  "v_fmac_f32 v240, v238, v232\n\t" \
  "v_fmac_f32 v241, v238, v233\n\t" \
  "v_fmac_f32 v242, v238, v234\n\t" \
  "v_fmac_f32 v243, v238, v235\n\t" \
  "v_cvt_pk_bf16_f32 v244, v240, v241\n\t" \
  "v_cvt_pk_bf16_f32 v245, v242, v243\n\t" \
  "buffer_store_dwordx2 v[244:245], v239, s[64:67], s35 offen\n\t" \
  "s_branch END_%=\n\t" \
  "SL_r2_%=:\n\t" \
  "s_call_b64 s[68:69], WAIT_F_%=\n\t" \
  "s_branch BK_r2_%=\n\t" \
  "SL_r3_%=:\n\t" \
  "s_call_b64 s[68:69], WAIT_F_%=\n\t" \
  "s_branch BK_r3_%=\n\t" \
  "SL_r4_%=:\n\t" \
  "s_call_b64 s[68:69], WAIT_F_%=\n\t" \
  "s_branch BK_r4_%=\n\t" \
  "X1_%=:\n\t" \
  "s_call_b64 s[68:69], WAIT_FQ_%=\n\t" \
  "s_cmp_lg_u32 s72, s73\n\t" \
  "s_cbranch_scc1 MIX0_%=\n\t" \
  "s_nop 7\n\t" \
  "v_mov_b32 v20, v36\n\t" \
  "v_mov_b32 v21, v37\n\t" \
  "v_mov_b32 v22, v38\n\t" \
  "v_mov_b32 v23, v39\n\t" \
  "v_mov_b32 v24, v40\n\t" \
  "v_mov_b32 v25, v41\n\t" \
  "v_mov_b32 v26, v42\n\t" \
  "v_mov_b32 v27, v43\n\t" \
  "v_mov_b32 v28, v44\n\t" \
  "v_mov_b32 v29, v45\n\t" \
  "v_mov_b32 v30, v46\n\t" \
  "v_mov_b32 v31, v47\n\t" \
  "v_mov_b32 v32, v48\n\t" \
  "v_mov_b32 v33, v49\n\t" \
  "v_mov_b32 v34, v50\n\t" \
  "v_mov_b32 v35, v51\n\t" \
  "v_mov_b32 v52, v60\n\t" \
  "v_mov_b32 v53, v61\n\t" \
  "v_mov_b32 v54, v62\n\t" \
  "v_mov_b32 v55, v63\n\t" \
  "v_mov_b32 v56, v64\n\t" \
  "v_mov_b32 v57, v65\n\t" \
  "v_mov_b32 v58, v66\n\t" \
  "v_mov_b32 v59, v67\n\t" \
  "s_nop 1\n\t" \
  "s_branch TAIL_%=\n\t" \
  "SL_t0_%=:\n\t" \
  "s_call_b64 s[68:69], WAIT_F_%=\n\t" \
  "s_branch BK_t0_%=\n\t" \
  "SL_q1_%=:\n\t" \
  "s_call_b64 s[68:69], WAIT_Q1_%=\n\t" \
  "s_branch BK_q1_%=\n\t" \
  "SL_t1_%=:\n\t" \
  "s_call_b64 s[68:69], WAIT_F_%=\n\t" \
  "s_branch BK_t1_%=\n\t" \
  "SL_q2_%=:\n\t" \
  "s_call_b64 s[68:69], WAIT_Q0_%=\n\t" \
  "s_branch BK_q2_%=\n\t" \
  "SL_t2_%=:\n\t" \
  "s_call_b64 s[68:69], WAIT_F_%=\n\t" \
  "s_branch BK_t2_%=\n\t" \
  "SL_q3_%=:\n\t" \
  "s_call_b64 s[68:69], WAIT_Q1_%=\n\t" \
  "s_branch BK_q3_%=\n\t" \
  "SL_t3_%=:\n\t" \
  "s_call_b64 s[68:69], WAIT_F_%=\n\t" \
  "s_branch BK_t3_%=\n\t" \
  "SL_q4_%=:\n\t" \
  "s_call_b64 s[68:69], WAIT_Q0_%=\n\t" \
  "s_branch BK_q4_%=\n\t" \
  "SL_t4_%=:\n\t" \
  "s_call_b64 s[68:69], WAIT_F_%=\n\t" \
  "s_branch BK_t4_%=\n\t" \
  "SL_q5_%=:\n\t" \
  "s_call_b64 s[68:69], WAIT_Q1_%=\n\t" \
  "s_branch BK_q5_%=\n\t" \
  "SL_t5_%=:\n\t" \
  "s_call_b64 s[68:69], WAIT_F_%=\n\t" \
  "s_branch BK_t5_%=\n\t" \
  "SL_q6_%=:\n\t" \
  "s_call_b64 s[68:69], WAIT_Q0_%=\n\t" \
  "s_branch BK_q6_%=\n\t" \
  "SL_q7_%=:\n\t" \
  "s_call_b64 s[68:69], WAIT_Q1_%=\n\t" \
  "s_branch BK_q7_%=\n\t" \
  "SL_q8_%=:\n\t" \
  "s_call_b64 s[68:69], WAIT_Q0_%=\n\t" \
  "s_branch BK_q8_%=\n\t" \
  "SL_q9_%=:\n\t" \
  "s_call_b64 s[68:69], WAIT_Q1_%=\n\t" \
  "s_branch BK_q9_%=\n\t" \
  "WAIT_F_%=:\n\t" \
  "s_mov_b32 s21, 40000\n\t" \
  "WaL_%=:\n\t" \
  "v_cmp_gt_u32 vcc, s97, v78\n\t" \
  "s_sub_u32 s21, s21, 1\n\t" \
  "s_cbranch_vccz WaD_%=\n\t" \
  "s_cmp_eq_u32 s21, 0\n\t" \
  "s_cbranch_scc1 WaD_%=\n\t" \
  "buffer_load_dword v78, off, s[60:63], s16 offset:16 sc1\n\t" \
  "s_waitcnt vmcnt(0)\n\t" \
  "s_branch WaL_%=\n\t" \
  "WaD_%=:\n\t" \
  "s_setpc_b64 s[68:69]\n\t" \
  "WAIT_FQ_%=:\n\t" \
  "s_mov_b32 s21, 40000\n\t" \
  "WbL_%=:\n\t" \
  "v_cmp_gt_u32 vcc, s97, v78\n\t" \
  "s_sub_u32 s21, s21, 1\n\t" \
  "s_cbranch_vccz WbD_%=\n\t" \
  "s_cmp_eq_u32 s21, 0\n\t" \
  "s_cbranch_scc1 WbD_%=\n\t" \
  "buffer_load_dword v78, off, s[60:63], s16 offset:16 sc1\n\t" \
  "s_waitcnt vmcnt(0)\n\t" \
  "s_branch WbL_%=\n\t" \
  "WbD_%=:\n\t" \
  "WAIT_Q0_%=:\n\t" \
  "s_mov_b32 s21, 40000\n\t" \
  "WcL_%=:\n\t" \
  "v_cmp_ne_u32 vcc, s75, v75\n\t" \
  "s_sub_u32 s21, s21, 1\n\t" \
  "s_cbranch_vccz WcD_%=\n\t" \
  "s_cmp_eq_u32 s21, 0\n\t" \
  "s_cbranch_scc1 WcD_%=\n\t" \
  "buffer_load_dwordx4 v[72:75], v0, s[48:51], s96 offen sc1\n\t" \
  "s_waitcnt vmcnt(0)\n\t" \
  "s_branch WcL_%=\n\t" \
  "WcD_%=:\n\t" \
  "s_setpc_b64 s[68:69]\n\t" \
  "WAIT_Q1_%=:\n\t" \
  "s_mov_b32 s21, 40000\n\t" \
  "WdL_%=:\n\t" \
  "v_cmp_ne_u32 vcc, s75, v251\n\t" \
  "s_sub_u32 s21, s21, 1\n\t" \
  "s_cbranch_vccz WdD_%=\n\t" \
  "s_cmp_eq_u32 s21, 0\n\t" \
  "s_cbranch_scc1 WdD_%=\n\t" \
  "buffer_load_dwordx4 v[248:251], v0, s[48:51], s94 offen sc1\n\t" \
  "s_waitcnt vmcnt(0)\n\t" \
  "s_branch WdL_%=\n\t" \
  "WdD_%=:\n\t" \
  "s_setpc_b64 s[68:69]\n\t" \
  "END_%=:\n\t" \
  "ffn_code_end:\n\t"

#define FFN_CLOB "v0", "v1", "v2", "v3", "v4", "v5", "v6", "v7", "v8", "v9", "v10", "v11", "v12", "v13", "v14", "v15", "v16", "v17", "v18", "v19", "v20", "v21", "v22", "v23", "v24", "v25", "v26", "v27", "v28", "v29", "v30", "v31", "v32", "v33", "v34", "v35", "v36", "v37", "v38", "v39", "v40", "v41", "v42", "v43", "v44", "v45", "v46", "v47", "v48", "v49", "v50", "v51", "v52", "v53", "v54", "v55", "v56", "v57", "v58", "v59", "v60", "v61", "v62", "v63", "v64", "v65", "v66", "v67", "v68", "v69", "v70", "v71", "v72", "v73", "v74", "v75", "v76", "v77", "v78", "v79", "v80", "v81", "v82", "v83", "v84", "v85", "v86", "v87", "v88", "v89", "v90", "v91", "v92", "v93", "v94", "v95", "v96", "v97", "v98", "v99", "v100", "v101", "v102", "v103", "v104", "v105", "v106", "v107", "v108", "v109", "v110", "v111", "v112", "v113", "v114", "v115", "v116", "v117", "v118", "v119", "v120", "v121", "v122", "v123", "v124", "v125", "v126", "v127", "v128", "v129", "v130", "v131", "v132", "v133", "v134", "v135", "v136", "v137", "v138", "v139", "v140", "v141", "v142", "v143", "v144", "v145", "v146", "v147", "v148", "v149", "v150", "v151", "v152", "v153", "v154", "v155", "v156", "v157", "v158", "v159", "v160", "v161", "v162", "v163", "v164", "v165", "v166", "v167", "v168", "v169", "v170", "v171", "v172", "v173", "v174", "v175", "v176", "v177", "v178", "v179", "v180", "v181", "v182", "v183", "v184", "v185", "v186", "v187", "v188", "v189", "v190", "v191", "v192", "v193", "v194", "v195", "v196", "v197", "v198", "v199", "v200", "v201", "v202", "v203", "v204", "v205", "v206", "v207", "v208", "v209", "v210", "v211", "v212", "v213", "v214", "v215", "v216", "v217", "v218", "v219", "v220", "v221", "v222", "v223", "v224", "v225", "v226", "v227", "v228", "v229", "v230", "v231", "v232", "v233", "v234", "v235", "v236", "v237", "v238", "v239", "v240", "v241", "v242", "v243", "v244", "v245", "v246", "v247", "v248", "v249", "v250", "v251", "v252", "v253", "v254", "v255", "a0", "a1", "a2", "a3", "a4", "a5", "a6", "a7", "a8", "a9", "a10", "a11", "a12", "a13", "a14", "a15", "a16", "a17", "a18", "a19", "a20", "a21", "a22", "a23", "a24", "a25", "a26", "a27", "a28", "a29", "a30", "a31", "a32", "a33", "a34", "a35", "a36", "a37", "a38", "a39", "a40", "a41", "a42", "a43", "a44", "a45", "a46", "a47", "a48", "a49", "a50", "a51", "a52", "a53", "a54", "a55", "a56", "a57", "a58", "a59", "a60", "a61", "a62", "a63", "a64", "a65", "a66", "a67", "a68", "a69", "a70", "a71", "a72", "a73", "a74", "a75", "a76", "a77", "a78", "a79", "a80", "a81", "a82", "a83", "a84", "a85", "a86", "a87", "a88", "a89", "a90", "a91", "a92", "a93", "a94", "a95", "a96", "a97", "a98", "a99", "a100", "a101", "a102", "a103", "a104", "a105", "a106", "a107", "a108", "a109", "a110", "a111", "a112", "a113", "a114", "a115", "a116", "a117", "a118", "a119", "a120", "a121", "a122", "a123", "a124", "a125", "a126", "a127", "a128", "a129", "a130", "a131", "a132", "a133", "a134", "a135", "a136", "a137", "a138", "a139", "a140", "a141", "a142", "a143", "a144", "a145", "a146", "a147", "a148", "a149", "a150", "a151", "a152", "a153", "a154", "a155", "a156", "a157", "a158", "a159", "a160", "a161", "a162", "a163", "a164", "a165", "a166", "a167", "a168", "a169", "a170", "a171", "a172", "a173", "a174", "a175", "a176", "a177", "a178", "a179", "a180", "a181", "a182", "a183", "a184", "a185", "a186", "a187", "a188", "a189", "a190", "a191", "a192", "a193", "a194", "a195", "a196", "a197", "a198", "a199", "a200", "a201", "a202", "a203", "a204", "a205", "a206", "a207", "a208", "a209", "a210", "a211", "a212", "a213", "a214", "a215", "a216", "a217", "a218", "a219", "a220", "a221", "a222", "a223", "a224", "a225", "a226", "a227", "a228", "a229", "a230", "a231", "a232", "a233", "a234", "a235", "a236", "a237", "a238", "a239", "a240", "a241", "a242", "a243", "a244", "a245", "a246", "a247", "a248", "a249", "a250", "a251", "a252", "a253", "a254", "a255", "s16", "s17", "s18", "s19", "s20", "s21", "s22", "s23", "s24", "s25", "s26", "s27", "s28", "s29", "s30", "s31", "s33", "s34", "s35", "s36", "s37", "s38", "s39", "s40", "s41", "s42", "s43", "s44", "s45", "s46", "s47", "s48", "s49", "s50", "s51", "s52", "s53", "s54", "s55", "s56", "s57", "s58", "s59", "s60", "s61", "s62", "s63", "s64", "s65", "s66", "s67", "s68", "s69", "s70", "s71", "s72", "s73", "s74", "s75", "s76", "s77", "s78", "s79", "s80", "s81", "s82", "s83", "s84", "s85", "s86", "s87", "s88", "s89", "s90", "s91", "s92", "s93", "s94", "s95", "s96", "s97", "s98", "s99"
#define FFN_LDS 160768


typedef unsigned short u16;
typedef unsigned u32;
typedef unsigned long long u64;

struct SsmArgs { const u16 *x, *lar, *aim, *cr, *ci, *ldt, *dsk, *wg, *wu, *wd; char* ws; };
struct FfnArgs { char* ws; const u16 *wg, *wu, *wd; u16* out; };

__global__ __launch_bounds__(512, 2) void ssm_kernel(SsmArgs a) {
  const u64 kp = (u64)__builtin_amdgcn_kernarg_segment_ptr();
  const u32 wid = __builtin_amdgcn_readfirstlane(threadIdx.x >> 6);
  asm volatile(SSM_ASM :: "s"(kp), "s"(blockIdx.x), "s"(wid) : "memory", "scc", "vcc", SSM_CLOB);
}

__global__ __launch_bounds__(256, 1) void ffn_kernel(FfnArgs a) {
  const u32 b = blockIdx.x, xcd = b & 7, slot = b >> 3, ks = xcd >> 1, half = xcd & 1;
  const u32 g = slot < 28 ? slot >> 2 : slot - 28 + 4 * half, j9 = slot < 28 ? (slot & 3) + 5 * half : 4;
  if (g == 7) return;
  const u32 wid = __builtin_amdgcn_readfirstlane(threadIdx.x >> 6);
  const u32 n = g < 2 ? 19 : 18, base = g < 2 ? 19 * g : 18 * g + 2;
  const u32 c3 = n - 4;
  const u32 c0 = ks == 3 ? c3 : 5 * ks, cend = ks == 3 ? n : (ks == 2 ? c3 : 5 * ks + 5);
  const u32 s2 = c3 - 10, b1 = cend - c0, b2 = b1 + (ks == 1 ? s2 : (ks == 2 ? 4 : 5)), b3 = n - (ks == 0 ? 4 : (ks == 3 ? s2 : 5));
  const u64 ones = 0x5555555555555555ull;
  const u64 mask = (((ones << (2 * b1)) + (ones << (2 * b2)) + (ones << (2 * b3))) & ((1ull << (2 * n)) - 1)) << 4;
  const u32 packed = wid | ks << 2 | j9 << 4 | base << 8 | n << 16 | c0 << 24;
  const u32 stat_bytes = j9 < b1 ? 18432u : 0u;
  const u64 kp = (u64)__builtin_amdgcn_kernarg_segment_ptr();
  asm volatile(FFN_ASM :: "s"(kp), "s"(packed), "s"(mask), "s"(stat_bytes) : "memory", "scc", "vcc", FFN_CLOB);
}

extern "C" void kernel_launch(void* const* d_in, const int* in_sizes, int n_in, void* d_out, int out_size, void* d_ws, size_t ws_size, hipStream_t stream) {
  char* ws = (char*)d_ws;
  SsmArgs a = {(const u16*)d_in[0], (const u16*)d_in[1], (const u16*)d_in[2], (const u16*)d_in[3], (const u16*)d_in[4], (const u16*)d_in[5], (const u16*)d_in[6],
               (const u16*)d_in[8], (const u16*)d_in[9], (const u16*)d_in[10], ws};
  FfnArgs f = {ws, (const u16*)d_in[8], (const u16*)d_in[9], (const u16*)d_in[10], (u16*)d_out};
  (void)hipFuncSetAttribute((const void*)ssm_kernel, hipFuncAttributeMaxDynamicSharedMemorySize, SSM_LDS);
  (void)hipFuncSetAttribute((const void*)ffn_kernel, hipFuncAttributeMaxDynamicSharedMemorySize, FFN_LDS);
  ssm_kernel<<<256, 512, SSM_LDS, stream>>>(a);
  ffn_kernel<<<256, 256, FFN_LDS, stream>>>(f);
}
```
